```python
import torch

def _softmax(x, axis=-1):
    return torch.softmax(x, dim=axis)
def _gelu(x):
    return torch.nn.functional.gelu(x, approximate="tanh")
def _silu(x):
    return torch.nn.functional.silu(x)
def _sigmoid(x):
    return torch.sigmoid(x)
def _layernorm(x, axis=-1, eps=1e-5):
    m = x.mean(dim=axis, keepdim=True)
    v = x.var(dim=axis, keepdim=True, unbiased=False)
    return (x - m) / torch.sqrt(v + eps)
def _scatter_add(tgt, idx, src, axis=0):
    return tgt.clone().index_add_(axis, idx, src)
def _segment_sum(src, seg, n):
    out = torch.zeros((n,) + src.shape[1:], dtype=src.dtype)
    return out.index_add_(0, seg, src)
def _pad(x, pw):
    flat = tuple(v for pair in reversed(pw) for v in pair)
    return torch.nn.functional.pad(x, flat)

kernel_name = 'parallel_sum__spectral_fnet__dense_activation_relu__prompt'

def setup_inputs():

    _w = (lambda a: (lambda t: t.to(torch.bfloat16) if t.is_floating_point() else t)(torch.as_tensor(a)))
    def _A_setup():
        B, S, D = 1, 2048, 576
        ns = torch.arange(S)[:, None] * torch.arange(S)[None, :]
        nd = torch.arange(D)[:, None] * torch.arange(D)[None, :]
        Ws_re = torch.cos(-2.0 * torch.pi * ns / S).to(float)
        Ws_im = torch.sin(-2.0 * torch.pi * ns / S).to(float)
        Wd_re = torch.cos(-2.0 * torch.pi * nd / D).to(float)
        Wd_im = torch.sin(-2.0 * torch.pi * nd / D).to(float)
        return {
            "x": _w((lambda *s: torch.randn(*s, dtype=torch.bfloat16) * 0.05)(B, S, D)),
            "dft_seq_re": _w(Ws_re),
            "dft_seq_im": _w(Ws_im),
            "dft_hid_re": _w(Wd_re),
            "dft_hid_im": _w(Wd_im),
        }
    _A_in = _A_setup()

    def _B_setup():
        B, S, D, Dff = 1, 2048, 576, 4608
        return {
            "x": _w((lambda *s: torch.randn(*s, dtype=torch.bfloat16) * 0.05)(B, S, D)),
            "w1": _w((lambda *s: torch.randn(*s, dtype=torch.bfloat16) * 0.4)(D, Dff)),
            "w2": _w((lambda *s: torch.randn(*s, dtype=torch.bfloat16) * 0.4)(Dff, D)),
        }
    _B_in = _B_setup()

    return {
        "A_x": _A_in["x"],
        "A_dft_seq_re": _A_in["dft_seq_re"],
        "A_dft_seq_im": _A_in["dft_seq_im"],
        "A_dft_hid_re": _A_in["dft_hid_re"],
        "A_dft_hid_im": _A_in["dft_hid_im"],
        "B_x": _B_in["x"],
        "B_w1": _B_in["w1"],
        "B_w2": _B_in["w2"],
    }

def reference(A_x, A_dft_seq_re, A_dft_seq_im, A_dft_hid_re, A_dft_hid_im, B_x, B_w1, B_w2):
    def _A_ref(x, dft_seq_re, dft_seq_im, dft_hid_re, dft_hid_im):
        a = torch.einsum("bsd,ed->bse", x, dft_hid_re)
        b = torch.einsum("bsd,ed->bse", x, dft_hid_im)
        y = torch.einsum("ts,bse->bte", dft_seq_re, a) - torch.einsum("ts,bse->bte", dft_seq_im, b)
        return y

    def _B_ref(x, w1, w2):
        h = torch.einsum("bsd,df->bsf", x, w1)
        zero = torch.zeros_like(h)
        h = torch.maximum(h, zero)
        return torch.einsum("bsf,fd->bsd", h, w2)

    x = A_x
    xn = _layernorm(x, axis=-1)
    return x + _A_ref(xn, A_dft_seq_re, A_dft_seq_im, A_dft_hid_re, A_dft_hid_im) + _B_ref(xn, B_w1, B_w2)
```

```cpp
#include <hip/hip_runtime.h>
#include <hip/hip_bf16.h>
#include <stdint.h>

typedef __attribute__((ext_vector_type(8))) short bf16x8;
typedef __attribute__((ext_vector_type(16))) float f32x16;
typedef unsigned short u16;

constexpr int S = 2048, D = 576, F = 4608;

__device__ __forceinline__ float bf2f(u16 v) { return __uint_as_float(((unsigned)v) << 16); }
__device__ __forceinline__ u16 f2bf(float f) {
    __hip_bfloat16 b = __float2bfloat16(f);
    return *reinterpret_cast<u16*>(&b);
}
__device__ __forceinline__ float rbf(float f) { return bf2f(f2bf(f)); }

__global__ __launch_bounds__(256) void ln_kernel(const u16* __restrict__ x, u16* __restrict__ xn) {
    int wave = threadIdx.x >> 6, lane = threadIdx.x & 63;
    int row = blockIdx.x * 4 + wave;
    const u16* xr = x + (size_t)row * D;
    float v[9];
    float s = 0.f;
#pragma unroll
    for (int i = 0; i < 9; ++i) { v[i] = bf2f(xr[lane + 64 * i]); s += v[i]; }
#pragma unroll
    for (int o = 32; o > 0; o >>= 1) s += __shfl_xor(s, o);
    float mean = s * (1.0f / D);
    float q = 0.f;
#pragma unroll
    for (int i = 0; i < 9; ++i) { float d = v[i] - mean; q += d * d; }
#pragma unroll
    for (int o = 32; o > 0; o >>= 1) q += __shfl_xor(q, o);
    float var = rbf(q * (1.0f / D));
    float mb = rbf(mean);
    float den = rbf(sqrtf(rbf(var + 1e-5f)));
#pragma unroll
    for (int i = 0; i < 9; ++i) {
        float d = rbf(v[i] - mb);
        xn[(size_t)row * D + lane + 64 * i] = f2bf(d / den);
    }
}

template <bool BT>
__device__ __forceinline__ void gemm_acc(f32x16& acc, const u16* __restrict__ A, int lda, const u16* __restrict__ B, int ldb,
                                         int K, int m0, int n0, u16 (*As)[40], u16 (*Bs)[40]) {
    int tid = threadIdx.x, wave = tid >> 6, lane = tid & 63;
    int wm = wave >> 1, wn = wave & 1;
    for (int k0 = 0; k0 < K; k0 += 32) {
        {
            int r = tid >> 2, c = (tid & 3) * 8;
            *(bf16x8*)&As[r][c] = *(const bf16x8*)&A[(size_t)(m0 + r) * lda + k0 + c];
        }
        if (BT) {
            int r = tid >> 2, c = (tid & 3) * 8;
            *(bf16x8*)&Bs[r][c] = *(const bf16x8*)&B[(size_t)(n0 + r) * ldb + k0 + c];
        } else {
            int k = tid >> 3, c = (tid & 7) * 8;
            bf16x8 v = *(const bf16x8*)&B[(size_t)(k0 + k) * ldb + n0 + c];
#pragma unroll
            for (int j = 0; j < 8; ++j) Bs[c + j][k] = (u16)v[j];
        }
        __syncthreads();
#pragma unroll
        for (int ks = 0; ks < 2; ++ks) {
            bf16x8 a = *(const bf16x8*)&As[wm * 32 + (lane & 31)][ks * 16 + (lane >> 5) * 8];
            bf16x8 b = *(const bf16x8*)&Bs[wn * 32 + (lane & 31)][ks * 16 + (lane >> 5) * 8];
            acc = __builtin_amdgcn_mfma_f32_32x32x16_bf16(a, b, acc, 0, 0, 0);
        }
        __syncthreads();
    }
}

__global__ __launch_bounds__(256) void stage1(const u16* __restrict__ xn, const u16* __restrict__ wr, const u16* __restrict__ wi,
                                              const u16* __restrict__ w1, u16* __restrict__ a, u16* __restrict__ b, u16* __restrict__ h) {
    __shared__ __attribute__((aligned(16))) u16 As[64][40];
    __shared__ __attribute__((aligned(16))) u16 Bs[64][40];
    int nt = blockIdx.x, m0 = blockIdx.y * 64;
    f32x16 acc = {};
    int tid = threadIdx.x, wave = tid >> 6, lane = tid & 63;
    int wm = wave >> 1, wn = wave & 1;
    if (nt < 18) {
        const u16* W = nt < 9 ? wr : wi;
        int n0 = (nt % 9) * 64;
        gemm_acc<true>(acc, xn, D, W, D, D, m0, n0, As, Bs);
        u16* o = nt < 9 ? a : b;
        float sgn = nt < 9 ? 1.f : -1.f;
#pragma unroll
        for (int r = 0; r < 16; ++r) {
            int row = m0 + wm * 32 + (r & 3) + 8 * (r >> 2) + 4 * (lane >> 5);
            int col = n0 + wn * 32 + (lane & 31);
            o[(size_t)row * D + col] = f2bf(sgn * acc[r]);
        }
    } else {
        int n0 = (nt - 18) * 64;
        gemm_acc<false>(acc, xn, D, w1, F, D, m0, n0, As, Bs);
#pragma unroll
        for (int r = 0; r < 16; ++r) {
            int row = m0 + wm * 32 + (r & 3) + 8 * (r >> 2) + 4 * (lane >> 5);
            int col = n0 + wn * 32 + (lane & 31);
            h[(size_t)row * F + col] = f2bf(fmaxf(acc[r], 0.f));
        }
    }
}

__global__ __launch_bounds__(256) void stage2(const u16* __restrict__ x, const u16* __restrict__ cr, const u16* __restrict__ ci,
                                              const u16* __restrict__ a, const u16* __restrict__ b, const u16* __restrict__ h,
                                              const u16* __restrict__ w2, u16* __restrict__ out) {
    __shared__ __attribute__((aligned(16))) u16 As[64][40];
    __shared__ __attribute__((aligned(16))) u16 Bs[64][40];
    int n0 = blockIdx.x * 64, m0 = blockIdx.y * 64;
    f32x16 acc = {};
    int tid = threadIdx.x, wave = tid >> 6, lane = tid & 63;
    int wm = wave >> 1, wn = wave & 1;
    gemm_acc<false>(acc, cr, S, a, D, S, m0, n0, As, Bs);
    gemm_acc<false>(acc, ci, S, b, D, S, m0, n0, As, Bs);
    gemm_acc<false>(acc, h, F, w2, D, F, m0, n0, As, Bs);
#pragma unroll
    for (int r = 0; r < 16; ++r) {
        int row = m0 + wm * 32 + (r & 3) + 8 * (r >> 2) + 4 * (lane >> 5);
        int col = n0 + wn * 32 + (lane & 31);
        size_t o = (size_t)row * D + col;
        out[o] = f2bf(bf2f(x[o]) + acc[r]);
    }
}

extern "C" void kernel_launch(void* const* d_in, const int* in_sizes, int n_in,
                              void* d_out, int out_size, void* d_ws, size_t ws_size,
                              hipStream_t stream) {
    const u16* x = (const u16*)d_in[0];
    const u16* cr = (const u16*)d_in[1];
    const u16* ci = (const u16*)d_in[2];
    const u16* wr = (const u16*)d_in[3];
    const u16* wi = (const u16*)d_in[4];
    const u16* w1 = (const u16*)d_in[6];
    const u16* w2 = (const u16*)d_in[7];
    u16* out = (u16*)d_out;
    u16* xn = (u16*)d_ws;
    u16* a = xn + (size_t)S * D;
    u16* b = a + (size_t)S * D;
    u16* h = b + (size_t)S * D;
    ln_kernel<<<S / 4, 256, 0, stream>>>(x, xn);
    stage1<<<dim3(90, S / 64), 256, 0, stream>>>(xn, wr, wi, w1, a, b, h);
    stage2<<<dim3(9, S / 64), 256, 0, stream>>>(x, cr, ci, a, b, h, w2, out);
}
```

```cpp
#include <hip/hip_runtime.h>
#include <stdint.h>

typedef __attribute__((ext_vector_type(8))) __bf16 bf16x8;
typedef __attribute__((ext_vector_type(2))) __bf16 bf16x2;
typedef __attribute__((ext_vector_type(4))) float f32x4;
typedef __attribute__((ext_vector_type(2))) float f32x2;
typedef __attribute__((ext_vector_type(4))) unsigned u32x4;
typedef __attribute__((ext_vector_type(2))) unsigned u32x2;
typedef __attribute__((address_space(3))) u32x4* lp16;
typedef __attribute__((address_space(1))) unsigned gu32;
typedef __amdgpu_buffer_rsrc_t rsrc_t;

#define DI __device__ __forceinline__
#define SCHED __builtin_amdgcn_sched_barrier(0)
#define BST do {} while (0)
#define BSTEND do {} while (0)
#define BAR do { BST; asm volatile("s_barrier" ::: "memory"); } while (0)
#define WBAR(w) do { asm volatile("s_waitcnt " w ::: "memory"); BST; asm volatile("s_barrier" ::: "memory"); } while (0)

namespace fk {

constexpr int S_ = 2048, D_ = 576, F_ = 4608;
constexpr int SLOT = 24576, NSLOT = 5, RING = SLOT * NSLOT, SCR = RING + 32768, LDS_BYTES = SCR + 8192;
constexpr unsigned CNT_BYTES = 4096;
constexpr unsigned AB_OFF = 65536, AB_BYTES = 16u * 9 * 8 * 4096;
constexpr unsigned PART_OFF = 8u << 20, PART_BYTES = 2304u * 16384;
constexpr int P = 4;
#define STAMP(i) do {} while (0)

DI rsrc_t mk(const void* p, unsigned bytes) { return __builtin_amdgcn_make_buffer_rsrc((void*)p, (short)0, bytes, 0x00020000); }
DI u32x4 gld4(rsrc_t r, unsigned v, unsigned s) { return __builtin_amdgcn_raw_buffer_load_b128(r, v, s, 0); }
DI u32x2 gld2(rsrc_t r, unsigned v, unsigned s) { return __builtin_amdgcn_raw_buffer_load_b64(r, v, s, 0); }
DI u32x4 lds_ld(unsigned a) { return *(lp16)(size_t)a; }
DI void lds_st(unsigned a, u32x4 v) { *(lp16)(size_t)a = v; }
DI void dma16(rsrc_t srd, unsigned voff, unsigned soff, unsigned ldsaddr) {
  asm volatile("s_mov_b32 m0, %0\n\ts_nop 0\n\tbuffer_load_dwordx4 %1, %2, %3 offen lds" ::"s"(ldsaddr), "v"(voff), "s"(srd), "s"(soff) : "memory");
}
DI f32x4 mfma(u32x4 a, u32x4 b, f32x4 c) {
  return __builtin_amdgcn_mfma_f32_16x16x32_bf16(__builtin_bit_cast(bf16x8, a), __builtin_bit_cast(bf16x8, b), c, 0, 0, 0);
}
DI unsigned pk(float a, float b) {
  f32x2 v = {a, b};
  return __builtin_bit_cast(unsigned, __builtin_convertvector(v, bf16x2));
}
DI float rbf(float f) { return __uint_as_float(pk(f, 0.f) << 16); }
DI unsigned relu2(unsigned v) {
  typedef __attribute__((ext_vector_type(2))) short s16x2;
  s16x2 z = {0, 0};
  return __builtin_bit_cast(unsigned, __builtin_elementwise_max(__builtin_bit_cast(s16x2, v), z));
}
DI int fresh_lane() {
  int l;
  asm volatile("v_mbcnt_lo_u32_b32 %0, -1, 0\n\tv_mbcnt_hi_u32_b32 %0, -1, %0" : "=v"(l));
  return l;
}
constexpr int cls(int f) { return (f ^ (f >> 1)) & 1; }
DI unsigned nextslot(unsigned s, unsigned inc) { s += inc; return s >= (unsigned)RING ? s - RING : s; }

struct Pipe {
  u32x4 A[P];
  unsigned l0, l1;
  unsigned c0, c1, n0, n1;
  unsigned sn;
};
template <int TY> DI unsigned faddr(int f, unsigned b0, unsigned b1) {
  if (TY == 0) return (cls(f) ? b1 : b0) + f * 1024;
  if (TY == 1) return (((f >> 2) & 1) ? b1 : b0) + ((f >> 3) * 4 + (f & 3)) * 2048;
  return ((f & 1) ? b1 : b0) + (f >> 1) * 2048;
}
template <int NF, int TY, int TYN, class OP, class EX>
DI void slab(Pipe& pp, unsigned a0, unsigned a1, OP op, EX ex) {
  u32x4 buf[NF + P];
#pragma unroll
  for (int i = 0; i < P; ++i) buf[i] = pp.A[i];
#pragma unroll
  for (int f = 0; f < NF; ++f) {
    op(f, buf[f], 0);
    SCHED;
    const int fn = f + P;
    if (fn < NF) buf[fn] = lds_ld(faddr<TY>(fn, pp.c0, pp.c1));
    else buf[fn] = lds_ld(faddr<TYN>(fn - NF, pp.n0, pp.n1));
    SCHED;
    op(f, buf[f], 1);
    SCHED;
    ex(f);
    SCHED;
  }
#pragma unroll
  for (int i = 0; i < P; ++i) pp.A[i] = buf[NF + i];
  pp.c0 = pp.n0; pp.c1 = pp.n1; pp.sn = nextslot(pp.sn, SLOT); pp.n0 = a0 + pp.sn; pp.n1 = a1 + pp.sn;
}

DI void tp8(const u32x4 (&in)[8], u32x4 (&o)[8]) {
#pragma unroll
  for (int i = 0; i < 8; ++i)
#pragma unroll
    for (int rp = 0; rp < 4; ++rp) o[i][rp] = __builtin_amdgcn_perm(in[2 * rp + 1][i >> 1], in[2 * rp][i >> 1], (i & 1) ? 0x07060302u : 0x05040100u);
}
DI void tp4(const u32x2 (&in)[8], u32x4 (&o)[8]) {
#pragma unroll
  for (int i = 0; i < 4; ++i)
#pragma unroll
    for (int rp = 0; rp < 4; ++rp) o[i][rp] = __builtin_amdgcn_perm(in[2 * rp + 1][i >> 1], in[2 * rp][i >> 1], (i & 1) ? 0x07060302u : 0x05040100u);
}
DI void wr8(const u32x4 (&o)[8], const unsigned (&wad)[4], unsigned sb, unsigned off) {
  unsigned t[4];
#pragma unroll
  for (int q = 0; q < 4; ++q) t[q] = wad[q] + sb;
#pragma unroll
  for (int i = 0; i < 8; ++i) lds_st(t[(i & 3) ^ (i >> 2)] + off + (i >> 2) * 1024, o[i]);
}
DI void wr4(const u32x4 (&o)[8], const unsigned (&wad)[4], unsigned sb, unsigned off) {
#pragma unroll
  for (int i = 0; i < 4; ++i) lds_st(wad[i] + sb + off, o[i]);
}
#define LBAR WBAR("lgkmcnt(0)")

__global__ __launch_bounds__(512, 2) void main_kernel(const void* __restrict__ x, const void* __restrict__ cr, const void* __restrict__ ci,
                                                      const void* __restrict__ wr, const void* __restrict__ wi, const void* __restrict__ w1,
                                                      const void* __restrict__ w2, void* __restrict__ ws) {
  __shared__ __attribute__((aligned(1024))) char smem[LDS_BYTES];
  const int wid = __builtin_amdgcn_readfirstlane(threadIdx.x >> 6);
  const unsigned ldsb = (unsigned)(size_t)smem;
  const int xcd = blockIdx.x & 7, idx = blockIdx.x >> 3;
  const int tb = (xcd & 1) * 8 + (idx & 7), j = (xcd >> 1) * 4 + (idx >> 3);
  const int jodd = j & 1;
  const int fb0 = j * 288 + (jodd ? 32 : 0), fbl = j * 288 + (jodd ? 0 : 256);
  gu32* cnt = (gu32*)ws;
#define LANEVARS                                                                                   \
  const int lane = fresh_lane();                                                                   \
  const int c = lane & 15, g = lane >> 4, l7 = lane & 7, l3 = lane >> 3;                           \
  const int rowp = 8 * (l3 >> 2) + (l3 & 3);   \
  (void)c; (void)g; (void)l7; (void)rowp;

  if (wid >= 4) {
    const int lw = wid - 4;
    LANEVARS
    STAMP(0);
    {
      const unsigned v0 = rowp * 1152 + ((l7 ^ g) * 16), v1 = rowp * 1152 + ((l7 ^ g ^ 4) * 16);
      if (lw < 3) {
        rsrc_t rw = mk(j < 9 ? wr : wi, D_ * D_ * 2);
        const unsigned sb = (unsigned)((j < 9 ? j : j - 9) * 64 * 1152 + lw * 6 * 64);
#pragma unroll
        for (int f = 0; f < 24; ++f) {
          const int hb = f & 1, tl = (f >> 1) & 3, kp = f >> 3;
          dma16(rw, hb ? v1 : v0, sb + (32 * (tl >> 1) + 4 * (tl & 1) + 16 * hb) * 1152 + kp * 128, ldsb + lw * SLOT + f * 1024);
        }
      } else {
        rsrc_t rw = mk(wi, D_ * D_ * 2);
        const int t = 28 + (j >> 1);
        const unsigned sb = (unsigned)((32 * (t >> 1) + 4 * (t & 1)) * 1152);
#pragma unroll
        for (int f = 0; f < 18; ++f) dma16(rw, (f & 1) ? v1 : v0, sb + (f & 1) * 16 * 1152 + (f >> 1) * 128, ldsb + 3 * SLOT + f * 1024);
      }
      asm volatile("s_waitcnt vmcnt(0)" ::: "memory");
    }
    STAMP(1);
    if (lw < 3) {
      rsrc_t r1 = mk(w1, D_ * F_ * 2), r2 = mk(w2, D_ * F_ * 2);
      const int cg8 = lane & 7, rg = lane >> 3;
      unsigned wa[4], wb[4], wc[4];
      {
        const int u = cg8 >> 2, gp = cg8 & 3, x0 = u | ((gp >> 1) << 1);
        const unsigned base = ldsb + ((rg >> 2) * 4 + 2 * u) * 1024 + ((rg & 3) * 16 + 4 * gp) * 16;
#pragma unroll
        for (int q = 0; q < 4; ++q) wa[q] = base + ((q ^ x0) * 16);
      }
      {
        const int h = c & 1, gp = (c >> 1) & 3, u = c >> 3, x0 = (h ^ u) | ((gp >> 1) << 1);
        const unsigned base = ldsb + (8 + 2 * u + h) * 1024 + (g * 16 + 4 * gp) * 16;
#pragma unroll
        for (int q = 0; q < 4; ++q) wb[q] = base + ((q ^ x0) * 16);
      }
      {
        const int h = cg8 & 1, gp = cg8 >> 1, ksl = rg >> 2, x0 = (h ^ ksl) | ((gp >> 1) << 1);
        const unsigned base = ldsb + (ksl * 2 + h) * 1024 + ((rg & 3) * 16 + 4 * gp) * 16;
#pragma unroll
        for (int q = 0; q < 4; ++q) wc[q] = base + ((q ^ x0) * 16);
      }
      const unsigned v1a = (unsigned)((192 * lw + 8 * rg) * (F_ * 2) + cg8 * 16 + fb0 * 2);
      const unsigned v1c = (unsigned)((192 * lw + 8 * rg) * (F_ * 2) + cg8 * 8 + fbl * 2);
      auto fk = [&](int kap) { return kap < 8 ? fb0 + 32 * kap : fbl; };
      const unsigned v2a = (unsigned)((fk(3 * lw + (rg >> 2)) + 8 * (rg & 3)) * (D_ * 2) + cg8 * 16);
      const unsigned v2b = (unsigned)((fk(3 * lw + 2) + 8 * g) * (D_ * 2) + c * 8);
      u32x4 X[6][8];
      u32x2 Y[3][8];
      u32x4 PA[2][8];
      u32x2 PB[2][8];
      auto ld1 = [&](u32x4(&d)[8], int rho) {
#pragma unroll
        for (int r = 0; r < 8; ++r) d[r] = gld4(r1, v1a, (unsigned)(((rho % 3) * 64 + r) * (F_ * 2) + (rho / 3) * 128));
      };
      auto ld1c = [&](u32x2(&d)[8], int rd) {
#pragma unroll
        for (int r = 0; r < 8; ++r) d[r] = gld2(r1, v1c, (unsigned)((rd * 64 + r) * (F_ * 2)));
      };
      auto ld2a = [&](u32x4(&d)[8], int eg) {
#pragma unroll
        for (int r = 0; r < 8; ++r) d[r] = gld4(r2, v2a, (unsigned)(r * (D_ * 2) + eg * 128));
      };
      auto ld2b = [&](u32x2(&d)[8], int eg) {
#pragma unroll
        for (int r = 0; r < 8; ++r) d[r] = gld2(r2, v2b, (unsigned)(r * (D_ * 2) + eg * 128));
      };
#pragma unroll
      for (int r = 0; r < 6; ++r) ld1(X[r], r);
      SCHED;
      u32x4 O[8];
      tp8(X[0], O);
      SCHED;
      for (int i = 0; i <= lw; ++i) BAR;
      unsigned sb = (unsigned)(((4 + lw) % NSLOT) * SLOT);
#pragma unroll
      for (int rho = 0; rho < 12; ++rho) {
        wr8(O, wa, sb, (rho % 3) * 8192);
        SCHED;
        if (rho < 6) ld1(X[rho], rho + 6);
        else if (rho < 9) ld1c(Y[rho - 6], rho - 6);
        else if (rho == 9) ld2a(PA[0], 0);
        else if (rho == 10) ld2b(PB[0], 0);
        SCHED;
        if (rho < 11) tp8(X[(rho + 1) % 6], O);
        else tp4(Y[0], O);
        SCHED;
        LBAR;
        if (rho % 3 == 2) sb = nextslot(sb, 3 * SLOT);
      }
#pragma unroll
      for (int rd = 0; rd < 3; ++rd) {
        wr4(O, wc, sb, rd * 4096);
        SCHED;
        if (rd == 0) ld2a(PA[1], 1);
        else if (rd == 1) ld2b(PB[1], 1);
        SCHED;
        if (rd < 2) tp4(Y[rd + 1], O);
        else tp8(PA[0], O);
        SCHED;
        LBAR;
      }
      sb = nextslot(sb, 3 * SLOT);
      auto job2 = [&](u32x4(&a)[8], u32x2(&b)[8], u32x4(&an)[8], int egn) {
        wr8(O, wa, sb, 0);
        SCHED;
        ld2a(a, egn);
        SCHED;
        tp4(b, O);
        SCHED;
        LBAR;
        wr4(O, wb, sb, 0);
        SCHED;
        ld2b(b, egn);
        SCHED;
        LBAR;
        tp8(an, O);
        SCHED;
        BAR;
        sb = nextslot(sb, 3 * SLOT);
      };
      for (int eg = 0; eg < 8; eg += 2) {
        job2(PA[0], PB[0], PA[1], eg + 2);
        job2(PA[1], PB[1], PA[0], eg + 3 < 9 ? eg + 3 : 8);
      }
      job2(PA[0], PB[0], PA[1], 8);
      for (int i = 0; i < 3 - lw; ++i) BAR;
      STAMP(2);
    } else {
      rsrc_t rab = mk((char*)ws + AB_OFF, AB_BYTES);
      for (int b = 0; b <= 9; ++b) BAR;
      unsigned v = __hip_atomic_load(cnt + j * 32, __ATOMIC_RELAXED, __HIP_MEMORY_SCOPE_AGENT);
      for (int b = 10; b <= 13; ++b) BAR;
      while (v < 64u) {
        __builtin_amdgcn_s_sleep(2);
        v = __hip_atomic_load(cnt + j * 32, __ATOMIC_RELAXED, __HIP_MEMORY_SCOPE_AGENT);
      }
      __builtin_amdgcn_fence(__ATOMIC_ACQUIRE, "agent");
      STAMP(2);
      BAR;
      BAR;
      unsigned sb = (unsigned)((19 % NSLOT) * SLOT) + 12288;
      unsigned so = (unsigned)(j * 9 * 32768);
      const unsigned vo = lane * 16;
      auto issue = [&](int n) {
        for (int i = 0; i < n; ++i) dma16(rab, vo, so + i * 1024, ldsb + sb + i * 1024);
        so += n * 1024;
        sb = nextslot(sb, SLOT);
      };
      issue(12); BAR;
      issue(12); BAR;
      issue(8);
      for (int eg = 0; eg < 8; ++eg) {
        WBAR("vmcnt(20)");
        issue(12);
        WBAR("vmcnt(20)");
        issue(12);
        WBAR("vmcnt(24)");
        issue(8);
      }
      WBAR("vmcnt(20)");
      WBAR("vmcnt(8)");
      WBAR("vmcnt(0)");
      BAR;
      STAMP(3);
    }
    BSTEND;
    return;
  }

  const int w = wid;
  LANEVARS
  STAMP(0);
  const int sw = (c >> 3) << 1;
  const unsigned off0 = (g * 16 + (c ^ sw)) * 16, off1 = off0 ^ 16;
#define SHUFVARS                                                                                                   \
  const unsigned m0 = (c >> 3) * 1024 + (c & 7) * 128 + ((g ^ (c >> 1)) * 16), m1 = m0 ^ 64;                       \
  const unsigned scr = ldsb + SCR + w * 2048;                                                                      \
  const unsigned sr0 = scr + m0, sr1 = scr + m1;                                                                   \
  const unsigned sw0 = scr + l3 * 128 + ((l7 ^ g) * 16), sw1 = scr + 1024 + l3 * 128 + ((l7 ^ g ^ 4) * 16);       \
  auto shuf = [&](u32x4& a, u32x4& b) {          \
    lds_st(sw0, a); lds_st(sw1, b);                                                                                \
    a = lds_ld(sr0); b = lds_ld(sr1);                                                                              \
  };                                                                                                               \
  const int tokp = tb * 128 + w * 32 + rowp;
  SHUFVARS
  u32x4 xf[18][2];
  {
    rsrc_t rx = mk(x, S_ * D_ * 2);
    const unsigned vx = (unsigned)(tokp * (D_ * 2) + l7 * 16);
#pragma unroll
    for (int kp = 0; kp < 9; ++kp)
#pragma unroll
      for (int T = 0; T < 2; ++T)
#pragma unroll
        for (int hb = 0; hb < 2; ++hb) { xf[2 * kp + hb][T] = gld4(rx, vx + kp * 128, (16 * hb + 4 * T) * D_ * 2); SCHED; }
    float s1[2][2] = {{0.f, 0.f}, {0.f, 0.f}}, s2[2][2] = {{0.f, 0.f}, {0.f, 0.f}};
    const bf16x2 ones = __builtin_bit_cast(bf16x2, 0x3f803f80u);
#pragma unroll
    for (int kp = 0; kp <= 9; ++kp) {
      if (kp < 9) {
#pragma unroll
        for (int T = 0; T < 2; ++T) shuf(xf[2 * kp][T], xf[2 * kp + 1][T]);
      }
      SCHED;
      if (kp > 0) {
#pragma unroll
        for (int ks = 2 * kp - 2; ks < 2 * kp; ++ks)
#pragma unroll
          for (int T = 0; T < 2; ++T)
#pragma unroll
            for (int q = 0; q < 4; ++q) {
              const unsigned tq = xf[ks][T][q];
              bf16x2 v = __builtin_bit_cast(bf16x2, tq);
              s1[T][q & 1] = __builtin_amdgcn_fdot2_f32_bf16(v, ones, s1[T][q & 1], false);
              s2[T][q & 1] = __builtin_amdgcn_fdot2_f32_bf16(v, v, s2[T][q & 1], false);
            }
      }
      SCHED;
    }
    STAMP(1);
    float rs[2], nb[2];
#pragma unroll
    for (int T = 0; T < 2; ++T) {
      float a = s1[T][0] + s1[T][1], b = s2[T][0] + s2[T][1];
      a += __shfl_xor(a, 16); b += __shfl_xor(b, 16);
      a += __shfl_xor(a, 32); b += __shfl_xor(b, 32);
      const float mean = a * (1.0f / D_);
      const float var = fmaxf(b * (1.0f / D_) - mean * mean, 0.f);
      const float den = rbf(__builtin_sqrtf(rbf(rbf(var) + 1e-5f)));
      rs[T] = 1.0f / den;
      nb[T] = -rbf(mean) * rs[T];
    }
#pragma unroll
    for (int ks = 0; ks < 18; ++ks)
#pragma unroll
      for (int T = 0; T < 2; ++T)
#pragma unroll
        for (int q = 0; q < 4; ++q) {
          const unsigned v = xf[ks][T][q];
          xf[ks][T][q] = pk(__builtin_fmaf(__uint_as_float(v << 16), rs[T], nb[T]), __builtin_fmaf(__uint_as_float(v & 0xffff0000u), rs[T], nb[T]));
        }
  }
  STAMP(2);
  Pipe pp;
  pp.l0 = ldsb + off0; pp.l1 = ldsb + off1;
  const unsigned r0 = ldsb + m0, r1 = ldsb + m1;
  pp.c0 = r0; pp.c1 = r1; pp.sn = SLOT; pp.n0 = r0 + SLOT; pp.n1 = r1 + SLOT;
  const f32x4 zero4 = {0.f, 0.f, 0.f, 0.f};
  auto noex = [](int) {};
  f32x4 acc[4][2];
  SCHED;
  BAR;
  STAMP(3);
#pragma unroll
  for (int i = 0; i < P; ++i) pp.A[i] = lds_ld(faddr<1>(i, pp.c0, pp.c1));
  SCHED;
  rsrc_t rab = mk((char*)ws + AB_OFF, AB_BYTES);
  slab<24, 1, 1>(pp, r0, r1, [&](int f, u32x4 a, int T) { const int ks = (f >> 2), tl = f & 3; acc[tl][T] = mfma(xf[ks][T], a, ks == 0 ? zero4 : acc[tl][T]); }, noex);
  BAR;
  slab<24, 1, 1>(pp, r0, r1, [&](int f, u32x4 a, int T) { const int ks = 6 + (f >> 2), tl = f & 3; acc[tl][T] = mfma(xf[ks][T], a, acc[tl][T]); }, noex);
  BAR;
  slab<24, 1, 2>(pp, pp.l0, pp.l1, [&](int f, u32x4 a, int T) { const int ks = 12 + (f >> 2), tl = f & 3; acc[tl][T] = mfma(xf[ks][T], a, acc[tl][T]); }, noex);
  BAR;
  {
    const int mat = j < 9 ? 0 : 1, eg = j < 9 ? j : j - 9;
    const unsigned sg = mat ? 0x80008000u : 0u;
    const unsigned so = (unsigned)((((tb * 9 + eg) * 8) + mat * 4 + w) * 4096);
    f32x4 hacc;
    auto ex = [&](int f) {
      if (f >= 1 && f < 5) {
        const int tl = f - 1;
        u32x4 o = {pk(acc[tl][0][0], acc[tl][0][1]) ^ sg, pk(acc[tl][0][2], acc[tl][0][3]) ^ sg, pk(acc[tl][1][0], acc[tl][1][1]) ^ sg, pk(acc[tl][1][2], acc[tl][1][3]) ^ sg};
        __builtin_amdgcn_raw_buffer_store_b128(o, rab, cls(tl) ? off1 : off0, so + tl * 1024, 16);
      }
    };
    if (jodd) slab<18, 2, 0>(pp, pp.l0, pp.l1, [&](int f, u32x4 a, int T) { if (T == 0) hacc = mfma(xf[f][1], a, f == 0 ? zero4 : hacc); }, ex);
    else slab<18, 2, 0>(pp, pp.l0, pp.l1, [&](int f, u32x4 a, int T) { if (T == 0) hacc = mfma(xf[f][0], a, f == 0 ? zero4 : hacc); }, ex);
    const int t = 28 + (j >> 1), tl = t & 3;
    const unsigned sh = (unsigned)((((tb * 9 + (t >> 2)) * 8) + 4 + w) * 4096 + tl * 1024 + jodd * 8);
    u32x2 o = {pk(-hacc[0], -hacc[1]), pk(-hacc[2], -hacc[3])};
    __builtin_amdgcn_raw_buffer_store_b64(o, rab, ((tl ^ (tl >> 1)) & 1) ? off1 : off0, sh, 16);
  }
  STAMP(4);
  u32x4 Hf[9][2], Cf[8][2];
  const unsigned park = ldsb + RING + w * 8192 + lane * 16;
  auto packh = [&](const f32x4& a, const f32x4& b) -> u32x4 {
    return (u32x4){relu2(pk(a[0], a[1])), relu2(pk(a[2], a[3])), relu2(pk(b[0], b[1])), relu2(pk(b[2], b[3]))};
  };
#pragma unroll 1
  for (int p = 0; p < 2; ++p) {
#pragma unroll
    for (int s = 0; s < 3; ++s) {
      BAR;
      slab<24, 0, 0>(pp, pp.l0, pp.l1, [&](int f, u32x4 a, int T) { const int ks = 6 * s + (f >> 2), tl = f & 3; acc[tl][T] = mfma(a, xf[ks][T], ks == 0 ? zero4 : acc[tl][T]); },
               [&](int f) { if (s == 2 && f >= 22) lds_st(park + p * 4096 + (f - 22) * 1024, packh(acc[0][f - 22], acc[1][f - 22])); });
      if (s == 0 && p == 0) {
        asm volatile("s_waitcnt vmcnt(0)" ::: "memory");
        if (lane == 0) __hip_atomic_fetch_add(cnt + tb * 32, 1u, __ATOMIC_RELAXED, __HIP_MEMORY_SCOPE_AGENT);
      }
    }
    lds_st(park + p * 4096 + 2048, packh(acc[2][0], acc[3][0]));
    lds_st(park + p * 4096 + 3072, packh(acc[2][1], acc[3][1]));
    STAMP(5 + p);
  }
#pragma unroll
  for (int p = 2; p < 4; ++p) {
#pragma unroll
    for (int s = 0; s < 3; ++s) {
      BAR;
      slab<24, 0, 0>(pp, pp.l0, pp.l1, [&](int f, u32x4 a, int T) { const int ks = 6 * s + (f >> 2), tl = f & 3; acc[tl][T] = mfma(a, xf[ks][T], ks == 0 ? zero4 : acc[tl][T]); },
               [&](int f) {
                 if (s == 2 && f >= 22) Hf[2 * p][f - 22] = packh(acc[0][f - 22], acc[1][f - 22]);
                 if (s == 0 && p == 3 && f < 2) Hf[5][f] = packh(acc[2][f], acc[3][f]);
               });
    }
    STAMP(5 + p);
  }
  {
    LANEVARS
    SHUFVARS
    rsrc_t rcr = mk(cr, S_ * S_ * 2), rci = mk(ci, S_ * S_ * 2);
    const unsigned vc = (unsigned)(tokp * (S_ * 2) + j * 256 + l7 * 16);
#pragma unroll
    for (int s = 0; s < 3; ++s) {
      BAR;
      slab<12, 0, 0>(pp, pp.l0, pp.l1, [&](int f, u32x4 a, int T) { const int ks = 6 * s + (f >> 1), tl = f & 1; acc[tl][T] = mfma(a, xf[ks][T], ks == 0 ? zero4 : acc[tl][T]); },
               [&](int f) {
                 if (s == 0 && f < 2) Hf[7][f] = packh(acc[2][f], acc[3][f]);
                 if (f & 1) {
                   const int ks = 6 * s + (f >> 1);
                   if (ks >= 2 && ks < 10) {
                     const int d = ks - 2;
#pragma unroll
                     for (int T = 0; T < 2; ++T) Cf[d][T] = gld4(d < 4 ? rcr : rci, vc + ((d >> 1) & 1) * 128, (16 * (d & 1) + 4 * T) * S_ * 2);
                   } else if (ks >= 10 && ks < 14) {
#pragma unroll
                     for (int T = 0; T < 2; ++T) Hf[ks - 10][T] = lds_ld(park + ((ks - 10) * 2 + T) * 1024);
                   } else if (ks >= 14) {
#pragma unroll
                     for (int T = 0; T < 2; ++T) shuf(Cf[2 * (ks - 14)][T], Cf[2 * (ks - 14) + 1][T]);
                   }
                 }
               });
    }
#pragma unroll
    for (int T = 0; T < 2; ++T) {
      Hf[8][T] = packh(acc[0][T], acc[1][T]);
    }
  }
  STAMP(9);
  rsrc_t rp = mk((char*)ws + PART_OFF, PART_BYTES);
  const unsigned vp = fresh_lane() * 16;
  const unsigned pbase = (unsigned)((tb * 4 + w) * 36 * 16384 + j * 1024);
  auto pst = [&](int eg, int u, int T) {
    const f32x4 &a = acc[2 * u][T], &b = acc[2 * u + 1][T];
    u32x4 o = {pk(a[0], a[1]), pk(a[2], a[3]), pk(b[0], b[1]), pk(b[2], b[3])};
    __builtin_amdgcn_raw_buffer_store_b128(o, rp, vp, pbase + (unsigned)((eg * 4 + u * 2 + T) * 16384), 0);
  };
  asm volatile("" : "=v"(acc[2][0]), "=v"(acc[2][1]), "=v"(acc[3][0]), "=v"(acc[3][1]));
#pragma unroll 1
  for (int eg = 0; eg < 9; ++eg) {
    const int egp = eg > 0 ? eg - 1 : 0;
    BAR;
    slab<24, 0, 0>(pp, pp.l0, pp.l1, [&](int f, u32x4 a, int T) { const int kk = f >> 2, tl = f & 3; acc[tl][T] = mfma(a, kk < 3 ? Hf[kk][T] : Cf[kk - 3][T], kk == 0 ? zero4 : acc[tl][T]); },
             [&](int f) { if (f < 2) pst(egp, 1, f); });
    BAR;
    slab<24, 0, 0>(pp, pp.l0, pp.l1, [&](int f, u32x4 a, int T) { const int kk = f >> 2, tl = f & 3; acc[tl][T] = mfma(a, kk < 3 ? Hf[3 + kk][T] : Cf[kk][T], acc[tl][T]); }, noex);
    BAR;
    slab<20, 0, 0>(pp, pp.l0, pp.l1, [&](int f, u32x4 a, int T) { const int kk = f >> 2, tl = f & 3; acc[tl][T] = mfma(a, kk < 3 ? Hf[6 + kk][T] : Cf[3 + kk][T], acc[tl][T]); },
             [&](int f) { if (f >= 18) pst(eg, 0, f - 18); });
    STAMP(10 + eg);
  }
  pst(8, 1, 0);
  pst(8, 1, 1);
  BST;
  BSTEND;
  STAMP(19);
}

__global__ __launch_bounds__(256) void combine_kernel(const void* __restrict__ x, const void* __restrict__ ws, void* __restrict__ out) {
  const int lane = threadIdx.x & 63;
  const int ch = blockIdx.x * 4 + __builtin_amdgcn_readfirstlane(threadIdx.x >> 6);
  const int T = ch & 1, u = (ch >> 1) & 1, q = ch >> 2, eg = q % 9, tw = q / 9;
  const int c = lane & 15, g = lane >> 4;
  const int tok = tw * 32 + 8 * (c >> 2) + 4 * T + (c & 3);
  const size_t o = (size_t)tok * D_ + eg * 64 + u * 32 + g * 8;
  const u32x4* p = (const u32x4*)((const char*)ws + PART_OFF + (size_t)ch * 16384) + lane;
  u32x4 v[16];
#pragma unroll
  for (int i = 0; i < 16; ++i) v[i] = __builtin_nontemporal_load(p + i * 64);
  u32x4 xv = *(const u32x4*)((const unsigned short*)x + o);
  float s[8];
#pragma unroll
  for (int k = 0; k < 4; ++k) { s[2 * k] = 0.f; s[2 * k + 1] = 0.f; }
#pragma unroll
  for (int i = 0; i < 16; ++i)
#pragma unroll
    for (int k = 0; k < 4; ++k) { s[2 * k] += __uint_as_float(v[i][k] << 16); s[2 * k + 1] += __uint_as_float(v[i][k] & 0xffff0000u); }
  u32x4 r;
#pragma unroll
  for (int k = 0; k < 4; ++k) r[k] = pk(s[2 * k] + __uint_as_float(xv[k] << 16), s[2 * k + 1] + __uint_as_float(xv[k] & 0xffff0000u));
  *(u32x4*)((unsigned short*)out + o) = r;
}

__global__ void tiny_kernel(unsigned* p) { if (threadIdx.x == 0 && blockIdx.x == 0) p[0] = 0u; }
__global__ void zero_kernel(unsigned* p) { p[threadIdx.x] = 0u; }
}

extern "C" void kernel_launch(void* const* d_in, const int* in_sizes, int n_in, void* d_out, int out_size, void* d_ws, size_t ws_size,
                              hipStream_t stream) {
  (void)in_sizes; (void)n_in; (void)out_size; (void)ws_size;
  (void)hipMemsetAsync(d_ws, 0, fk::CNT_BYTES, stream);
  fk::main_kernel<<<256, 512, 0, stream>>>(d_in[0], d_in[1], d_in[2], d_in[3], d_in[4], d_in[6], d_in[7], d_ws);
  fk::combine_kernel<<<576, 256, 0, stream>>>(d_in[0], d_ws, d_out);
}
```

```cpp
#include <hip/hip_runtime.h>
#include <stdint.h>

typedef __attribute__((ext_vector_type(8))) __bf16 bf16x8;
typedef __attribute__((ext_vector_type(2))) __bf16 bf16x2;
typedef __attribute__((ext_vector_type(4))) float f32x4;
typedef __attribute__((ext_vector_type(2))) float f32x2;
typedef __attribute__((ext_vector_type(4))) unsigned u32x4;
typedef __attribute__((ext_vector_type(2))) unsigned u32x2;
typedef __attribute__((address_space(3))) u32x4* lp16;
typedef __attribute__((address_space(1))) unsigned gu32;
typedef __amdgpu_buffer_rsrc_t rsrc_t;

#define DI __device__ __forceinline__
#define SCHED __builtin_amdgcn_sched_barrier(0)
#define BST do {} while (0)
#define BSTEND do {} while (0)
#define BAR do { BST; asm volatile("s_barrier" ::: "memory"); } while (0)
#define WBAR(w) do { asm volatile("s_waitcnt " w ::: "memory"); BST; asm volatile("s_barrier" ::: "memory"); } while (0)

namespace fk {

constexpr int S_ = 2048, D_ = 576, F_ = 4608;
constexpr int SLOT = 24576, NSLOT = 5, RING = SLOT * NSLOT, SCR = RING + 32768, LDS_BYTES = SCR + 8192;
constexpr unsigned CNT_BYTES = 4096;
constexpr unsigned AB_OFF = 65536, AB_BYTES = 16u * 9 * 8 * 4096;
constexpr unsigned PART_OFF = 8u << 20, PART_BYTES = 2304u * 16384;
constexpr int P = 4;
#define STAMP(i) do {} while (0)

DI rsrc_t mk(const void* p, unsigned bytes) { return __builtin_amdgcn_make_buffer_rsrc((void*)p, (short)0, bytes, 0x00020000); }
DI u32x4 gld4(rsrc_t r, unsigned v, unsigned s) { return __builtin_amdgcn_raw_buffer_load_b128(r, v, s, 0); }
DI u32x2 gld2(rsrc_t r, unsigned v, unsigned s) { return __builtin_amdgcn_raw_buffer_load_b64(r, v, s, 0); }
DI u32x4 lds_ld(unsigned a) { return *(lp16)(size_t)a; }
DI void lds_st(unsigned a, u32x4 v) { *(lp16)(size_t)a = v; }
DI void dma16(rsrc_t srd, unsigned voff, unsigned soff, unsigned ldsaddr) {
  asm volatile("s_mov_b32 m0, %0\n\ts_nop 0\n\tbuffer_load_dwordx4 %1, %2, %3 offen lds" ::"s"(ldsaddr), "v"(voff), "s"(srd), "s"(soff) : "memory");
}
DI f32x4 mfma(u32x4 a, u32x4 b, f32x4 c) {
  return __builtin_amdgcn_mfma_f32_16x16x32_bf16(__builtin_bit_cast(bf16x8, a), __builtin_bit_cast(bf16x8, b), c, 0, 0, 0);
}
DI unsigned pk(float a, float b) {
  f32x2 v = {a, b};
  return __builtin_bit_cast(unsigned, __builtin_convertvector(v, bf16x2));
}
DI float rbf(float f) { return __uint_as_float(pk(f, 0.f) << 16); }
DI unsigned relu2(unsigned v) {
  typedef __attribute__((ext_vector_type(2))) short s16x2;
  s16x2 z = {0, 0};
  return __builtin_bit_cast(unsigned, __builtin_elementwise_max(__builtin_bit_cast(s16x2, v), z));
}
DI int fresh_lane() {
  int l;
  asm volatile("v_mbcnt_lo_u32_b32 %0, -1, 0\n\tv_mbcnt_hi_u32_b32 %0, -1, %0" : "=v"(l));
  return l;
}
constexpr int cls(int f) { return (f ^ (f >> 1)) & 1; }
DI unsigned nextslot(unsigned s, unsigned inc) { s += inc; return s >= (unsigned)RING ? s - RING : s; }

struct Pipe {
  u32x4 A[P];
  unsigned l0, l1;
  unsigned c0, c1, n0, n1;
  unsigned sn;
};
template <int TY> DI unsigned faddr(int f, unsigned b0, unsigned b1) {
  if (TY == 0) return (cls(f) ? b1 : b0) + f * 1024;
  if (TY == 1) return (((f >> 2) & 1) ? b1 : b0) + ((f >> 3) * 4 + (f & 3)) * 2048;
  return ((f & 1) ? b1 : b0) + (f >> 1) * 2048;
}
template <int NF, int TY, int TYN, class OP, class EX>
DI void slab(Pipe& pp, unsigned a0, unsigned a1, OP op, EX ex) {
  u32x4 buf[NF + P];
#pragma unroll
  for (int i = 0; i < P; ++i) buf[i] = pp.A[i];
#pragma unroll
  for (int f = 0; f < NF; ++f) {
    op(f, buf[f], 0);
    SCHED;
    const int fn = f + P;
    if (fn < NF) buf[fn] = lds_ld(faddr<TY>(fn, pp.c0, pp.c1));
    else buf[fn] = lds_ld(faddr<TYN>(fn - NF, pp.n0, pp.n1));
    SCHED;
    op(f, buf[f], 1);
    SCHED;
    ex(f);
    SCHED;
  }
#pragma unroll
  for (int i = 0; i < P; ++i) pp.A[i] = buf[NF + i];
  pp.c0 = pp.n0; pp.c1 = pp.n1; pp.sn = nextslot(pp.sn, SLOT); pp.n0 = a0 + pp.sn; pp.n1 = a1 + pp.sn;
}

DI void tp8(const u32x4 (&in)[8], u32x4 (&o)[8]) {
#pragma unroll
  for (int i = 0; i < 8; ++i)
#pragma unroll
    for (int rp = 0; rp < 4; ++rp) o[i][rp] = __builtin_amdgcn_perm(in[2 * rp + 1][i >> 1], in[2 * rp][i >> 1], (i & 1) ? 0x07060302u : 0x05040100u);
}
DI void tp4(const u32x2 (&in)[8], u32x4 (&o)[8]) {
#pragma unroll
  for (int i = 0; i < 4; ++i)
#pragma unroll
    for (int rp = 0; rp < 4; ++rp) o[i][rp] = __builtin_amdgcn_perm(in[2 * rp + 1][i >> 1], in[2 * rp][i >> 1], (i & 1) ? 0x07060302u : 0x05040100u);
}
DI void wr8(const u32x4 (&o)[8], const unsigned (&wad)[4], unsigned sb, unsigned off) {
  unsigned t[4];
#pragma unroll
  for (int q = 0; q < 4; ++q) t[q] = wad[q] + sb;
#pragma unroll
  for (int i = 0; i < 8; ++i) lds_st(t[(i & 3) ^ (i >> 2)] + off + (i >> 2) * 1024, o[i]);
}
DI void wr4(const u32x4 (&o)[8], const unsigned (&wad)[4], unsigned sb, unsigned off) {
#pragma unroll
  for (int i = 0; i < 4; ++i) lds_st(wad[i] + sb + off, o[i]);
}
#define LBAR WBAR("lgkmcnt(0)")

__global__ __launch_bounds__(512, 2) void main_kernel(const void* __restrict__ x, const void* __restrict__ cr, const void* __restrict__ ci,
                                                      const void* __restrict__ wr, const void* __restrict__ wi, const void* __restrict__ w1,
                                                      const void* __restrict__ w2, void* __restrict__ ws) {
  __shared__ __attribute__((aligned(1024))) char smem[LDS_BYTES];
  const int wid = __builtin_amdgcn_readfirstlane(threadIdx.x >> 6);
  const unsigned ldsb = (unsigned)(size_t)smem;
  const int xcd = blockIdx.x & 7, idx = blockIdx.x >> 3;
  const int tb = (xcd & 1) * 8 + (idx & 7), j = (xcd >> 1) * 4 + (idx >> 3);
  const int jodd = j & 1;
  const int fb0 = j * 288 + (jodd ? 32 : 0), fbl = j * 288 + (jodd ? 0 : 256);
  gu32* cnt = (gu32*)ws;
  {
    unsigned long long pc;
    asm volatile("s_getpc_b64 %0" : "=s"(pc));
    rsrc_t rc = mk((const void*)pc, 36864);
    const unsigned vo = fresh_lane() * 16 + wid * 1024;
    u32x4 t[5];
#pragma unroll
    for (int i = 0; i < 5; ++i) t[i] = gld4(rc, vo, i * 8192);
#pragma unroll
    for (int i = 0; i < 5; ++i) asm volatile("" :: "v"(t[i]));
  }
#define LANEVARS                                                                                   \
  const int lane = fresh_lane();                                                                   \
  const int c = lane & 15, g = lane >> 4, l7 = lane & 7, l3 = lane >> 3;                           \
  const int rowp = 8 * (l3 >> 2) + (l3 & 3);   \
  (void)c; (void)g; (void)l7; (void)rowp;

  if (wid >= 4) {
    const int lw = wid - 4;
    LANEVARS
    STAMP(0);
    {
      const unsigned v0 = rowp * 1152 + ((l7 ^ g) * 16), v1 = rowp * 1152 + ((l7 ^ g ^ 4) * 16);
      if (lw < 3) {
        rsrc_t rw = mk(j < 9 ? wr : wi, D_ * D_ * 2);
        const unsigned sb = (unsigned)((j < 9 ? j : j - 9) * 64 * 1152 + lw * 6 * 64);
#pragma unroll
        for (int f = 0; f < 24; ++f) {
          const int hb = f & 1, tl = (f >> 1) & 3, kp = f >> 3;
          dma16(rw, hb ? v1 : v0, sb + (32 * (tl >> 1) + 4 * (tl & 1) + 16 * hb) * 1152 + kp * 128, ldsb + lw * SLOT + f * 1024);
        }
      } else {
        rsrc_t rw = mk(wi, D_ * D_ * 2);
        const int t = 28 + (j >> 1);
        const unsigned sb = (unsigned)((32 * (t >> 1) + 4 * (t & 1)) * 1152);
#pragma unroll
        for (int f = 0; f < 18; ++f) dma16(rw, (f & 1) ? v1 : v0, sb + (f & 1) * 16 * 1152 + (f >> 1) * 128, ldsb + 3 * SLOT + f * 1024);
      }
      asm volatile("s_waitcnt vmcnt(0)" ::: "memory");
    }
    STAMP(1);
    if (lw < 3) {
      rsrc_t r1 = mk(w1, D_ * F_ * 2), r2 = mk(w2, D_ * F_ * 2);
      const int cg8 = lane & 7, rg = lane >> 3;
      unsigned wa[4], wb[4], wc[4];
      {
        const int u = cg8 >> 2, gp = cg8 & 3, x0 = u | ((gp >> 1) << 1);
        const unsigned base = ldsb + ((rg >> 2) * 4 + 2 * u) * 1024 + ((rg & 3) * 16 + 4 * gp) * 16;
#pragma unroll
        for (int q = 0; q < 4; ++q) wa[q] = base + ((q ^ x0) * 16);
      }
      {
        const int h = c & 1, gp = (c >> 1) & 3, u = c >> 3, x0 = (h ^ u) | ((gp >> 1) << 1);
        const unsigned base = ldsb + (8 + 2 * u + h) * 1024 + (g * 16 + 4 * gp) * 16;
#pragma unroll
        for (int q = 0; q < 4; ++q) wb[q] = base + ((q ^ x0) * 16);
      }
      {
        const int h = cg8 & 1, gp = cg8 >> 1, ksl = rg >> 2, x0 = (h ^ ksl) | ((gp >> 1) << 1);
        const unsigned base = ldsb + (ksl * 2 + h) * 1024 + ((rg & 3) * 16 + 4 * gp) * 16;
#pragma unroll
        for (int q = 0; q < 4; ++q) wc[q] = base + ((q ^ x0) * 16);
      }
      const unsigned v1a = (unsigned)((192 * lw + 8 * rg) * (F_ * 2) + cg8 * 16 + fb0 * 2);
      const unsigned v1c = (unsigned)((192 * lw + 8 * rg) * (F_ * 2) + cg8 * 8 + fbl * 2);
      auto fk = [&](int kap) { return kap < 8 ? fb0 + 32 * kap : fbl; };
      const unsigned v2a = (unsigned)((fk(3 * lw + (rg >> 2)) + 8 * (rg & 3)) * (D_ * 2) + cg8 * 16);
      const unsigned v2b = (unsigned)((fk(3 * lw + 2) + 8 * g) * (D_ * 2) + c * 8);
      u32x4 X[6][8];
      u32x2 Y[3][8];
      u32x4 PA[2][8];
      u32x2 PB[2][8];
      auto ld1 = [&](u32x4(&d)[8], int rho) {
#pragma unroll
        for (int r = 0; r < 8; ++r) d[r] = gld4(r1, v1a, (unsigned)(((rho % 3) * 64 + r) * (F_ * 2) + (rho / 3) * 128));
      };
      auto ld1c = [&](u32x2(&d)[8], int rd) {
#pragma unroll
        for (int r = 0; r < 8; ++r) d[r] = gld2(r1, v1c, (unsigned)((rd * 64 + r) * (F_ * 2)));
      };
      auto ld2a = [&](u32x4(&d)[8], int eg) {
#pragma unroll
        for (int r = 0; r < 8; ++r) d[r] = gld4(r2, v2a, (unsigned)(r * (D_ * 2) + eg * 128));
      };
      auto ld2b = [&](u32x2(&d)[8], int eg) {
#pragma unroll
        for (int r = 0; r < 8; ++r) d[r] = gld2(r2, v2b, (unsigned)(r * (D_ * 2) + eg * 128));
      };
#pragma unroll
      for (int r = 0; r < 6; ++r) ld1(X[r], r);
      SCHED;
      u32x4 O[8];
      tp8(X[0], O);
      SCHED;
      for (int i = 0; i <= lw; ++i) BAR;
      unsigned sb = (unsigned)(((4 + lw) % NSLOT) * SLOT);
#pragma unroll
      for (int rho = 0; rho < 12; ++rho) {
        wr8(O, wa, sb, (rho % 3) * 8192);
        SCHED;
        if (rho < 6) ld1(X[rho], rho + 6);
        else if (rho < 9) ld1c(Y[rho - 6], rho - 6);
        else if (rho == 9) ld2a(PA[0], 0);
        else if (rho == 10) ld2b(PB[0], 0);
        SCHED;
        if (rho < 11) tp8(X[(rho + 1) % 6], O);
        else tp4(Y[0], O);
        SCHED;
        LBAR;
        if (rho % 3 == 2) sb = nextslot(sb, 3 * SLOT);
      }
#pragma unroll
      for (int rd = 0; rd < 3; ++rd) {
        wr4(O, wc, sb, rd * 4096);
        SCHED;
        if (rd == 0) ld2a(PA[1], 1);
        else if (rd == 1) ld2b(PB[1], 1);
        SCHED;
        if (rd < 2) tp4(Y[rd + 1], O);
        else tp8(PA[0], O);
        SCHED;
        LBAR;
      }
      sb = nextslot(sb, 3 * SLOT);
      auto job2 = [&](u32x4(&a)[8], u32x2(&b)[8], u32x4(&an)[8], int egn) {
        wr8(O, wa, sb, 0);
        SCHED;
        ld2a(a, egn);
        SCHED;
        tp4(b, O);
        SCHED;
        LBAR;
        wr4(O, wb, sb, 0);
        SCHED;
        ld2b(b, egn);
        SCHED;
        LBAR;
        tp8(an, O);
        SCHED;
        BAR;
        sb = nextslot(sb, 3 * SLOT);
      };
      for (int eg = 0; eg < 8; eg += 2) {
        job2(PA[0], PB[0], PA[1], eg + 2);
        job2(PA[1], PB[1], PA[0], eg + 3 < 9 ? eg + 3 : 8);
      }
      job2(PA[0], PB[0], PA[1], 8);
      for (int i = 0; i < 3 - lw; ++i) BAR;
      STAMP(2);
    } else {
      rsrc_t rab = mk((char*)ws + AB_OFF, AB_BYTES);
      for (int b = 0; b <= 9; ++b) BAR;
      unsigned v = __hip_atomic_load(cnt + j * 32, __ATOMIC_RELAXED, __HIP_MEMORY_SCOPE_AGENT);
      for (int b = 10; b <= 13; ++b) BAR;
      while (v < 64u) {
        __builtin_amdgcn_s_sleep(2);
        v = __hip_atomic_load(cnt + j * 32, __ATOMIC_RELAXED, __HIP_MEMORY_SCOPE_AGENT);
      }
      __builtin_amdgcn_fence(__ATOMIC_ACQUIRE, "agent");
      STAMP(2);
      BAR;
      BAR;
      unsigned sb = (unsigned)((19 % NSLOT) * SLOT) + 12288;
      unsigned so = (unsigned)(j * 9 * 32768);
      const unsigned vo = lane * 16;
      auto issue = [&](int n) {
        for (int i = 0; i < n; ++i) dma16(rab, vo, so + i * 1024, ldsb + sb + i * 1024);
        so += n * 1024;
        sb = nextslot(sb, SLOT);
      };
      issue(12); BAR;
      issue(12); BAR;
      issue(8);
      for (int eg = 0; eg < 8; ++eg) {
        WBAR("vmcnt(20)");
        issue(12);
        WBAR("vmcnt(20)");
        issue(12);
        WBAR("vmcnt(24)");
        issue(8);
      }
      WBAR("vmcnt(20)");
      WBAR("vmcnt(8)");
      WBAR("vmcnt(0)");
      BAR;
      STAMP(3);
    }
    BSTEND;
    return;
  }

  const int w = wid;
  LANEVARS
  STAMP(0);
  const int sw = (c >> 3) << 1;
  const unsigned off0 = (g * 16 + (c ^ sw)) * 16, off1 = off0 ^ 16;
#define SHUFVARS                                                                                                   \
  const unsigned m0 = (c >> 3) * 1024 + (c & 7) * 128 + ((g ^ (c >> 1)) * 16), m1 = m0 ^ 64;                       \
  const unsigned scr = ldsb + SCR + w * 2048;                                                                      \
  const unsigned sr0 = scr + m0, sr1 = scr + m1;                                                                   \
  const unsigned sw0 = scr + l3 * 128 + ((l7 ^ g) * 16), sw1 = scr + 1024 + l3 * 128 + ((l7 ^ g ^ 4) * 16);       \
  auto shuf = [&](u32x4& a, u32x4& b) {          \
    lds_st(sw0, a); lds_st(sw1, b);                                                                                \
    a = lds_ld(sr0); b = lds_ld(sr1);                                                                              \
  };                                                                                                               \
  const int tokp = tb * 128 + w * 32 + rowp;
  SHUFVARS
  u32x4 xf[18][2];
  {
    rsrc_t rx = mk(x, S_ * D_ * 2);
    const unsigned vx = (unsigned)(tokp * (D_ * 2) + l7 * 16);
#pragma unroll
    for (int kp = 0; kp < 9; ++kp)
#pragma unroll
      for (int T = 0; T < 2; ++T)
#pragma unroll
        for (int hb = 0; hb < 2; ++hb) { xf[2 * kp + hb][T] = gld4(rx, vx + kp * 128, (16 * hb + 4 * T) * D_ * 2); SCHED; }
    float s1[2][2] = {{0.f, 0.f}, {0.f, 0.f}}, s2[2][2] = {{0.f, 0.f}, {0.f, 0.f}};
    const bf16x2 ones = __builtin_bit_cast(bf16x2, 0x3f803f80u);
#pragma unroll
    for (int kp = 0; kp <= 9; ++kp) {
      if (kp < 9) {
#pragma unroll
        for (int T = 0; T < 2; ++T) shuf(xf[2 * kp][T], xf[2 * kp + 1][T]);
      }
      SCHED;
      if (kp > 0) {
#pragma unroll
        for (int ks = 2 * kp - 2; ks < 2 * kp; ++ks)
#pragma unroll
          for (int T = 0; T < 2; ++T)
#pragma unroll
            for (int q = 0; q < 4; ++q) {
              const unsigned tq = xf[ks][T][q];
              bf16x2 v = __builtin_bit_cast(bf16x2, tq);
              s1[T][q & 1] = __builtin_amdgcn_fdot2_f32_bf16(v, ones, s1[T][q & 1], false);
              s2[T][q & 1] = __builtin_amdgcn_fdot2_f32_bf16(v, v, s2[T][q & 1], false);
            }
      }
      SCHED;
    }
    STAMP(1);
    float rs[2], nb[2];
#pragma unroll
    for (int T = 0; T < 2; ++T) {
      float a = s1[T][0] + s1[T][1], b = s2[T][0] + s2[T][1];
      a += __shfl_xor(a, 16); b += __shfl_xor(b, 16);
      a += __shfl_xor(a, 32); b += __shfl_xor(b, 32);
      const float mean = a * (1.0f / D_);
      const float var = fmaxf(b * (1.0f / D_) - mean * mean, 0.f);
      const float den = rbf(__builtin_sqrtf(rbf(rbf(var) + 1e-5f)));
      rs[T] = 1.0f / den;
      nb[T] = -rbf(mean) * rs[T];
    }
#pragma unroll
    for (int ks = 0; ks < 18; ++ks)
#pragma unroll
      for (int T = 0; T < 2; ++T)
#pragma unroll
        for (int q = 0; q < 4; ++q) {
          const unsigned v = xf[ks][T][q];
          xf[ks][T][q] = pk(__builtin_fmaf(__uint_as_float(v << 16), rs[T], nb[T]), __builtin_fmaf(__uint_as_float(v & 0xffff0000u), rs[T], nb[T]));
        }
  }
  STAMP(2);
  Pipe pp;
  pp.l0 = ldsb + off0; pp.l1 = ldsb + off1;
  const unsigned r0 = ldsb + m0, r1 = ldsb + m1;
  pp.c0 = r0; pp.c1 = r1; pp.sn = SLOT; pp.n0 = r0 + SLOT; pp.n1 = r1 + SLOT;
  const f32x4 zero4 = {0.f, 0.f, 0.f, 0.f};
  auto noex = [](int) {};
  f32x4 acc[4][2];
  SCHED;
  BAR;
  STAMP(3);
#pragma unroll
  for (int i = 0; i < P; ++i) pp.A[i] = lds_ld(faddr<1>(i, pp.c0, pp.c1));
  SCHED;
  rsrc_t rab = mk((char*)ws + AB_OFF, AB_BYTES);
  slab<24, 1, 1>(pp, r0, r1, [&](int f, u32x4 a, int T) { const int ks = (f >> 2), tl = f & 3; acc[tl][T] = mfma(xf[ks][T], a, ks == 0 ? zero4 : acc[tl][T]); }, noex);
  BAR;
  slab<24, 1, 1>(pp, r0, r1, [&](int f, u32x4 a, int T) { const int ks = 6 + (f >> 2), tl = f & 3; acc[tl][T] = mfma(xf[ks][T], a, acc[tl][T]); }, noex);
  BAR;
  slab<24, 1, 2>(pp, pp.l0, pp.l1, [&](int f, u32x4 a, int T) { const int ks = 12 + (f >> 2), tl = f & 3; acc[tl][T] = mfma(xf[ks][T], a, acc[tl][T]); }, noex);
  BAR;
  {
    const int mat = j < 9 ? 0 : 1, eg = j < 9 ? j : j - 9;
    const unsigned sg = mat ? 0x80008000u : 0u;
    const unsigned so = (unsigned)((((tb * 9 + eg) * 8) + mat * 4 + w) * 4096);
    f32x4 hacc;
    auto ex = [&](int f) {
      if (f >= 1 && f < 5) {
        const int tl = f - 1;
        u32x4 o = {pk(acc[tl][0][0], acc[tl][0][1]) ^ sg, pk(acc[tl][0][2], acc[tl][0][3]) ^ sg, pk(acc[tl][1][0], acc[tl][1][1]) ^ sg, pk(acc[tl][1][2], acc[tl][1][3]) ^ sg};
        __builtin_amdgcn_raw_buffer_store_b128(o, rab, cls(tl) ? off1 : off0, so + tl * 1024, 16);
      }
    };
    if (jodd) slab<18, 2, 0>(pp, pp.l0, pp.l1, [&](int f, u32x4 a, int T) { if (T == 0) hacc = mfma(xf[f][1], a, f == 0 ? zero4 : hacc); }, ex);
    else slab<18, 2, 0>(pp, pp.l0, pp.l1, [&](int f, u32x4 a, int T) { if (T == 0) hacc = mfma(xf[f][0], a, f == 0 ? zero4 : hacc); }, ex);
    const int t = 28 + (j >> 1), tl = t & 3;
    const unsigned sh = (unsigned)((((tb * 9 + (t >> 2)) * 8) + 4 + w) * 4096 + tl * 1024 + jodd * 8);
    u32x2 o = {pk(-hacc[0], -hacc[1]), pk(-hacc[2], -hacc[3])};
    __builtin_amdgcn_raw_buffer_store_b64(o, rab, ((tl ^ (tl >> 1)) & 1) ? off1 : off0, sh, 16);
  }
  STAMP(4);
  u32x4 Hf[9][2], Cf[8][2];
  const unsigned park = ldsb + RING + w * 8192 + lane * 16;
  auto packh = [&](const f32x4& a, const f32x4& b) -> u32x4 {
    return (u32x4){relu2(pk(a[0], a[1])), relu2(pk(a[2], a[3])), relu2(pk(b[0], b[1])), relu2(pk(b[2], b[3]))};
  };
#pragma unroll 1
  for (int p = 0; p < 2; ++p) {
#pragma unroll
    for (int s = 0; s < 3; ++s) {
      BAR;
      slab<24, 0, 0>(pp, pp.l0, pp.l1, [&](int f, u32x4 a, int T) { const int ks = 6 * s + (f >> 2), tl = f & 3; acc[tl][T] = mfma(a, xf[ks][T], ks == 0 ? zero4 : acc[tl][T]); },
               [&](int f) { if (s == 2 && f >= 22) lds_st(park + p * 4096 + (f - 22) * 1024, packh(acc[0][f - 22], acc[1][f - 22])); });
      if (s == 0 && p == 0) {
        asm volatile("s_waitcnt vmcnt(0)" ::: "memory");
        if (lane == 0) __hip_atomic_fetch_add(cnt + tb * 32, 1u, __ATOMIC_RELAXED, __HIP_MEMORY_SCOPE_AGENT);
      }
    }
    lds_st(park + p * 4096 + 2048, packh(acc[2][0], acc[3][0]));
    lds_st(park + p * 4096 + 3072, packh(acc[2][1], acc[3][1]));
    STAMP(5 + p);
  }
#pragma unroll
  for (int p = 2; p < 4; ++p) {
#pragma unroll
    for (int s = 0; s < 3; ++s) {
      BAR;
      slab<24, 0, 0>(pp, pp.l0, pp.l1, [&](int f, u32x4 a, int T) { const int ks = 6 * s + (f >> 2), tl = f & 3; acc[tl][T] = mfma(a, xf[ks][T], ks == 0 ? zero4 : acc[tl][T]); },
               [&](int f) {
                 if (s == 2 && f >= 22) Hf[2 * p][f - 22] = packh(acc[0][f - 22], acc[1][f - 22]);
                 if (s == 0 && p == 3 && f < 2) Hf[5][f] = packh(acc[2][f], acc[3][f]);
               });
    }
    STAMP(5 + p);
  }
  {
    LANEVARS
    SHUFVARS
    rsrc_t rcr = mk(cr, S_ * S_ * 2), rci = mk(ci, S_ * S_ * 2);
    const unsigned vc = (unsigned)(tokp * (S_ * 2) + j * 256 + l7 * 16);
#pragma unroll
    for (int s = 0; s < 3; ++s) {
      BAR;
      slab<12, 0, 0>(pp, pp.l0, pp.l1, [&](int f, u32x4 a, int T) { const int ks = 6 * s + (f >> 1), tl = f & 1; acc[tl][T] = mfma(a, xf[ks][T], ks == 0 ? zero4 : acc[tl][T]); },
               [&](int f) {
                 if (s == 0 && f < 2) Hf[7][f] = packh(acc[2][f], acc[3][f]);
                 if (f & 1) {
                   const int ks = 6 * s + (f >> 1);
                   if (ks >= 2 && ks < 10) {
                     const int d = ks - 2;
#pragma unroll
                     for (int T = 0; T < 2; ++T) Cf[d][T] = gld4(d < 4 ? rcr : rci, vc + ((d >> 1) & 1) * 128, (16 * (d & 1) + 4 * T) * S_ * 2);
                   } else if (ks >= 10 && ks < 14) {
#pragma unroll
                     for (int T = 0; T < 2; ++T) Hf[ks - 10][T] = lds_ld(park + ((ks - 10) * 2 + T) * 1024);
                   } else if (ks >= 14) {
#pragma unroll
                     for (int T = 0; T < 2; ++T) shuf(Cf[2 * (ks - 14)][T], Cf[2 * (ks - 14) + 1][T]);
                   }
                 }
               });
    }
#pragma unroll
    for (int T = 0; T < 2; ++T) {
      Hf[8][T] = packh(acc[0][T], acc[1][T]);
    }
  }
  STAMP(9);
  rsrc_t rp = mk((char*)ws + PART_OFF, PART_BYTES);
  const unsigned vp = fresh_lane() * 16;
  const unsigned pbase = (unsigned)((tb * 4 + w) * 36 * 16384 + j * 1024);
  auto pst = [&](int eg, int u, int T) {
    const f32x4 &a = acc[2 * u][T], &b = acc[2 * u + 1][T];
    u32x4 o = {pk(a[0], a[1]), pk(a[2], a[3]), pk(b[0], b[1]), pk(b[2], b[3])};
    __builtin_amdgcn_raw_buffer_store_b128(o, rp, vp, pbase + (unsigned)((eg * 4 + u * 2 + T) * 16384), 0);
  };
  asm volatile("" : "=v"(acc[2][0]), "=v"(acc[2][1]), "=v"(acc[3][0]), "=v"(acc[3][1]));
#pragma unroll 1
  for (int eg = 0; eg < 9; ++eg) {
    const int egp = eg > 0 ? eg - 1 : 0;
    BAR;
    slab<24, 0, 0>(pp, pp.l0, pp.l1, [&](int f, u32x4 a, int T) { const int kk = f >> 2, tl = f & 3; acc[tl][T] = mfma(a, kk < 3 ? Hf[kk][T] : Cf[kk - 3][T], kk == 0 ? zero4 : acc[tl][T]); },
             [&](int f) { if (f < 2) pst(egp, 1, f); });
    BAR;
    slab<24, 0, 0>(pp, pp.l0, pp.l1, [&](int f, u32x4 a, int T) { const int kk = f >> 2, tl = f & 3; acc[tl][T] = mfma(a, kk < 3 ? Hf[3 + kk][T] : Cf[kk][T], acc[tl][T]); }, noex);
    BAR;
    slab<20, 0, 0>(pp, pp.l0, pp.l1, [&](int f, u32x4 a, int T) { const int kk = f >> 2, tl = f & 3; acc[tl][T] = mfma(a, kk < 3 ? Hf[6 + kk][T] : Cf[3 + kk][T], acc[tl][T]); },
             [&](int f) { if (f >= 18) pst(eg, 0, f - 18); });
    STAMP(10 + eg);
  }
  pst(8, 1, 0);
  pst(8, 1, 1);
  BST;
  BSTEND;
  STAMP(19);
}

__global__ __launch_bounds__(256) void combine_kernel(const void* __restrict__ x, const void* __restrict__ ws, void* __restrict__ out) {
  const int lane = threadIdx.x & 63;
  const int ch = blockIdx.x * 4 + __builtin_amdgcn_readfirstlane(threadIdx.x >> 6);
  const int T = ch & 1, u = (ch >> 1) & 1, q = ch >> 2, eg = q % 9, tw = q / 9;
  const int c = lane & 15, g = lane >> 4;
  const int tok = tw * 32 + 8 * (c >> 2) + 4 * T + (c & 3);
  const size_t o = (size_t)tok * D_ + eg * 64 + u * 32 + g * 8;
  const u32x4* p = (const u32x4*)((const char*)ws + PART_OFF + (size_t)ch * 16384) + lane;
  u32x4 v[16];
#pragma unroll
  for (int i = 0; i < 16; ++i) v[i] = __builtin_nontemporal_load(p + i * 64);
  u32x4 xv = *(const u32x4*)((const unsigned short*)x + o);
  float s[8];
#pragma unroll
  for (int k = 0; k < 4; ++k) { s[2 * k] = 0.f; s[2 * k + 1] = 0.f; }
#pragma unroll
  for (int i = 0; i < 16; ++i)
#pragma unroll
    for (int k = 0; k < 4; ++k) { s[2 * k] += __uint_as_float(v[i][k] << 16); s[2 * k + 1] += __uint_as_float(v[i][k] & 0xffff0000u); }
  u32x4 r;
#pragma unroll
  for (int k = 0; k < 4; ++k) r[k] = pk(s[2 * k] + __uint_as_float(xv[k] << 16), s[2 * k + 1] + __uint_as_float(xv[k] & 0xffff0000u));
  *(u32x4*)((unsigned short*)out + o) = r;
}

}

extern "C" void kernel_launch(void* const* d_in, const int* in_sizes, int n_in, void* d_out, int out_size, void* d_ws, size_t ws_size,
                              hipStream_t stream) {
  (void)in_sizes; (void)n_in; (void)out_size; (void)ws_size;
  (void)hipMemsetAsync(d_ws, 0, fk::CNT_BYTES, stream);
  fk::main_kernel<<<256, 512, 0, stream>>>(d_in[0], d_in[1], d_in[2], d_in[3], d_in[4], d_in[6], d_in[7], d_ws);
  fk::combine_kernel<<<576, 256, 0, stream>>>(d_in[0], d_ws, d_out);
}
```

```cpp
#include <hip/hip_runtime.h>
#include <stdint.h>

typedef __attribute__((ext_vector_type(2))) __bf16 bf16x2;
typedef __attribute__((ext_vector_type(2))) float f32x2;
typedef __attribute__((ext_vector_type(4))) float f32x4;
typedef __attribute__((ext_vector_type(4))) unsigned u32x4;
typedef __attribute__((ext_vector_type(8))) unsigned u32x8;
typedef __attribute__((ext_vector_type(16))) unsigned u32x16;
typedef __attribute__((address_space(3))) f32x4* lpf4;
typedef __attribute__((address_space(3))) float* lpf;
typedef __attribute__((address_space(1))) const unsigned* gcu32;
typedef __amdgpu_buffer_rsrc_t rsrc_t;
#define DI __device__ __forceinline__

namespace fk {
constexpr int S_ = 2048, D_ = 576, F_ = 4608;
constexpr unsigned XN_OFF = 1u << 20, AB_OFF = 12u << 20, PART_OFF = 20u << 20, FLAG_OFF = 57u << 20, WS_BYTES = 60u << 20;
constexpr unsigned Q2 = 122880, FL = 139264, LIN = 36864, OOB = 0x80000000u, SENT = 0x7f817f81u;
constexpr unsigned CODE_BYTES = 40960;

DI rsrc_t mk(const void* p, unsigned bytes) { return __builtin_amdgcn_make_buffer_rsrc((void*)p, (short)0, bytes, 0x00020000); }
DI u32x4 srd(const void* p, unsigned bytes) {
  const unsigned long long a = (unsigned long long)p;
  return (u32x4){(unsigned)a, (unsigned)(a >> 32) & 0xffffu, bytes, 0x00020000u};
}
DI u32x16 cat4(u32x4 a, u32x4 b, u32x4 c, u32x4 d) {
  return __builtin_shufflevector(__builtin_shufflevector(a, b, 0, 1, 2, 3, 4, 5, 6, 7), __builtin_shufflevector(c, d, 0, 1, 2, 3, 4, 5, 6, 7), 0, 1, 2, 3, 4, 5,
                                 6, 7, 8, 9, 10, 11, 12, 13, 14, 15);
}
DI unsigned pk(float a, float b) {
  f32x2 v = {a, b};
  return __builtin_bit_cast(unsigned, __builtin_convertvector(v, bf16x2));
}
DI float rbf(float f) { return __uint_as_float(pk(f, 0.f) << 16); }
DI float blo(unsigned v) { return __uint_as_float(v << 16); }
DI float bhi(unsigned v) { return __uint_as_float(v & 0xffff0000u); }
DI float dot2(unsigned a, unsigned b, float c) { return __builtin_amdgcn_fdot2_f32_bf16(__builtin_bit_cast(bf16x2, a), __builtin_bit_cast(bf16x2, b), c, false); }
DI unsigned fsw(unsigned q) { return (0x78u >> (2 * q)) & 3u; }

#define BLOCKVARS                                                             \
  const int wid = __builtin_amdgcn_readfirstlane(threadIdx.x >> 6);           \
  const unsigned lane = threadIdx.x & 63, c = lane & 15, g = lane >> 4;       \
  const int xcd = blockIdx.x & 7, idx = blockIdx.x >> 3;                      \
  const int tb = (xcd & 1) * 8 + (idx & 7), j = (xcd >> 1) * 4 + (idx >> 3);  \
  const int jodd = j & 1, eg = j < 9 ? j : j - 9, t5 = 28 + (j >> 1);

__global__ __launch_bounds__(512, 2) void main_kernel(const void* __restrict__ x, const void* __restrict__ cr, const void* __restrict__ ci,
                                                      const void* __restrict__ wr, const void* __restrict__ wi, const void* __restrict__ w1,
                                                      const void* __restrict__ w2, void* __restrict__ ws, void* __restrict__ out) {
  __shared__ __attribute__((aligned(1024))) char smem[163840];
  BLOCKVARS
  const unsigned ldsb = (unsigned)(size_t)smem, lane0 = lane;
  const unsigned row = (lane >> 2) & 7, seg = lane >> 5, pos = lane & 3;
  if (wid < 4) {
    const unsigned w = wid;
    const unsigned t0 = ldsb + seg * 1024 + row * 64 + (row >> 2) * 32 + pos * 8;
    const unsigned rb = ldsb + (c >> 3) * 1024 + (c & 7) * 64 + ((g ^ fsw(c >> 2)) * 16);
    const unsigned xb = ldsb + lane * 16 + 8192 + w * 4096;
    const u32x16 vi = {xb, xb + 65536, rb, rb + 65536, rb + 131072, ldsb + lane * 16 + LIN + 69632, 1, 0,
                       t0, t0 ^ 32, t0 + 61440, (t0 ^ 32) + 61440, ldsb + FL + w * 256 + lane * 4, 0, ldsb + lane * 16 + LIN, rb + Q2 + w * 4096};
    const u32x4 sc = {(unsigned)(((tb * 9 + eg) * 8 + (j < 9 ? 0 : 4) + w) * 4096), (unsigned)(((tb * 9 + (t5 >> 2)) * 8 + 4 + w) * 4096 + (t5 & 3) * 1024),
                      ((w >> 1) ^ (unsigned)jodd) ^ 1u, (unsigned)((tb * 9 * 16 + w * 4) * 16384 + j * 1024)};
    const u32x16 si = cat4(srd((char*)ws + AB_OFF - LIN, 8u << 20), srd((char*)ws + PART_OFF - LIN, 40u << 20), sc, (u32x4){0, 0, 0, j < 9 ? 0u : 0x80008000u});
    asm volatile(
"  v_mov_b32 v253, 0\n"
"  s_mov_b32 s50, 4194304\n"
"  s_mov_b32 s48, 28\n"
"  s_mov_b32 s49, 3\n"
"  ds_write_b32 v252, v253\n"
"  ds_write_b32 v252, v253 offset:1024\n"
"  ds_write_b32 v245, v246 offset:34816\n"
"  ds_write_b32 v245, v246 offset:34820\n"
"  s_waitcnt lgkmcnt(0)\n"
"  s_barrier\n"
"  s_branch .Lc_sp0\n"
".Lc_bk0:\n"
"  ds_read_b128 v[0:3], v240 offset:49152\n"
"  ds_read_b128 v[4:7], v240 offset:50176\n"
"  ds_read_b128 v[144:147], v242 offset:49152\n"
"  ds_read_b128 v[148:151], v242 offset:51200\n"
"  ds_read_b128 v[152:155], v242 offset:53248\n"
"  ds_read_b128 v[156:159], v242 offset:55296\n"
"  ds_read_b128 v[8:11], v240 offset:51200\n"
"  ds_read_b128 v[12:15], v240 offset:52224\n"
"  ds_read_b128 v[160:163], v242 offset:49664\n"
"  s_waitcnt lgkmcnt(6)\n"
"  v_mfma_f32_16x16x32_bf16 v[168:171], v[0:3], v[144:147], 0\n"
"  ds_read_b128 v[164:167], v242 offset:51712\n"
"  v_mfma_f32_16x16x32_bf16 v[172:175], v[4:7], v[144:147], 0\n"
"  s_waitcnt lgkmcnt(6)\n"
"  v_mfma_f32_16x16x32_bf16 v[176:179], v[0:3], v[148:151], 0\n"
"  ds_read_b32 v253, v252\n"
"  v_mfma_f32_16x16x32_bf16 v[180:183], v[4:7], v[148:151], 0\n"
"  ds_read_b128 v[144:147], v242 offset:53760\n"
"  s_waitcnt lgkmcnt(7)\n"
"  v_mfma_f32_16x16x32_bf16 v[184:187], v[0:3], v[152:155], 0\n"
"  ds_read_b128 v[148:151], v242 offset:55808\n"
"  v_mfma_f32_16x16x32_bf16 v[188:191], v[4:7], v[152:155], 0\n"
"  s_waitcnt lgkmcnt(4)\n"
"  v_mfma_f32_16x16x32_bf16 v[192:195], v[0:3], v[156:159], 0\n"
"  v_mfma_f32_16x16x32_bf16 v[196:199], v[4:7], v[156:159], 0\n"
"  ds_read_b128 v[152:155], v244 offset:14336\n"
"  v_mfma_f32_16x16x32_bf16 v[168:171], v[8:11], v[160:163], v[168:171]\n"
"  s_waitcnt lgkmcnt(3)\n"
"  v_cmp_gt_u32 vcc, 2, v253\n"
"  ds_read_b128 v[156:159], v244 offset:14848\n"
"  v_mfma_f32_16x16x32_bf16 v[172:175], v[12:15], v[160:163], v[172:175]\n"
"  ds_inc_u32 v252, v252 offset:1024\n"
"  s_cbranch_vccnz .Lc_sp1\n"
".Lc_bk1:\n"
"  v_mfma_f32_16x16x32_bf16 v[176:179], v[8:11], v[164:167], v[176:179]\n"
"  ds_read_b128 v[16:19], v241 offset:8192\n"
"  ds_read_b128 v[20:23], v241 offset:9216\n"
"  v_mfma_f32_16x16x32_bf16 v[180:183], v[12:15], v[164:167], v[180:183]\n"
"  ds_read_b128 v[160:163], v243 offset:8192\n"
"  s_waitcnt lgkmcnt(6)\n"
"  v_mfma_f32_16x16x32_bf16 v[184:187], v[8:11], v[144:147], v[184:187]\n"
"  ds_read_b128 v[164:167], v243 offset:10240\n"
"  v_mfma_f32_16x16x32_bf16 v[188:191], v[12:15], v[144:147], v[188:191]\n"
"  v_mfma_f32_16x16x32_bf16 v[192:195], v[8:11], v[148:151], v[192:195]\n"
"  v_mfma_f32_16x16x32_bf16 v[196:199], v[12:15], v[148:151], v[196:199]\n"
"  ds_read_b128 v[144:147], v243 offset:12288\n"
"  s_cmp_eq_u32 s46, 0\n"
"  s_waitcnt lgkmcnt(5)\n"
"  s_cbranch_scc1 .Lc_g16\n"
"  v_mfma_f32_16x16x32_bf16 v[200:203], v[0:3], v[152:155], 0\n"
"  v_mfma_f32_16x16x32_bf16 v[204:207], v[4:7], v[152:155], 0\n"
"  v_mfma_f32_16x16x32_bf16 v[200:203], v[8:11], v[156:159], v[200:203]\n"
"  v_mfma_f32_16x16x32_bf16 v[204:207], v[12:15], v[156:159], v[204:207]\n"
".Lc_g16:\n"
"  ds_read_b128 v[148:151], v243 offset:14336\n"
"  ds_read_b128 v[24:27], v241 offset:10240\n"
"  ds_read_b128 v[28:31], v241 offset:11264\n"
"  ds_read_b128 v[152:155], v243 offset:8704\n"
"  s_waitcnt lgkmcnt(6)\n"
"  v_mfma_f32_16x16x32_bf16 v[168:171], v[16:19], v[160:163], v[168:171]\n"
"  ds_read_b128 v[156:159], v243 offset:10752\n"
"  v_mfma_f32_16x16x32_bf16 v[172:175], v[20:23], v[160:163], v[172:175]\n"
"  s_waitcnt lgkmcnt(6)\n"
"  v_mfma_f32_16x16x32_bf16 v[176:179], v[16:19], v[164:167], v[176:179]\n"
"  ds_read_b32 v253, v252\n"
"  v_mfma_f32_16x16x32_bf16 v[180:183], v[20:23], v[164:167], v[180:183]\n"
"  ds_read_b128 v[160:163], v243 offset:12800\n"
"  s_waitcnt lgkmcnt(7)\n"
"  v_mfma_f32_16x16x32_bf16 v[184:187], v[16:19], v[144:147], v[184:187]\n"
"  ds_read_b128 v[164:167], v243 offset:14848\n"
"  v_mfma_f32_16x16x32_bf16 v[188:191], v[20:23], v[144:147], v[188:191]\n"
"  s_waitcnt lgkmcnt(4)\n"
"  v_mfma_f32_16x16x32_bf16 v[192:195], v[16:19], v[148:151], v[192:195]\n"
"  v_mfma_f32_16x16x32_bf16 v[196:199], v[20:23], v[148:151], v[196:199]\n"
"  ds_read_b128 v[144:147], v244 offset:16384\n"
"  v_mfma_f32_16x16x32_bf16 v[168:171], v[24:27], v[152:155], v[168:171]\n"
"  s_waitcnt lgkmcnt(3)\n"
"  v_cmp_gt_u32 vcc, 3, v253\n"
"  ds_read_b128 v[148:151], v244 offset:16896\n"
"  v_mfma_f32_16x16x32_bf16 v[172:175], v[28:31], v[152:155], v[172:175]\n"
"  ds_inc_u32 v252, v252 offset:1024\n"
"  s_cbranch_vccnz .Lc_sp2\n"
".Lc_bk2:\n"
"  v_mfma_f32_16x16x32_bf16 v[176:179], v[24:27], v[156:159], v[176:179]\n"
"  ds_read_b128 v[32:35], v241 offset:32768\n"
"  ds_read_b128 v[36:39], v241 offset:33792\n"
"  v_mfma_f32_16x16x32_bf16 v[180:183], v[28:31], v[156:159], v[180:183]\n"
"  ds_read_b128 v[152:155], v243 offset:32768\n"
"  s_waitcnt lgkmcnt(6)\n"
"  v_mfma_f32_16x16x32_bf16 v[184:187], v[24:27], v[160:163], v[184:187]\n"
"  ds_read_b128 v[156:159], v243 offset:34816\n"
"  v_mfma_f32_16x16x32_bf16 v[188:191], v[28:31], v[160:163], v[188:191]\n"
"  v_mfma_f32_16x16x32_bf16 v[192:195], v[24:27], v[164:167], v[192:195]\n"
"  v_mfma_f32_16x16x32_bf16 v[196:199], v[28:31], v[164:167], v[196:199]\n"
"  ds_read_b128 v[160:163], v243 offset:36864\n"
"  s_cmp_eq_u32 s46, 0\n"
"  s_waitcnt lgkmcnt(5)\n"
"  s_cbranch_scc1 .Lc_g36\n"
"  v_mfma_f32_16x16x32_bf16 v[200:203], v[16:19], v[144:147], v[200:203]\n"
"  v_mfma_f32_16x16x32_bf16 v[204:207], v[20:23], v[144:147], v[204:207]\n"
"  v_mfma_f32_16x16x32_bf16 v[200:203], v[24:27], v[148:151], v[200:203]\n"
"  v_mfma_f32_16x16x32_bf16 v[204:207], v[28:31], v[148:151], v[204:207]\n"
".Lc_g36:\n"
"  ds_read_b128 v[164:167], v243 offset:38912\n"
"  ds_read_b128 v[40:43], v241 offset:34816\n"
"  ds_read_b128 v[44:47], v241 offset:35840\n"
"  ds_read_b128 v[144:147], v243 offset:33280\n"
"  s_waitcnt lgkmcnt(6)\n"
"  v_mfma_f32_16x16x32_bf16 v[168:171], v[32:35], v[152:155], v[168:171]\n"
"  ds_read_b128 v[148:151], v243 offset:35328\n"
"  v_mfma_f32_16x16x32_bf16 v[172:175], v[36:39], v[152:155], v[172:175]\n"
"  s_waitcnt lgkmcnt(6)\n"
"  v_mfma_f32_16x16x32_bf16 v[176:179], v[32:35], v[156:159], v[176:179]\n"
"  ds_read_b32 v253, v252\n"
"  v_mfma_f32_16x16x32_bf16 v[180:183], v[36:39], v[156:159], v[180:183]\n"
"  ds_read_b128 v[152:155], v243 offset:37376\n"
"  s_waitcnt lgkmcnt(7)\n"
"  v_mfma_f32_16x16x32_bf16 v[184:187], v[32:35], v[160:163], v[184:187]\n"
"  ds_read_b128 v[156:159], v243 offset:39424\n"
"  v_mfma_f32_16x16x32_bf16 v[188:191], v[36:39], v[160:163], v[188:191]\n"
"  s_waitcnt lgkmcnt(4)\n"
"  v_mfma_f32_16x16x32_bf16 v[192:195], v[32:35], v[164:167], v[192:195]\n"
"  v_mfma_f32_16x16x32_bf16 v[196:199], v[36:39], v[164:167], v[196:199]\n"
"  ds_read_b128 v[160:163], v244 offset:18432\n"
"  v_mfma_f32_16x16x32_bf16 v[168:171], v[40:43], v[144:147], v[168:171]\n"
"  s_waitcnt lgkmcnt(3)\n"
"  v_cmp_gt_u32 vcc, 4, v253\n"
"  ds_read_b128 v[164:167], v244 offset:18944\n"
"  v_mfma_f32_16x16x32_bf16 v[172:175], v[44:47], v[144:147], v[172:175]\n"
"  ds_inc_u32 v252, v252 offset:1024\n"
"  s_cbranch_vccnz .Lc_sp3\n"
".Lc_bk3:\n"
"  v_mfma_f32_16x16x32_bf16 v[176:179], v[40:43], v[148:151], v[176:179]\n"
"  ds_read_b64_tr_b16 v[144:145], v248 offset:0\n"
"  v_mfma_f32_16x16x32_bf16 v[180:183], v[44:47], v[148:151], v[180:183]\n"
"  ds_read_b64_tr_b16 v[146:147], v248 offset:2048\n"
"  s_waitcnt lgkmcnt(5)\n"
"  v_mfma_f32_16x16x32_bf16 v[184:187], v[40:43], v[152:155], v[184:187]\n"
"  ds_read_b64_tr_b16 v[148:149], v249 offset:0\n"
"  v_mfma_f32_16x16x32_bf16 v[188:191], v[44:47], v[152:155], v[188:191]\n"
"  ds_read_b64_tr_b16 v[150:151], v249 offset:2048\n"
"  v_mfma_f32_16x16x32_bf16 v[192:195], v[40:43], v[156:159], v[192:195]\n"
"  ds_read_b64_tr_b16 v[152:153], v248 offset:512\n"
"  v_mfma_f32_16x16x32_bf16 v[196:199], v[44:47], v[156:159], v[196:199]\n"
"  ds_read_b64_tr_b16 v[154:155], v248 offset:2560\n"
"  s_cmp_eq_u32 s46, 0\n"
"  s_waitcnt lgkmcnt(6)\n"
"  s_cbranch_scc1 .Lc_g56\n"
"  v_mfma_f32_16x16x32_bf16 v[200:203], v[32:35], v[160:163], v[200:203]\n"
"  v_mfma_f32_16x16x32_bf16 v[204:207], v[36:39], v[160:163], v[204:207]\n"
"  v_mfma_f32_16x16x32_bf16 v[200:203], v[40:43], v[164:167], v[200:203]\n"
"  v_mfma_f32_16x16x32_bf16 v[204:207], v[44:47], v[164:167], v[204:207]\n"
".Lc_g56:\n"
"  ds_read_b64_tr_b16 v[156:157], v249 offset:512\n"
"  ds_read_b64_tr_b16 v[158:159], v249 offset:2560\n"
"  ds_read_b64_tr_b16 v[160:161], v248 offset:4096\n"
"  ds_read_b64_tr_b16 v[162:163], v248 offset:6144\n"
"  s_waitcnt lgkmcnt(8)\n"
"  v_mfma_f32_16x16x32_bf16 v[208:211], v[144:147], v[0:3], 0\n"
"  ds_read_b64_tr_b16 v[164:165], v249 offset:4096\n"
"  v_mfma_f32_16x16x32_bf16 v[212:215], v[144:147], v[4:7], 0\n"
"  ds_read_b64_tr_b16 v[166:167], v249 offset:6144\n"
"  s_waitcnt lgkmcnt(8)\n"
"  v_mfma_f32_16x16x32_bf16 v[216:219], v[148:151], v[0:3], 0\n"
"  ds_read_b64_tr_b16 v[144:145], v248 offset:4608\n"
"  v_mfma_f32_16x16x32_bf16 v[220:223], v[148:151], v[4:7], 0\n"
"  ds_read_b64_tr_b16 v[146:147], v248 offset:6656\n"
"  s_waitcnt lgkmcnt(8)\n"
"  v_mfma_f32_16x16x32_bf16 v[224:227], v[152:155], v[0:3], 0\n"
"  ds_read_b64_tr_b16 v[148:149], v249 offset:4608\n"
"  v_mfma_f32_16x16x32_bf16 v[228:231], v[152:155], v[4:7], 0\n"
"  ds_read_b64_tr_b16 v[150:151], v249 offset:6656\n"
"  s_waitcnt lgkmcnt(6)\n"
"  v_mfma_f32_16x16x32_bf16 v[232:235], v[156:159], v[0:3], 0\n"
"  ds_read_b64_tr_b16 v[152:153], v248 offset:8192\n"
"  v_mfma_f32_16x16x32_bf16 v[236:239], v[156:159], v[4:7], 0\n"
"  ds_read_b64_tr_b16 v[154:155], v248 offset:10240\n"
"  v_mfma_f32_16x16x32_bf16 v[208:211], v[160:163], v[8:11], v[208:211]\n"
"  ds_read_b64_tr_b16 v[156:157], v249 offset:8192\n"
"  v_mfma_f32_16x16x32_bf16 v[212:215], v[160:163], v[12:15], v[212:215]\n"
"  ds_read_b64_tr_b16 v[158:159], v249 offset:10240\n"
"  s_waitcnt lgkmcnt(6)\n"
"  v_mfma_f32_16x16x32_bf16 v[216:219], v[164:167], v[8:11], v[216:219]\n"
"  ds_read_b64_tr_b16 v[160:161], v248 offset:8704\n"
"  v_mfma_f32_16x16x32_bf16 v[220:223], v[164:167], v[12:15], v[220:223]\n"
"  ds_read_b64_tr_b16 v[162:163], v248 offset:10752\n"
"  v_mfma_f32_16x16x32_bf16 v[224:227], v[144:147], v[8:11], v[224:227]\n"
"  ds_read_b64_tr_b16 v[164:165], v249 offset:8704\n"
"  v_mfma_f32_16x16x32_bf16 v[228:231], v[144:147], v[12:15], v[228:231]\n"
"  ds_read_b64_tr_b16 v[166:167], v249 offset:10752\n"
"  s_waitcnt lgkmcnt(6)\n"
"  v_mfma_f32_16x16x32_bf16 v[232:235], v[148:151], v[8:11], v[232:235]\n"
"  ds_read_b64_tr_b16 v[144:145], v248 offset:12288\n"
"  v_mfma_f32_16x16x32_bf16 v[236:239], v[148:151], v[12:15], v[236:239]\n"
"  ds_read_b64_tr_b16 v[146:147], v248 offset:14336\n"
"  v_mfma_f32_16x16x32_bf16 v[208:211], v[152:155], v[16:19], v[208:211]\n"
"  ds_read_b64_tr_b16 v[148:149], v249 offset:12288\n"
"  v_mfma_f32_16x16x32_bf16 v[212:215], v[152:155], v[20:23], v[212:215]\n"
"  ds_read_b64_tr_b16 v[150:151], v249 offset:14336\n"
"  s_waitcnt lgkmcnt(6)\n"
"  v_mfma_f32_16x16x32_bf16 v[216:219], v[156:159], v[16:19], v[216:219]\n"
"  ds_read_b64_tr_b16 v[152:153], v248 offset:12800\n"
"  v_mfma_f32_16x16x32_bf16 v[220:223], v[156:159], v[20:23], v[220:223]\n"
"  ds_read_b64_tr_b16 v[154:155], v248 offset:14848\n"
"  v_mfma_f32_16x16x32_bf16 v[224:227], v[160:163], v[16:19], v[224:227]\n"
"  ds_read_b64_tr_b16 v[156:157], v249 offset:12800\n"
"  v_mfma_f32_16x16x32_bf16 v[228:231], v[160:163], v[20:23], v[228:231]\n"
"  ds_read_b64_tr_b16 v[158:159], v249 offset:14848\n"
"  s_waitcnt lgkmcnt(6)\n"
"  v_mfma_f32_16x16x32_bf16 v[232:235], v[164:167], v[16:19], v[232:235]\n"
"  ds_read_b64_tr_b16 v[160:161], v248 offset:16384\n"
"  v_mfma_f32_16x16x32_bf16 v[236:239], v[164:167], v[20:23], v[236:239]\n"
"  ds_read_b64_tr_b16 v[162:163], v248 offset:18432\n"
"  v_mfma_f32_16x16x32_bf16 v[208:211], v[144:147], v[24:27], v[208:211]\n"
"  ds_read_b64_tr_b16 v[164:165], v249 offset:16384\n"
"  v_mfma_f32_16x16x32_bf16 v[212:215], v[144:147], v[28:31], v[212:215]\n"
"  ds_read_b64_tr_b16 v[166:167], v249 offset:18432\n"
"  s_waitcnt lgkmcnt(6)\n"
"  v_mfma_f32_16x16x32_bf16 v[216:219], v[148:151], v[24:27], v[216:219]\n"
"  ds_read_b64_tr_b16 v[144:145], v248 offset:16896\n"
"  v_mfma_f32_16x16x32_bf16 v[220:223], v[148:151], v[28:31], v[220:223]\n"
"  ds_read_b64_tr_b16 v[146:147], v248 offset:18944\n"
"  v_mfma_f32_16x16x32_bf16 v[224:227], v[152:155], v[24:27], v[224:227]\n"
"  ds_read_b64_tr_b16 v[148:149], v249 offset:16896\n"
"  v_mfma_f32_16x16x32_bf16 v[228:231], v[152:155], v[28:31], v[228:231]\n"
"  ds_read_b64_tr_b16 v[150:151], v249 offset:18944\n"
"  s_waitcnt lgkmcnt(6)\n"
"  v_mfma_f32_16x16x32_bf16 v[232:235], v[156:159], v[24:27], v[232:235]\n"
"  ds_read_b32 v253, v252\n"
"  ds_read_b64_tr_b16 v[152:153], v248 offset:20480\n"
"  v_mfma_f32_16x16x32_bf16 v[236:239], v[156:159], v[28:31], v[236:239]\n"
"  ds_read_b64_tr_b16 v[154:155], v248 offset:22528\n"
"  v_mfma_f32_16x16x32_bf16 v[208:211], v[160:163], v[32:35], v[208:211]\n"
"  ds_read_b64_tr_b16 v[156:157], v249 offset:20480\n"
"  v_mfma_f32_16x16x32_bf16 v[212:215], v[160:163], v[36:39], v[212:215]\n"
"  ds_read_b64_tr_b16 v[158:159], v249 offset:22528\n"
"  s_waitcnt lgkmcnt(7)\n"
"  v_mfma_f32_16x16x32_bf16 v[216:219], v[164:167], v[32:35], v[216:219]\n"
"  ds_read_b64_tr_b16 v[160:161], v248 offset:20992\n"
"  v_mfma_f32_16x16x32_bf16 v[220:223], v[164:167], v[36:39], v[220:223]\n"
"  ds_read_b64_tr_b16 v[162:163], v248 offset:23040\n"
"  v_mfma_f32_16x16x32_bf16 v[224:227], v[144:147], v[32:35], v[224:227]\n"
"  s_waitcnt lgkmcnt(5)\n"
"  v_cmp_gt_u32 vcc, 5, v253\n"
"  ds_read_b64_tr_b16 v[164:165], v249 offset:20992\n"
"  v_mfma_f32_16x16x32_bf16 v[228:231], v[144:147], v[36:39], v[228:231]\n"
"  ds_read_b64_tr_b16 v[166:167], v249 offset:23040\n"
"  ds_inc_u32 v252, v252 offset:1024\n"
"  s_cbranch_vccnz .Lc_sp4\n"
".Lc_bk4:\n"
"  v_mfma_f32_16x16x32_bf16 v[232:235], v[148:151], v[32:35], v[232:235]\n"
"  ds_read_b128 v[48:51], v240 offset:24576\n"
"  ds_read_b128 v[52:55], v240 offset:25600\n"
"  v_mfma_f32_16x16x32_bf16 v[236:239], v[148:151], v[36:39], v[236:239]\n"
"  ds_read_b128 v[144:147], v242 offset:24576\n"
"  s_waitcnt lgkmcnt(8)\n"
"  v_mfma_f32_16x16x32_bf16 v[208:211], v[152:155], v[40:43], v[208:211]\n"
"  ds_read_b128 v[148:151], v242 offset:26624\n"
"  v_mfma_f32_16x16x32_bf16 v[212:215], v[152:155], v[44:47], v[212:215]\n"
"  v_mfma_f32_16x16x32_bf16 v[216:219], v[156:159], v[40:43], v[216:219]\n"
"  v_mfma_f32_16x16x32_bf16 v[220:223], v[156:159], v[44:47], v[220:223]\n"
"  ds_read_b128 v[152:155], v242 offset:28672\n"
"  s_waitcnt lgkmcnt(5)\n"
"  v_mfma_f32_16x16x32_bf16 v[224:227], v[160:163], v[40:43], v[224:227]\n"
"  ds_read_b128 v[156:159], v242 offset:30720\n"
"  v_mfma_f32_16x16x32_bf16 v[228:231], v[160:163], v[44:47], v[228:231]\n"
"  v_mfma_f32_16x16x32_bf16 v[232:235], v[164:167], v[40:43], v[232:235]\n"
"  ds_read_b128 v[56:59], v240 offset:26624\n"
"  ds_read_b128 v[60:63], v240 offset:27648\n"
"  v_mfma_f32_16x16x32_bf16 v[236:239], v[164:167], v[44:47], v[236:239]\n"
"  ds_read_b128 v[160:163], v242 offset:25088\n"
"  s_waitcnt lgkmcnt(5)\n"
"  v_mfma_f32_16x16x32_bf16 v[168:171], v[48:51], v[144:147], v[168:171]\n"
"  ds_read_b128 v[164:167], v242 offset:27136\n"
"  v_mfma_f32_16x16x32_bf16 v[172:175], v[52:55], v[144:147], v[172:175]\n"
"  v_mfma_f32_16x16x32_bf16 v[176:179], v[48:51], v[148:151], v[176:179]\n"
"  ds_read_b32 v253, v252\n"
"  v_mfma_f32_16x16x32_bf16 v[180:183], v[52:55], v[148:151], v[180:183]\n"
"  ds_read_b128 v[144:147], v242 offset:29184\n"
"  s_waitcnt lgkmcnt(6)\n"
"  v_mfma_f32_16x16x32_bf16 v[184:187], v[48:51], v[152:155], v[184:187]\n"
"  ds_read_b128 v[148:151], v242 offset:31232\n"
"  v_mfma_f32_16x16x32_bf16 v[188:191], v[52:55], v[152:155], v[188:191]\n"
"  v_mfma_f32_16x16x32_bf16 v[192:195], v[48:51], v[156:159], v[192:195]\n"
"  v_mfma_f32_16x16x32_bf16 v[196:199], v[52:55], v[156:159], v[196:199]\n"
"  ds_read_b128 v[152:155], v244 offset:20480\n"
"  s_waitcnt lgkmcnt(4)\n"
"  v_mfma_f32_16x16x32_bf16 v[168:171], v[56:59], v[160:163], v[168:171]\n"
"  s_waitcnt lgkmcnt(3)\n"
"  v_cmp_gt_u32 vcc, 6, v253\n"
"  ds_read_b128 v[156:159], v244 offset:20992\n"
"  v_mfma_f32_16x16x32_bf16 v[172:175], v[60:63], v[160:163], v[172:175]\n"
"  ds_inc_u32 v252, v252 offset:1024\n"
"  s_cbranch_vccnz .Lc_sp5\n"
".Lc_bk5:\n"
"  v_mfma_f32_16x16x32_bf16 v[176:179], v[56:59], v[164:167], v[176:179]\n"
"  ds_read_b128 v[64:67], v240 offset:49152\n"
"  ds_read_b128 v[68:71], v240 offset:50176\n"
"  v_mfma_f32_16x16x32_bf16 v[180:183], v[60:63], v[164:167], v[180:183]\n"
"  ds_read_b128 v[160:163], v242 offset:49152\n"
"  s_waitcnt lgkmcnt(6)\n"
"  v_mfma_f32_16x16x32_bf16 v[184:187], v[56:59], v[144:147], v[184:187]\n"
"  ds_read_b128 v[164:167], v242 offset:51200\n"
"  v_mfma_f32_16x16x32_bf16 v[188:191], v[60:63], v[144:147], v[188:191]\n"
"  v_mfma_f32_16x16x32_bf16 v[192:195], v[56:59], v[148:151], v[192:195]\n"
"  v_mfma_f32_16x16x32_bf16 v[196:199], v[60:63], v[148:151], v[196:199]\n"
"  ds_read_b128 v[144:147], v242 offset:53248\n"
"  s_cmp_eq_u32 s46, 0\n"
"  s_waitcnt lgkmcnt(5)\n"
"  s_cbranch_scc1 .Lc_g124\n"
"  v_mfma_f32_16x16x32_bf16 v[200:203], v[48:51], v[152:155], v[200:203]\n"
"  v_mfma_f32_16x16x32_bf16 v[204:207], v[52:55], v[152:155], v[204:207]\n"
"  v_mfma_f32_16x16x32_bf16 v[200:203], v[56:59], v[156:159], v[200:203]\n"
"  v_mfma_f32_16x16x32_bf16 v[204:207], v[60:63], v[156:159], v[204:207]\n"
".Lc_g124:\n"
"  ds_read_b128 v[148:151], v242 offset:55296\n"
"  ds_read_b128 v[72:75], v240 offset:51200\n"
"  ds_read_b128 v[76:79], v240 offset:52224\n"
"  ds_read_b128 v[152:155], v242 offset:49664\n"
"  s_waitcnt lgkmcnt(6)\n"
"  v_mfma_f32_16x16x32_bf16 v[168:171], v[64:67], v[160:163], v[168:171]\n"
"  ds_read_b128 v[156:159], v242 offset:51712\n"
"  v_mfma_f32_16x16x32_bf16 v[172:175], v[68:71], v[160:163], v[172:175]\n"
"  s_waitcnt lgkmcnt(6)\n"
"  v_mfma_f32_16x16x32_bf16 v[176:179], v[64:67], v[164:167], v[176:179]\n"
"  ds_read_b32 v253, v252\n"
"  v_mfma_f32_16x16x32_bf16 v[180:183], v[68:71], v[164:167], v[180:183]\n"
"  ds_read_b128 v[160:163], v242 offset:53760\n"
"  s_waitcnt lgkmcnt(7)\n"
"  v_mfma_f32_16x16x32_bf16 v[184:187], v[64:67], v[144:147], v[184:187]\n"
"  ds_read_b128 v[164:167], v242 offset:55808\n"
"  v_mfma_f32_16x16x32_bf16 v[188:191], v[68:71], v[144:147], v[188:191]\n"
"  s_waitcnt lgkmcnt(4)\n"
"  v_mfma_f32_16x16x32_bf16 v[192:195], v[64:67], v[148:151], v[192:195]\n"
"  v_mfma_f32_16x16x32_bf16 v[196:199], v[68:71], v[148:151], v[196:199]\n"
"  ds_read_b128 v[144:147], v244 offset:22528\n"
"  v_mfma_f32_16x16x32_bf16 v[168:171], v[72:75], v[152:155], v[168:171]\n"
"  s_waitcnt lgkmcnt(3)\n"
"  v_cmp_gt_u32 vcc, 7, v253\n"
"  ds_read_b128 v[148:151], v244 offset:23040\n"
"  v_mfma_f32_16x16x32_bf16 v[172:175], v[76:79], v[152:155], v[172:175]\n"
"  ds_inc_u32 v252, v252 offset:1024\n"
"  s_cbranch_vccnz .Lc_sp6\n"
".Lc_bk6:\n"
"  v_mfma_f32_16x16x32_bf16 v[176:179], v[72:75], v[156:159], v[176:179]\n"
"  ds_read_b128 v[80:83], v241 offset:8192\n"
"  ds_read_b128 v[84:87], v241 offset:9216\n"
"  v_mfma_f32_16x16x32_bf16 v[180:183], v[76:79], v[156:159], v[180:183]\n"
"  ds_read_b128 v[152:155], v243 offset:8192\n"
"  s_waitcnt lgkmcnt(6)\n"
"  v_mfma_f32_16x16x32_bf16 v[184:187], v[72:75], v[160:163], v[184:187]\n"
"  ds_read_b128 v[156:159], v243 offset:10240\n"
"  v_mfma_f32_16x16x32_bf16 v[188:191], v[76:79], v[160:163], v[188:191]\n"
"  v_mfma_f32_16x16x32_bf16 v[192:195], v[72:75], v[164:167], v[192:195]\n"
"  v_mfma_f32_16x16x32_bf16 v[196:199], v[76:79], v[164:167], v[196:199]\n"
"  ds_read_b128 v[160:163], v243 offset:12288\n"
"  s_cmp_eq_u32 s46, 0\n"
"  s_waitcnt lgkmcnt(5)\n"
"  s_cbranch_scc1 .Lc_g144\n"
"  v_mfma_f32_16x16x32_bf16 v[200:203], v[64:67], v[144:147], v[200:203]\n"
"  v_mfma_f32_16x16x32_bf16 v[204:207], v[68:71], v[144:147], v[204:207]\n"
"  v_mfma_f32_16x16x32_bf16 v[200:203], v[72:75], v[148:151], v[200:203]\n"
"  v_mfma_f32_16x16x32_bf16 v[204:207], v[76:79], v[148:151], v[204:207]\n"
".Lc_g144:\n"
"  ds_read_b128 v[164:167], v243 offset:14336\n"
"  ds_read_b128 v[88:91], v241 offset:10240\n"
"  ds_read_b128 v[92:95], v241 offset:11264\n"
"  ds_read_b128 v[144:147], v243 offset:8704\n"
"  s_waitcnt lgkmcnt(6)\n"
"  v_mfma_f32_16x16x32_bf16 v[168:171], v[80:83], v[152:155], v[168:171]\n"
"  ds_read_b128 v[148:151], v243 offset:10752\n"
"  v_mfma_f32_16x16x32_bf16 v[172:175], v[84:87], v[152:155], v[172:175]\n"
"  s_waitcnt lgkmcnt(6)\n"
"  v_mfma_f32_16x16x32_bf16 v[176:179], v[80:83], v[156:159], v[176:179]\n"
"  ds_read_b32 v253, v252\n"
"  v_mfma_f32_16x16x32_bf16 v[180:183], v[84:87], v[156:159], v[180:183]\n"
"  ds_read_b128 v[152:155], v243 offset:12800\n"
"  s_waitcnt lgkmcnt(7)\n"
"  v_mfma_f32_16x16x32_bf16 v[184:187], v[80:83], v[160:163], v[184:187]\n"
"  ds_read_b128 v[156:159], v243 offset:14848\n"
"  v_mfma_f32_16x16x32_bf16 v[188:191], v[84:87], v[160:163], v[188:191]\n"
"  s_waitcnt lgkmcnt(4)\n"
"  v_mfma_f32_16x16x32_bf16 v[192:195], v[80:83], v[164:167], v[192:195]\n"
"  v_mfma_f32_16x16x32_bf16 v[196:199], v[84:87], v[164:167], v[196:199]\n"
"  ds_read_b128 v[160:163], v244 offset:24576\n"
"  v_mfma_f32_16x16x32_bf16 v[168:171], v[88:91], v[144:147], v[168:171]\n"
"  s_waitcnt lgkmcnt(3)\n"
"  v_cmp_gt_u32 vcc, 8, v253\n"
"  ds_read_b128 v[164:167], v244 offset:25088\n"
"  v_mfma_f32_16x16x32_bf16 v[172:175], v[92:95], v[144:147], v[172:175]\n"
"  ds_inc_u32 v252, v252 offset:1024\n"
"  s_cbranch_vccnz .Lc_sp7\n"
".Lc_bk7:\n"
"  v_mfma_f32_16x16x32_bf16 v[176:179], v[88:91], v[148:151], v[176:179]\n"
"  ds_read_b64_tr_b16 v[144:145], v250 offset:36864\n"
"  v_mfma_f32_16x16x32_bf16 v[180:183], v[92:95], v[148:151], v[180:183]\n"
"  ds_read_b64_tr_b16 v[146:147], v250 offset:38912\n"
"  s_waitcnt lgkmcnt(5)\n"
"  v_mfma_f32_16x16x32_bf16 v[184:187], v[88:91], v[152:155], v[184:187]\n"
"  ds_read_b64_tr_b16 v[148:149], v251 offset:36864\n"
"  v_mfma_f32_16x16x32_bf16 v[188:191], v[92:95], v[152:155], v[188:191]\n"
"  ds_read_b64_tr_b16 v[150:151], v251 offset:38912\n"
"  v_mfma_f32_16x16x32_bf16 v[192:195], v[88:91], v[156:159], v[192:195]\n"
"  ds_read_b64_tr_b16 v[152:153], v250 offset:37376\n"
"  v_mfma_f32_16x16x32_bf16 v[196:199], v[92:95], v[156:159], v[196:199]\n"
"  ds_read_b64_tr_b16 v[154:155], v250 offset:39424\n"
"  s_cmp_eq_u32 s46, 0\n"
"  s_waitcnt lgkmcnt(6)\n"
"  s_cbranch_scc1 .Lc_g164\n"
"  v_mfma_f32_16x16x32_bf16 v[200:203], v[80:83], v[160:163], v[200:203]\n"
"  v_mfma_f32_16x16x32_bf16 v[204:207], v[84:87], v[160:163], v[204:207]\n"
"  v_mfma_f32_16x16x32_bf16 v[200:203], v[88:91], v[164:167], v[200:203]\n"
"  v_mfma_f32_16x16x32_bf16 v[204:207], v[92:95], v[164:167], v[204:207]\n"
".Lc_g164:\n"
"  ds_read_b64_tr_b16 v[156:157], v251 offset:37376\n"
"  ds_read_b64_tr_b16 v[158:159], v251 offset:39424\n"
"  ds_read_b64_tr_b16 v[160:161], v250 offset:40960\n"
"  ds_read_b64_tr_b16 v[162:163], v250 offset:43008\n"
"  s_waitcnt lgkmcnt(8)\n"
"  v_mfma_f32_16x16x32_bf16 v[208:211], v[144:147], v[48:51], v[208:211]\n"
"  ds_read_b64_tr_b16 v[164:165], v251 offset:40960\n"
"  v_mfma_f32_16x16x32_bf16 v[212:215], v[144:147], v[52:55], v[212:215]\n"
"  ds_read_b64_tr_b16 v[166:167], v251 offset:43008\n"
"  s_waitcnt lgkmcnt(8)\n"
"  v_mfma_f32_16x16x32_bf16 v[216:219], v[148:151], v[48:51], v[216:219]\n"
"  ds_read_b64_tr_b16 v[144:145], v250 offset:41472\n"
"  v_mfma_f32_16x16x32_bf16 v[220:223], v[148:151], v[52:55], v[220:223]\n"
"  ds_read_b64_tr_b16 v[146:147], v250 offset:43520\n"
"  s_waitcnt lgkmcnt(8)\n"
"  v_mfma_f32_16x16x32_bf16 v[224:227], v[152:155], v[48:51], v[224:227]\n"
"  ds_read_b64_tr_b16 v[148:149], v251 offset:41472\n"
"  v_mfma_f32_16x16x32_bf16 v[228:231], v[152:155], v[52:55], v[228:231]\n"
"  ds_read_b64_tr_b16 v[150:151], v251 offset:43520\n"
"  s_waitcnt lgkmcnt(6)\n"
"  v_mfma_f32_16x16x32_bf16 v[232:235], v[156:159], v[48:51], v[232:235]\n"
"  ds_read_b64_tr_b16 v[152:153], v250 offset:45056\n"
"  v_mfma_f32_16x16x32_bf16 v[236:239], v[156:159], v[52:55], v[236:239]\n"
"  ds_read_b64_tr_b16 v[154:155], v250 offset:47104\n"
"  v_mfma_f32_16x16x32_bf16 v[208:211], v[160:163], v[56:59], v[208:211]\n"
"  ds_read_b64_tr_b16 v[156:157], v251 offset:45056\n"
"  v_mfma_f32_16x16x32_bf16 v[212:215], v[160:163], v[60:63], v[212:215]\n"
"  ds_read_b64_tr_b16 v[158:159], v251 offset:47104\n"
"  s_waitcnt lgkmcnt(6)\n"
"  v_mfma_f32_16x16x32_bf16 v[216:219], v[164:167], v[56:59], v[216:219]\n"
"  ds_read_b64_tr_b16 v[160:161], v250 offset:45568\n"
"  v_mfma_f32_16x16x32_bf16 v[220:223], v[164:167], v[60:63], v[220:223]\n"
"  ds_read_b64_tr_b16 v[162:163], v250 offset:47616\n"
"  v_mfma_f32_16x16x32_bf16 v[224:227], v[144:147], v[56:59], v[224:227]\n"
"  ds_read_b64_tr_b16 v[164:165], v251 offset:45568\n"
"  v_mfma_f32_16x16x32_bf16 v[228:231], v[144:147], v[60:63], v[228:231]\n"
"  ds_read_b64_tr_b16 v[166:167], v251 offset:47616\n"
"  s_waitcnt lgkmcnt(6)\n"
"  v_mfma_f32_16x16x32_bf16 v[232:235], v[148:151], v[56:59], v[232:235]\n"
"  ds_read_b64_tr_b16 v[144:145], v250 offset:49152\n"
"  v_mfma_f32_16x16x32_bf16 v[236:239], v[148:151], v[60:63], v[236:239]\n"
"  ds_read_b64_tr_b16 v[146:147], v250 offset:51200\n"
"  v_mfma_f32_16x16x32_bf16 v[208:211], v[152:155], v[64:67], v[208:211]\n"
"  ds_read_b64_tr_b16 v[148:149], v251 offset:49152\n"
"  v_mfma_f32_16x16x32_bf16 v[212:215], v[152:155], v[68:71], v[212:215]\n"
"  ds_read_b64_tr_b16 v[150:151], v251 offset:51200\n"
"  s_waitcnt lgkmcnt(6)\n"
"  v_mfma_f32_16x16x32_bf16 v[216:219], v[156:159], v[64:67], v[216:219]\n"
"  ds_read_b64_tr_b16 v[152:153], v250 offset:49664\n"
"  v_mfma_f32_16x16x32_bf16 v[220:223], v[156:159], v[68:71], v[220:223]\n"
"  ds_read_b64_tr_b16 v[154:155], v250 offset:51712\n"
"  v_mfma_f32_16x16x32_bf16 v[224:227], v[160:163], v[64:67], v[224:227]\n"
"  ds_read_b64_tr_b16 v[156:157], v251 offset:49664\n"
"  v_mfma_f32_16x16x32_bf16 v[228:231], v[160:163], v[68:71], v[228:231]\n"
"  ds_read_b64_tr_b16 v[158:159], v251 offset:51712\n"
"  s_waitcnt lgkmcnt(6)\n"
"  v_mfma_f32_16x16x32_bf16 v[232:235], v[164:167], v[64:67], v[232:235]\n"
"  ds_read_b64_tr_b16 v[160:161], v250 offset:53248\n"
"  v_mfma_f32_16x16x32_bf16 v[236:239], v[164:167], v[68:71], v[236:239]\n"
"  ds_read_b64_tr_b16 v[162:163], v250 offset:55296\n"
"  v_mfma_f32_16x16x32_bf16 v[208:211], v[144:147], v[72:75], v[208:211]\n"
"  ds_read_b64_tr_b16 v[164:165], v251 offset:53248\n"
"  v_mfma_f32_16x16x32_bf16 v[212:215], v[144:147], v[76:79], v[212:215]\n"
"  ds_read_b64_tr_b16 v[166:167], v251 offset:55296\n"
"  s_waitcnt lgkmcnt(6)\n"
"  v_mfma_f32_16x16x32_bf16 v[216:219], v[148:151], v[72:75], v[216:219]\n"
"  ds_read_b64_tr_b16 v[144:145], v250 offset:53760\n"
"  v_mfma_f32_16x16x32_bf16 v[220:223], v[148:151], v[76:79], v[220:223]\n"
"  ds_read_b64_tr_b16 v[146:147], v250 offset:55808\n"
"  v_mfma_f32_16x16x32_bf16 v[224:227], v[152:155], v[72:75], v[224:227]\n"
"  ds_read_b64_tr_b16 v[148:149], v251 offset:53760\n"
"  v_mfma_f32_16x16x32_bf16 v[228:231], v[152:155], v[76:79], v[228:231]\n"
"  ds_read_b64_tr_b16 v[150:151], v251 offset:55808\n"
"  s_waitcnt lgkmcnt(6)\n"
"  v_mfma_f32_16x16x32_bf16 v[232:235], v[156:159], v[72:75], v[232:235]\n"
"  ds_read_b32 v253, v252\n"
"  ds_read_b64_tr_b16 v[152:153], v250 offset:57344\n"
"  v_mfma_f32_16x16x32_bf16 v[236:239], v[156:159], v[76:79], v[236:239]\n"
"  ds_read_b64_tr_b16 v[154:155], v250 offset:59392\n"
"  v_mfma_f32_16x16x32_bf16 v[208:211], v[160:163], v[80:83], v[208:211]\n"
"  ds_read_b64_tr_b16 v[156:157], v251 offset:57344\n"
"  v_mfma_f32_16x16x32_bf16 v[212:215], v[160:163], v[84:87], v[212:215]\n"
"  ds_read_b64_tr_b16 v[158:159], v251 offset:59392\n"
"  s_waitcnt lgkmcnt(7)\n"
"  v_mfma_f32_16x16x32_bf16 v[216:219], v[164:167], v[80:83], v[216:219]\n"
"  ds_read_b64_tr_b16 v[160:161], v250 offset:57856\n"
"  v_mfma_f32_16x16x32_bf16 v[220:223], v[164:167], v[84:87], v[220:223]\n"
"  ds_read_b64_tr_b16 v[162:163], v250 offset:59904\n"
"  v_mfma_f32_16x16x32_bf16 v[224:227], v[144:147], v[80:83], v[224:227]\n"
"  s_waitcnt lgkmcnt(5)\n"
"  v_cmp_gt_u32 vcc, 9, v253\n"
"  ds_read_b64_tr_b16 v[164:165], v251 offset:57856\n"
"  v_mfma_f32_16x16x32_bf16 v[228:231], v[144:147], v[84:87], v[228:231]\n"
"  ds_read_b64_tr_b16 v[166:167], v251 offset:59904\n"
"  ds_inc_u32 v252, v252 offset:1024\n"
"  s_cbranch_vccnz .Lc_sp8\n"
".Lc_bk8:\n"
"  v_mfma_f32_16x16x32_bf16 v[232:235], v[148:151], v[80:83], v[232:235]\n"
"  ds_read_b128 v[96:99], v240 offset:0\n"
"  ds_read_b128 v[100:103], v240 offset:1024\n"
"  v_mfma_f32_16x16x32_bf16 v[236:239], v[148:151], v[84:87], v[236:239]\n"
"  ds_read_b128 v[144:147], v242 offset:0\n"
"  s_waitcnt lgkmcnt(8)\n"
"  v_mfma_f32_16x16x32_bf16 v[208:211], v[152:155], v[88:91], v[208:211]\n"
"  ds_read_b128 v[148:151], v242 offset:2048\n"
"  v_mfma_f32_16x16x32_bf16 v[212:215], v[152:155], v[92:95], v[212:215]\n"
"  v_mfma_f32_16x16x32_bf16 v[216:219], v[156:159], v[88:91], v[216:219]\n"
"  v_mfma_f32_16x16x32_bf16 v[220:223], v[156:159], v[92:95], v[220:223]\n"
"  ds_read_b128 v[152:155], v242 offset:4096\n"
"  s_waitcnt lgkmcnt(5)\n"
"  v_mfma_f32_16x16x32_bf16 v[224:227], v[160:163], v[88:91], v[224:227]\n"
"  ds_read_b128 v[156:159], v242 offset:6144\n"
"  v_mfma_f32_16x16x32_bf16 v[228:231], v[160:163], v[92:95], v[228:231]\n"
"  v_mfma_f32_16x16x32_bf16 v[232:235], v[164:167], v[88:91], v[232:235]\n"
"  ds_read_b128 v[104:107], v240 offset:2048\n"
"  ds_read_b128 v[108:111], v240 offset:3072\n"
"  v_mfma_f32_16x16x32_bf16 v[236:239], v[164:167], v[92:95], v[236:239]\n"
"  ds_read_b128 v[160:163], v242 offset:512\n"
"  s_waitcnt lgkmcnt(5)\n"
"  v_mfma_f32_16x16x32_bf16 v[168:171], v[96:99], v[144:147], v[168:171]\n"
"  ds_read_b128 v[164:167], v242 offset:2560\n"
"  v_mfma_f32_16x16x32_bf16 v[172:175], v[100:103], v[144:147], v[172:175]\n"
"  v_mfma_f32_16x16x32_bf16 v[176:179], v[96:99], v[148:151], v[176:179]\n"
"  ds_read_b32 v253, v252\n"
"  v_mfma_f32_16x16x32_bf16 v[180:183], v[100:103], v[148:151], v[180:183]\n"
"  ds_read_b128 v[144:147], v242 offset:4608\n"
"  s_waitcnt lgkmcnt(6)\n"
"  v_mfma_f32_16x16x32_bf16 v[184:187], v[96:99], v[152:155], v[184:187]\n"
"  ds_read_b128 v[148:151], v242 offset:6656\n"
"  v_mfma_f32_16x16x32_bf16 v[188:191], v[100:103], v[152:155], v[188:191]\n"
"  v_mfma_f32_16x16x32_bf16 v[192:195], v[96:99], v[156:159], v[192:195]\n"
"  v_mfma_f32_16x16x32_bf16 v[196:199], v[100:103], v[156:159], v[196:199]\n"
"  ds_read_b128 v[152:155], v244 offset:26624\n"
"  s_waitcnt lgkmcnt(4)\n"
"  v_mfma_f32_16x16x32_bf16 v[168:171], v[104:107], v[160:163], v[168:171]\n"
"  s_waitcnt lgkmcnt(3)\n"
"  v_cmp_gt_u32 vcc, 10, v253\n"
"  ds_read_b128 v[156:159], v244 offset:27136\n"
"  v_mfma_f32_16x16x32_bf16 v[172:175], v[108:111], v[160:163], v[172:175]\n"
"  ds_inc_u32 v252, v252 offset:1024\n"
"  s_cbranch_vccnz .Lc_sp9\n"
".Lc_bk9:\n"
"  v_mfma_f32_16x16x32_bf16 v[176:179], v[104:107], v[164:167], v[176:179]\n"
"  ds_read_b128 v[112:115], v240 offset:24576\n"
"  ds_read_b128 v[116:119], v240 offset:25600\n"
"  v_mfma_f32_16x16x32_bf16 v[180:183], v[108:111], v[164:167], v[180:183]\n"
"  ds_read_b128 v[160:163], v242 offset:24576\n"
"  s_waitcnt lgkmcnt(6)\n"
"  v_mfma_f32_16x16x32_bf16 v[184:187], v[104:107], v[144:147], v[184:187]\n"
"  ds_read_b128 v[164:167], v242 offset:26624\n"
"  v_mfma_f32_16x16x32_bf16 v[188:191], v[108:111], v[144:147], v[188:191]\n"
"  v_mfma_f32_16x16x32_bf16 v[192:195], v[104:107], v[148:151], v[192:195]\n"
"  v_mfma_f32_16x16x32_bf16 v[196:199], v[108:111], v[148:151], v[196:199]\n"
"  ds_read_b128 v[144:147], v242 offset:28672\n"
"  s_cmp_eq_u32 s46, 0\n"
"  s_waitcnt lgkmcnt(5)\n"
"  s_cbranch_scc1 .Lc_g232\n"
"  v_mfma_f32_16x16x32_bf16 v[200:203], v[96:99], v[152:155], v[200:203]\n"
"  v_mfma_f32_16x16x32_bf16 v[204:207], v[100:103], v[152:155], v[204:207]\n"
"  v_mfma_f32_16x16x32_bf16 v[200:203], v[104:107], v[156:159], v[200:203]\n"
"  v_mfma_f32_16x16x32_bf16 v[204:207], v[108:111], v[156:159], v[204:207]\n"
".Lc_g232:\n"
"  ds_read_b128 v[148:151], v242 offset:30720\n"
"  ds_read_b128 v[120:123], v240 offset:26624\n"
"  ds_read_b128 v[124:127], v240 offset:27648\n"
"  ds_read_b128 v[152:155], v242 offset:25088\n"
"  s_waitcnt lgkmcnt(6)\n"
"  v_mfma_f32_16x16x32_bf16 v[168:171], v[112:115], v[160:163], v[168:171]\n"
"  ds_read_b128 v[156:159], v242 offset:27136\n"
"  v_mfma_f32_16x16x32_bf16 v[172:175], v[116:119], v[160:163], v[172:175]\n"
"  s_waitcnt lgkmcnt(6)\n"
"  v_mfma_f32_16x16x32_bf16 v[176:179], v[112:115], v[164:167], v[176:179]\n"
"  ds_read_b32 v253, v252\n"
"  v_mfma_f32_16x16x32_bf16 v[180:183], v[116:119], v[164:167], v[180:183]\n"
"  ds_read_b128 v[160:163], v242 offset:29184\n"
"  s_waitcnt lgkmcnt(7)\n"
"  v_mfma_f32_16x16x32_bf16 v[184:187], v[112:115], v[144:147], v[184:187]\n"
"  ds_read_b128 v[164:167], v242 offset:31232\n"
"  v_mfma_f32_16x16x32_bf16 v[188:191], v[116:119], v[144:147], v[188:191]\n"
"  s_waitcnt lgkmcnt(4)\n"
"  v_mfma_f32_16x16x32_bf16 v[192:195], v[112:115], v[148:151], v[192:195]\n"
"  v_mfma_f32_16x16x32_bf16 v[196:199], v[116:119], v[148:151], v[196:199]\n"
"  ds_read_b128 v[144:147], v244 offset:28672\n"
"  v_mfma_f32_16x16x32_bf16 v[168:171], v[120:123], v[152:155], v[168:171]\n"
"  s_waitcnt lgkmcnt(3)\n"
"  v_cmp_gt_u32 vcc, 11, v253\n"
"  ds_read_b128 v[148:151], v244 offset:29184\n"
"  v_mfma_f32_16x16x32_bf16 v[172:175], v[124:127], v[152:155], v[172:175]\n"
"  ds_inc_u32 v252, v252 offset:1024\n"
"  s_cbranch_vccnz .Lc_sp10\n"
".Lc_bk10:\n"
"  v_mfma_f32_16x16x32_bf16 v[176:179], v[120:123], v[156:159], v[176:179]\n"
"  ds_read_b128 v[128:131], v240 offset:49152\n"
"  ds_read_b128 v[132:135], v240 offset:50176\n"
"  v_mfma_f32_16x16x32_bf16 v[180:183], v[124:127], v[156:159], v[180:183]\n"
"  ds_read_b128 v[152:155], v242 offset:49152\n"
"  s_waitcnt lgkmcnt(6)\n"
"  v_mfma_f32_16x16x32_bf16 v[184:187], v[120:123], v[160:163], v[184:187]\n"
"  ds_read_b128 v[156:159], v242 offset:51200\n"
"  v_mfma_f32_16x16x32_bf16 v[188:191], v[124:127], v[160:163], v[188:191]\n"
"  v_mfma_f32_16x16x32_bf16 v[192:195], v[120:123], v[164:167], v[192:195]\n"
"  v_mfma_f32_16x16x32_bf16 v[196:199], v[124:127], v[164:167], v[196:199]\n"
"  ds_read_b128 v[160:163], v242 offset:53248\n"
"  s_cmp_eq_u32 s46, 0\n"
"  s_waitcnt lgkmcnt(5)\n"
"  s_cbranch_scc1 .Lc_g252\n"
"  v_mfma_f32_16x16x32_bf16 v[200:203], v[112:115], v[144:147], v[200:203]\n"
"  v_mfma_f32_16x16x32_bf16 v[204:207], v[116:119], v[144:147], v[204:207]\n"
"  v_mfma_f32_16x16x32_bf16 v[200:203], v[120:123], v[148:151], v[200:203]\n"
"  v_mfma_f32_16x16x32_bf16 v[204:207], v[124:127], v[148:151], v[204:207]\n"
".Lc_g252:\n"
"  ds_read_b128 v[164:167], v242 offset:55296\n"
"  ds_read_b128 v[136:139], v240 offset:51200\n"
"  ds_read_b128 v[140:143], v240 offset:52224\n"
"  ds_read_b128 v[144:147], v242 offset:49664\n"
"  s_waitcnt lgkmcnt(6)\n"
"  v_mfma_f32_16x16x32_bf16 v[168:171], v[128:131], v[152:155], v[168:171]\n"
"  ds_read_b128 v[148:151], v242 offset:51712\n"
"  v_mfma_f32_16x16x32_bf16 v[172:175], v[132:135], v[152:155], v[172:175]\n"
"  s_waitcnt lgkmcnt(6)\n"
"  v_mfma_f32_16x16x32_bf16 v[176:179], v[128:131], v[156:159], v[176:179]\n"
"  ds_read_b32 v253, v252\n"
"  v_mfma_f32_16x16x32_bf16 v[180:183], v[132:135], v[156:159], v[180:183]\n"
"  ds_read_b128 v[152:155], v242 offset:53760\n"
"  s_waitcnt lgkmcnt(7)\n"
"  v_mfma_f32_16x16x32_bf16 v[184:187], v[128:131], v[160:163], v[184:187]\n"
"  ds_read_b128 v[156:159], v242 offset:55808\n"
"  v_mfma_f32_16x16x32_bf16 v[188:191], v[132:135], v[160:163], v[188:191]\n"
"  s_waitcnt lgkmcnt(4)\n"
"  v_mfma_f32_16x16x32_bf16 v[192:195], v[128:131], v[164:167], v[192:195]\n"
"  v_mfma_f32_16x16x32_bf16 v[196:199], v[132:135], v[164:167], v[196:199]\n"
"  ds_read_b128 v[160:163], v244 offset:30720\n"
"  v_mfma_f32_16x16x32_bf16 v[168:171], v[136:139], v[144:147], v[168:171]\n"
"  s_waitcnt lgkmcnt(3)\n"
"  v_cmp_gt_u32 vcc, 12, v253\n"
"  ds_read_b128 v[164:167], v244 offset:31232\n"
"  v_mfma_f32_16x16x32_bf16 v[172:175], v[140:143], v[144:147], v[172:175]\n"
"  ds_inc_u32 v252, v252 offset:1024\n"
"  s_cbranch_vccnz .Lc_sp11\n"
".Lc_bk11:\n"
"  v_mfma_f32_16x16x32_bf16 v[176:179], v[136:139], v[148:151], v[176:179]\n"
"  ds_read_b64_tr_b16 v[144:145], v250 offset:12288\n"
"  v_mfma_f32_16x16x32_bf16 v[180:183], v[140:143], v[148:151], v[180:183]\n"
"  ds_read_b64_tr_b16 v[146:147], v250 offset:14336\n"
"  s_waitcnt lgkmcnt(5)\n"
"  v_mfma_f32_16x16x32_bf16 v[184:187], v[136:139], v[152:155], v[184:187]\n"
"  ds_read_b64_tr_b16 v[148:149], v251 offset:12288\n"
"  v_mfma_f32_16x16x32_bf16 v[188:191], v[140:143], v[152:155], v[188:191]\n"
"  ds_read_b64_tr_b16 v[150:151], v251 offset:14336\n"
"  v_mfma_f32_16x16x32_bf16 v[192:195], v[136:139], v[156:159], v[192:195]\n"
"  ds_read_b64_tr_b16 v[152:153], v250 offset:12800\n"
"  v_mfma_f32_16x16x32_bf16 v[196:199], v[140:143], v[156:159], v[196:199]\n"
"  ds_read_b64_tr_b16 v[154:155], v250 offset:14848\n"
"  s_cmp_eq_u32 s46, 0\n"
"  s_waitcnt lgkmcnt(6)\n"
"  s_cbranch_scc1 .Lc_g272\n"
"  v_mfma_f32_16x16x32_bf16 v[200:203], v[128:131], v[160:163], v[200:203]\n"
"  v_mfma_f32_16x16x32_bf16 v[204:207], v[132:135], v[160:163], v[204:207]\n"
"  v_mfma_f32_16x16x32_bf16 v[200:203], v[136:139], v[164:167], v[200:203]\n"
"  v_mfma_f32_16x16x32_bf16 v[204:207], v[140:143], v[164:167], v[204:207]\n"
".Lc_g272:\n"
"  ds_read_b64_tr_b16 v[156:157], v251 offset:12800\n"
"  ds_read_b64_tr_b16 v[158:159], v251 offset:14848\n"
"  ds_read_b64_tr_b16 v[160:161], v250 offset:16384\n"
"  ds_read_b64_tr_b16 v[162:163], v250 offset:18432\n"
"  s_waitcnt lgkmcnt(8)\n"
"  v_mfma_f32_16x16x32_bf16 v[208:211], v[144:147], v[96:99], v[208:211]\n"
"  ds_read_b64_tr_b16 v[164:165], v251 offset:16384\n"
"  v_mfma_f32_16x16x32_bf16 v[212:215], v[144:147], v[100:103], v[212:215]\n"
"  ds_read_b64_tr_b16 v[166:167], v251 offset:18432\n"
"  s_waitcnt lgkmcnt(8)\n"
"  v_mfma_f32_16x16x32_bf16 v[216:219], v[148:151], v[96:99], v[216:219]\n"
"  ds_read_b64_tr_b16 v[144:145], v250 offset:16896\n"
"  v_mfma_f32_16x16x32_bf16 v[220:223], v[148:151], v[100:103], v[220:223]\n"
"  ds_read_b64_tr_b16 v[146:147], v250 offset:18944\n"
"  v_cvt_pk_bf16_f32 v168, v168, v169\n"
"  v_cvt_pk_bf16_f32 v169, v170, v171\n"
"  s_waitcnt lgkmcnt(8)\n"
"  v_mfma_f32_16x16x32_bf16 v[224:227], v[152:155], v[96:99], v[224:227]\n"
"  ds_read_b64_tr_b16 v[148:149], v251 offset:16896\n"
"  v_xor_b32 v168, s51, v168\n"
"  v_mfma_f32_16x16x32_bf16 v[228:231], v[152:155], v[100:103], v[228:231]\n"
"  ds_read_b64_tr_b16 v[150:151], v251 offset:18944\n"
"  v_cvt_pk_bf16_f32 v170, v172, v173\n"
"  s_waitcnt lgkmcnt(6)\n"
"  v_mfma_f32_16x16x32_bf16 v[232:235], v[156:159], v[96:99], v[232:235]\n"
"  ds_read_b64_tr_b16 v[152:153], v250 offset:20480\n"
"  v_xor_b32 v169, s51, v169\n"
"  v_mfma_f32_16x16x32_bf16 v[236:239], v[156:159], v[100:103], v[236:239]\n"
"  ds_read_b64_tr_b16 v[154:155], v250 offset:22528\n"
"  v_cvt_pk_bf16_f32 v171, v174, v175\n"
"  v_mfma_f32_16x16x32_bf16 v[208:211], v[160:163], v[104:107], v[208:211]\n"
"  ds_read_b64_tr_b16 v[156:157], v251 offset:20480\n"
"  v_xor_b32 v170, s51, v170\n"
"  v_xor_b32 v171, s51, v171\n"
"  v_mfma_f32_16x16x32_bf16 v[212:215], v[160:163], v[108:111], v[212:215]\n"
"  ds_read_b64_tr_b16 v[158:159], v251 offset:22528\n"
"  buffer_store_dwordx4 v[168:171], v254, s[36:39], s44 offen offset:0 sc1\n"
"  s_waitcnt lgkmcnt(6)\n"
"  v_mfma_f32_16x16x32_bf16 v[216:219], v[164:167], v[104:107], v[216:219]\n"
"  ds_read_b64_tr_b16 v[160:161], v250 offset:20992\n"
"  v_cvt_pk_bf16_f32 v176, v176, v177\n"
"  v_mfma_f32_16x16x32_bf16 v[220:223], v[164:167], v[108:111], v[220:223]\n"
"  ds_read_b64_tr_b16 v[162:163], v250 offset:23040\n"
"  v_cvt_pk_bf16_f32 v177, v178, v179\n"
"  v_mfma_f32_16x16x32_bf16 v[224:227], v[144:147], v[104:107], v[224:227]\n"
"  ds_read_b64_tr_b16 v[164:165], v251 offset:20992\n"
"  v_xor_b32 v176, s51, v176\n"
"  v_mfma_f32_16x16x32_bf16 v[228:231], v[144:147], v[108:111], v[228:231]\n"
"  ds_read_b64_tr_b16 v[166:167], v251 offset:23040\n"
"  v_cvt_pk_bf16_f32 v178, v180, v181\n"
"  s_waitcnt lgkmcnt(6)\n"
"  v_mfma_f32_16x16x32_bf16 v[232:235], v[148:151], v[104:107], v[232:235]\n"
"  ds_read_b64_tr_b16 v[144:145], v250 offset:24576\n"
"  v_xor_b32 v177, s51, v177\n"
"  v_cvt_pk_bf16_f32 v179, v182, v183\n"
"  v_mfma_f32_16x16x32_bf16 v[236:239], v[148:151], v[108:111], v[236:239]\n"
"  ds_read_b64_tr_b16 v[146:147], v250 offset:26624\n"
"  v_xor_b32 v178, s51, v178\n"
"  v_mfma_f32_16x16x32_bf16 v[208:211], v[152:155], v[112:115], v[208:211]\n"
"  ds_read_b64_tr_b16 v[148:149], v251 offset:24576\n"
"  v_xor_b32 v179, s51, v179\n"
"  v_mfma_f32_16x16x32_bf16 v[212:215], v[152:155], v[116:119], v[212:215]\n"
"  ds_read_b64_tr_b16 v[150:151], v251 offset:26624\n"
"  buffer_store_dwordx4 v[176:179], v254, s[36:39], s44 offen offset:1024 sc1\n"
"  s_waitcnt lgkmcnt(6)\n"
"  v_mfma_f32_16x16x32_bf16 v[216:219], v[156:159], v[112:115], v[216:219]\n"
"  ds_read_b64_tr_b16 v[152:153], v250 offset:25088\n"
"  v_cvt_pk_bf16_f32 v184, v184, v185\n"
"  v_mfma_f32_16x16x32_bf16 v[220:223], v[156:159], v[116:119], v[220:223]\n"
"  ds_read_b64_tr_b16 v[154:155], v250 offset:27136\n"
"  v_cvt_pk_bf16_f32 v185, v186, v187\n"
"  v_mfma_f32_16x16x32_bf16 v[224:227], v[160:163], v[112:115], v[224:227]\n"
"  ds_read_b64_tr_b16 v[156:157], v251 offset:25088\n"
"  v_xor_b32 v184, s51, v184\n"
"  v_cvt_pk_bf16_f32 v186, v188, v189\n"
"  v_mfma_f32_16x16x32_bf16 v[228:231], v[160:163], v[116:119], v[228:231]\n"
"  ds_read_b64_tr_b16 v[158:159], v251 offset:27136\n"
"  v_xor_b32 v185, s51, v185\n"
"  s_waitcnt lgkmcnt(6)\n"
"  v_mfma_f32_16x16x32_bf16 v[232:235], v[164:167], v[112:115], v[232:235]\n"
"  ds_read_b64_tr_b16 v[160:161], v250 offset:28672\n"
"  v_cvt_pk_bf16_f32 v187, v190, v191\n"
"  v_mfma_f32_16x16x32_bf16 v[236:239], v[164:167], v[116:119], v[236:239]\n"
"  ds_read_b64_tr_b16 v[162:163], v250 offset:30720\n"
"  v_xor_b32 v186, s51, v186\n"
"  v_mfma_f32_16x16x32_bf16 v[208:211], v[144:147], v[120:123], v[208:211]\n"
"  ds_read_b64_tr_b16 v[164:165], v251 offset:28672\n"
"  v_xor_b32 v187, s51, v187\n"
"  v_mfma_f32_16x16x32_bf16 v[212:215], v[144:147], v[124:127], v[212:215]\n"
"  ds_read_b64_tr_b16 v[166:167], v251 offset:30720\n"
"  buffer_store_dwordx4 v[184:187], v254, s[36:39], s44 offen offset:2048 sc1\n"
"  v_cvt_pk_bf16_f32 v192, v192, v193\n"
"  s_waitcnt lgkmcnt(6)\n"
"  v_mfma_f32_16x16x32_bf16 v[216:219], v[148:151], v[120:123], v[216:219]\n"
"  ds_read_b64_tr_b16 v[144:145], v250 offset:29184\n"
"  v_cvt_pk_bf16_f32 v193, v194, v195\n"
"  v_mfma_f32_16x16x32_bf16 v[220:223], v[148:151], v[124:127], v[220:223]\n"
"  ds_read_b64_tr_b16 v[146:147], v250 offset:31232\n"
"  v_xor_b32 v192, s51, v192\n"
"  v_mfma_f32_16x16x32_bf16 v[224:227], v[152:155], v[120:123], v[224:227]\n"
"  ds_read_b64_tr_b16 v[148:149], v251 offset:29184\n"
"  v_cvt_pk_bf16_f32 v194, v196, v197\n"
"  v_mfma_f32_16x16x32_bf16 v[228:231], v[152:155], v[124:127], v[228:231]\n"
"  ds_read_b64_tr_b16 v[150:151], v251 offset:31232\n"
"  v_xor_b32 v193, s51, v193\n"
"  s_waitcnt lgkmcnt(6)\n"
"  v_mfma_f32_16x16x32_bf16 v[232:235], v[156:159], v[120:123], v[232:235]\n"
"  ds_read_b32 v253, v252\n"
"  ds_read_b64_tr_b16 v[152:153], v250 offset:32768\n"
"  v_cvt_pk_bf16_f32 v195, v198, v199\n"
"  v_mfma_f32_16x16x32_bf16 v[236:239], v[156:159], v[124:127], v[236:239]\n"
"  ds_read_b64_tr_b16 v[154:155], v250 offset:34816\n"
"  v_xor_b32 v194, s51, v194\n"
"  v_xor_b32 v195, s51, v195\n"
"  v_mfma_f32_16x16x32_bf16 v[208:211], v[160:163], v[128:131], v[208:211]\n"
"  ds_read_b64_tr_b16 v[156:157], v251 offset:32768\n"
"  buffer_store_dwordx4 v[192:195], v254, s[36:39], s44 offen offset:3072 sc1\n"
"  v_mfma_f32_16x16x32_bf16 v[212:215], v[160:163], v[132:135], v[212:215]\n"
"  ds_read_b64_tr_b16 v[158:159], v251 offset:34816\n"
"  s_waitcnt lgkmcnt(7)\n"
"  v_mfma_f32_16x16x32_bf16 v[216:219], v[164:167], v[128:131], v[216:219]\n"
"  ds_read_b64_tr_b16 v[160:161], v250 offset:33280\n"
"  s_cmp_eq_u32 s46, 0\n"
"  s_cbranch_scc1 .Lc_g5\n"
"  v_cvt_pk_bf16_f32 v200, -v200, -v201\n"
"  v_cvt_pk_bf16_f32 v201, -v202, -v203\n"
"  v_cvt_pk_bf16_f32 v202, -v204, -v205\n"
"  v_cvt_pk_bf16_f32 v203, -v206, -v207\n"
"  buffer_store_dwordx4 v[200:203], v254, s[36:39], s45 offen sc1\n"
".Lc_g5:\n"
"  v_mfma_f32_16x16x32_bf16 v[220:223], v[164:167], v[132:135], v[220:223]\n"
"  ds_read_b64_tr_b16 v[162:163], v250 offset:35328\n"
"  v_mfma_f32_16x16x32_bf16 v[224:227], v[144:147], v[128:131], v[224:227]\n"
"  s_waitcnt lgkmcnt(5)\n"
"  v_cmp_gt_u32 vcc, 13, v253\n"
"  ds_read_b64_tr_b16 v[164:165], v251 offset:33280\n"
"  v_mfma_f32_16x16x32_bf16 v[228:231], v[144:147], v[132:135], v[228:231]\n"
"  ds_read_b64_tr_b16 v[166:167], v251 offset:35328\n"
"  ds_inc_u32 v252, v252 offset:1024\n"
"  s_cbranch_vccnz .Lc_sp12\n"
".Lc_bk12:\n"
"  v_mfma_f32_16x16x32_bf16 v[232:235], v[148:151], v[128:131], v[232:235]\n"
"  ds_read_b64_tr_b16 v[144:145], v250 offset:36864\n"
"  v_mfma_f32_16x16x32_bf16 v[236:239], v[148:151], v[132:135], v[236:239]\n"
"  ds_read_b64_tr_b16 v[146:147], v250 offset:38912\n"
"  s_waitcnt lgkmcnt(7)\n"
"  v_mfma_f32_16x16x32_bf16 v[208:211], v[152:155], v[136:139], v[208:211]\n"
"  ds_read_b64_tr_b16 v[148:149], v251 offset:36864\n"
"  v_mfma_f32_16x16x32_bf16 v[212:215], v[152:155], v[140:143], v[212:215]\n"
"  ds_read_b64_tr_b16 v[150:151], v251 offset:38912\n"
"  v_mfma_f32_16x16x32_bf16 v[216:219], v[156:159], v[136:139], v[216:219]\n"
"  ds_read_b64_tr_b16 v[152:153], v250 offset:37376\n"
"  v_mfma_f32_16x16x32_bf16 v[220:223], v[156:159], v[140:143], v[220:223]\n"
"  ds_read_b64_tr_b16 v[154:155], v250 offset:39424\n"
"  s_waitcnt lgkmcnt(6)\n"
"  v_mfma_f32_16x16x32_bf16 v[224:227], v[160:163], v[136:139], v[224:227]\n"
"  ds_read_b64_tr_b16 v[156:157], v251 offset:37376\n"
"  v_mfma_f32_16x16x32_bf16 v[228:231], v[160:163], v[140:143], v[228:231]\n"
"  ds_read_b64_tr_b16 v[158:159], v251 offset:39424\n"
"  v_mfma_f32_16x16x32_bf16 v[232:235], v[164:167], v[136:139], v[232:235]\n"
"  ds_read_b64_tr_b16 v[160:161], v250 offset:40960\n"
"  v_mfma_f32_16x16x32_bf16 v[236:239], v[164:167], v[140:143], v[236:239]\n"
"  ds_read_b64_tr_b16 v[162:163], v250 offset:43008\n"
"  s_waitcnt lgkmcnt(6)\n"
"  v_mfma_f32_16x16x32_bf16 v[168:171], v[144:147], v[0:3], 0\n"
"  ds_read_b64_tr_b16 v[164:165], v251 offset:40960\n"
"  v_mfma_f32_16x16x32_bf16 v[172:175], v[144:147], v[4:7], 0\n"
"  ds_read_b64_tr_b16 v[166:167], v251 offset:43008\n"
"  v_mfma_f32_16x16x32_bf16 v[176:179], v[148:151], v[0:3], 0\n"
"  ds_read_b64_tr_b16 v[144:145], v250 offset:41472\n"
"  v_mfma_f32_16x16x32_bf16 v[180:183], v[148:151], v[4:7], 0\n"
"  ds_read_b64_tr_b16 v[146:147], v250 offset:43520\n"
"  v_cvt_pk_bf16_f32 v208, v208, v209\n"
"  s_waitcnt lgkmcnt(6)\n"
"  v_mfma_f32_16x16x32_bf16 v[184:187], v[152:155], v[0:3], 0\n"
"  ds_read_b64_tr_b16 v[148:149], v251 offset:41472\n"
"  v_mfma_f32_16x16x32_bf16 v[188:191], v[152:155], v[4:7], 0\n"
"  ds_read_b64_tr_b16 v[150:151], v251 offset:43520\n"
"  v_cvt_pk_bf16_f32 v209, v210, v211\n"
"  v_mfma_f32_16x16x32_bf16 v[192:195], v[156:159], v[0:3], 0\n"
"  ds_read_b64_tr_b16 v[152:153], v250 offset:45056\n"
"  v_mfma_f32_16x16x32_bf16 v[196:199], v[156:159], v[4:7], 0\n"
"  ds_read_b64_tr_b16 v[154:155], v250 offset:47104\n"
"  v_pk_max_i16 v208, v208, 0\n"
"  s_waitcnt lgkmcnt(6)\n"
"  v_mfma_f32_16x16x32_bf16 v[168:171], v[160:163], v[8:11], v[168:171]\n"
"  ds_read_b64_tr_b16 v[156:157], v251 offset:45056\n"
"  v_mfma_f32_16x16x32_bf16 v[172:175], v[160:163], v[12:15], v[172:175]\n"
"  ds_read_b64_tr_b16 v[158:159], v251 offset:47104\n"
"  v_cvt_pk_bf16_f32 v210, v216, v217\n"
"  v_mfma_f32_16x16x32_bf16 v[176:179], v[164:167], v[8:11], v[176:179]\n"
"  ds_read_b64_tr_b16 v[160:161], v250 offset:45568\n"
"  v_mfma_f32_16x16x32_bf16 v[180:183], v[164:167], v[12:15], v[180:183]\n"
"  ds_read_b64_tr_b16 v[162:163], v250 offset:47616\n"
"  v_pk_max_i16 v209, v209, 0\n"
"  s_waitcnt lgkmcnt(6)\n"
"  v_mfma_f32_16x16x32_bf16 v[184:187], v[144:147], v[8:11], v[184:187]\n"
"  ds_read_b64_tr_b16 v[164:165], v251 offset:45568\n"
"  v_mfma_f32_16x16x32_bf16 v[188:191], v[144:147], v[12:15], v[188:191]\n"
"  ds_read_b64_tr_b16 v[166:167], v251 offset:47616\n"
"  v_cvt_pk_bf16_f32 v211, v218, v219\n"
"  v_mfma_f32_16x16x32_bf16 v[192:195], v[148:151], v[8:11], v[192:195]\n"
"  ds_read_b64_tr_b16 v[144:145], v250 offset:49152\n"
"  v_mfma_f32_16x16x32_bf16 v[196:199], v[148:151], v[12:15], v[196:199]\n"
"  ds_read_b64_tr_b16 v[146:147], v250 offset:51200\n"
"  v_pk_max_i16 v210, v210, 0\n"
"  s_waitcnt lgkmcnt(6)\n"
"  v_mfma_f32_16x16x32_bf16 v[168:171], v[152:155], v[16:19], v[168:171]\n"
"  ds_read_b64_tr_b16 v[148:149], v251 offset:49152\n"
"  v_mfma_f32_16x16x32_bf16 v[172:175], v[152:155], v[20:23], v[172:175]\n"
"  ds_read_b64_tr_b16 v[150:151], v251 offset:51200\n"
"  v_pk_max_i16 v211, v211, 0\n"
"  v_mfma_f32_16x16x32_bf16 v[176:179], v[156:159], v[16:19], v[176:179]\n"
"  ds_read_b64_tr_b16 v[152:153], v250 offset:49664\n"
"  v_mfma_f32_16x16x32_bf16 v[180:183], v[156:159], v[20:23], v[180:183]\n"
"  ds_read_b64_tr_b16 v[154:155], v250 offset:51712\n"
"  v_cvt_pk_bf16_f32 v212, v212, v213\n"
"  s_waitcnt lgkmcnt(6)\n"
"  v_mfma_f32_16x16x32_bf16 v[184:187], v[160:163], v[16:19], v[184:187]\n"
"  ds_read_b64_tr_b16 v[156:157], v251 offset:49664\n"
"  v_mfma_f32_16x16x32_bf16 v[188:191], v[160:163], v[20:23], v[188:191]\n"
"  ds_read_b64_tr_b16 v[158:159], v251 offset:51712\n"
"  v_cvt_pk_bf16_f32 v213, v214, v215\n"
"  v_mfma_f32_16x16x32_bf16 v[192:195], v[164:167], v[16:19], v[192:195]\n"
"  ds_read_b64_tr_b16 v[160:161], v250 offset:53248\n"
"  v_mfma_f32_16x16x32_bf16 v[196:199], v[164:167], v[20:23], v[196:199]\n"
"  ds_read_b64_tr_b16 v[162:163], v250 offset:55296\n"
"  v_pk_max_i16 v212, v212, 0\n"
"  s_waitcnt lgkmcnt(6)\n"
"  v_mfma_f32_16x16x32_bf16 v[168:171], v[144:147], v[24:27], v[168:171]\n"
"  ds_read_b64_tr_b16 v[164:165], v251 offset:53248\n"
"  v_mfma_f32_16x16x32_bf16 v[172:175], v[144:147], v[28:31], v[172:175]\n"
"  ds_read_b64_tr_b16 v[166:167], v251 offset:55296\n"
"  v_cvt_pk_bf16_f32 v214, v220, v221\n"
"  v_mfma_f32_16x16x32_bf16 v[176:179], v[148:151], v[24:27], v[176:179]\n"
"  ds_read_b64_tr_b16 v[144:145], v250 offset:53760\n"
"  v_mfma_f32_16x16x32_bf16 v[180:183], v[148:151], v[28:31], v[180:183]\n"
"  ds_read_b64_tr_b16 v[146:147], v250 offset:55808\n"
"  v_pk_max_i16 v213, v213, 0\n"
"  s_waitcnt lgkmcnt(6)\n"
"  v_mfma_f32_16x16x32_bf16 v[184:187], v[152:155], v[24:27], v[184:187]\n"
"  ds_read_b64_tr_b16 v[148:149], v251 offset:53760\n"
"  v_mfma_f32_16x16x32_bf16 v[188:191], v[152:155], v[28:31], v[188:191]\n"
"  ds_read_b64_tr_b16 v[150:151], v251 offset:55808\n"
"  v_cvt_pk_bf16_f32 v215, v222, v223\n"
"  v_mfma_f32_16x16x32_bf16 v[192:195], v[156:159], v[24:27], v[192:195]\n"
"  ds_read_b32 v253, v252\n"
"  ds_read_b64_tr_b16 v[152:153], v250 offset:57344\n"
"  v_mfma_f32_16x16x32_bf16 v[196:199], v[156:159], v[28:31], v[196:199]\n"
"  ds_read_b64_tr_b16 v[154:155], v250 offset:59392\n"
"  v_pk_max_i16 v214, v214, 0\n"
"  s_waitcnt lgkmcnt(7)\n"
"  v_mfma_f32_16x16x32_bf16 v[168:171], v[160:163], v[32:35], v[168:171]\n"
"  ds_read_b64_tr_b16 v[156:157], v251 offset:57344\n"
"  v_mfma_f32_16x16x32_bf16 v[172:175], v[160:163], v[36:39], v[172:175]\n"
"  ds_read_b64_tr_b16 v[158:159], v251 offset:59392\n"
"  v_pk_max_i16 v215, v215, 0\n"
"  v_mfma_f32_16x16x32_bf16 v[176:179], v[164:167], v[32:35], v[176:179]\n"
"  ds_read_b64_tr_b16 v[160:161], v250 offset:57856\n"
"  v_mfma_f32_16x16x32_bf16 v[180:183], v[164:167], v[36:39], v[180:183]\n"
"  ds_read_b64_tr_b16 v[162:163], v250 offset:59904\n"
"  v_cvt_pk_bf16_f32 v224, v224, v225\n"
"  s_waitcnt lgkmcnt(7)\n"
"  v_mfma_f32_16x16x32_bf16 v[184:187], v[144:147], v[32:35], v[184:187]\n"
"  s_waitcnt lgkmcnt(5)\n"
"  v_cmp_gt_u32 vcc, 14, v253\n"
"  ds_read_b64_tr_b16 v[164:165], v251 offset:57856\n"
"  v_mfma_f32_16x16x32_bf16 v[188:191], v[144:147], v[36:39], v[188:191]\n"
"  ds_read_b64_tr_b16 v[166:167], v251 offset:59904\n"
"  ds_inc_u32 v252, v252 offset:1024\n"
"  v_cvt_pk_bf16_f32 v225, v226, v227\n"
"  s_cbranch_vccnz .Lc_sp13\n"
".Lc_bk13:\n"
"  v_mfma_f32_16x16x32_bf16 v[192:195], v[148:151], v[32:35], v[192:195]\n"
"  ds_read_b64_tr_b16 v[144:145], v248 offset:0\n"
"  s_waitcnt vmcnt(0)\n"
"  ds_inc_rtn_u32 v246, v245, v252 offset:34816\n"
"  v_mfma_f32_16x16x32_bf16 v[196:199], v[148:151], v[36:39], v[196:199]\n"
"  ds_read_b64_tr_b16 v[146:147], v248 offset:2048\n"
"  v_pk_max_i16 v224, v224, 0\n"
"  s_waitcnt lgkmcnt(8)\n"
"  v_mfma_f32_16x16x32_bf16 v[168:171], v[152:155], v[40:43], v[168:171]\n"
"  ds_read_b64_tr_b16 v[148:149], v249 offset:0\n"
"  v_mfma_f32_16x16x32_bf16 v[172:175], v[152:155], v[44:47], v[172:175]\n"
"  ds_read_b64_tr_b16 v[150:151], v249 offset:2048\n"
"  v_cvt_pk_bf16_f32 v226, v232, v233\n"
"  v_mfma_f32_16x16x32_bf16 v[176:179], v[156:159], v[40:43], v[176:179]\n"
"  ds_read_b64_tr_b16 v[152:153], v248 offset:512\n"
"  v_lshlrev_b32 v247, 4, v254\n"
"  v_mfma_f32_16x16x32_bf16 v[180:183], v[156:159], v[44:47], v[180:183]\n"
"  ds_read_b64_tr_b16 v[154:155], v248 offset:2560\n"
"  v_pk_max_i16 v225, v225, 0\n"
"  s_waitcnt lgkmcnt(7)\n"
"  v_mfma_f32_16x16x32_bf16 v[184:187], v[160:163], v[40:43], v[184:187]\n"
"  ds_read_b64_tr_b16 v[156:157], v249 offset:512\n"
"  v_mfma_f32_16x16x32_bf16 v[188:191], v[160:163], v[44:47], v[188:191]\n"
"  ds_read_b64_tr_b16 v[158:159], v249 offset:2560\n"
"  v_cvt_pk_bf16_f32 v227, v234, v235\n"
"  v_mfma_f32_16x16x32_bf16 v[192:195], v[164:167], v[40:43], v[192:195]\n"
"  ds_read_b64_tr_b16 v[160:161], v248 offset:4096\n"
"  s_waitcnt lgkmcnt(6)\n"
"  v_lshl_add_u32 v247, v246, 30, v247\n"
"  v_mfma_f32_16x16x32_bf16 v[196:199], v[164:167], v[44:47], v[196:199]\n"
"  ds_read_b64_tr_b16 v[162:163], v248 offset:6144\n"
"  buffer_store_dword v246, v247, s[96:99], 0 offen sc1\n"
"  v_pk_max_i16 v226, v226, 0\n"
"  v_mfma_f32_16x16x32_bf16 v[168:171], v[144:147], v[48:51], v[168:171]\n"
"  ds_read_b64_tr_b16 v[164:165], v249 offset:4096\n"
"  v_mfma_f32_16x16x32_bf16 v[172:175], v[144:147], v[52:55], v[172:175]\n"
"  ds_read_b64_tr_b16 v[166:167], v249 offset:6144\n"
"  s_add_u32 s96, s96, 16384\n"
"  s_addc_u32 s97, s97, 0\n"
"  v_pk_max_i16 v227, v227, 0\n"
"  s_waitcnt lgkmcnt(6)\n"
"  v_mfma_f32_16x16x32_bf16 v[176:179], v[148:151], v[48:51], v[176:179]\n"
"  ds_read_b64_tr_b16 v[144:145], v248 offset:4608\n"
"  v_mfma_f32_16x16x32_bf16 v[180:183], v[148:151], v[52:55], v[180:183]\n"
"  ds_read_b64_tr_b16 v[146:147], v248 offset:6656\n"
"  v_cvt_pk_bf16_f32 v228, v228, v229\n"
"  v_mfma_f32_16x16x32_bf16 v[184:187], v[152:155], v[48:51], v[184:187]\n"
"  ds_read_b64_tr_b16 v[148:149], v249 offset:4608\n"
"  v_mfma_f32_16x16x32_bf16 v[188:191], v[152:155], v[52:55], v[188:191]\n"
"  ds_read_b64_tr_b16 v[150:151], v249 offset:6656\n"
"  v_cvt_pk_bf16_f32 v229, v230, v231\n"
"  s_waitcnt lgkmcnt(6)\n"
"  v_mfma_f32_16x16x32_bf16 v[192:195], v[156:159], v[48:51], v[192:195]\n"
"  ds_read_b64_tr_b16 v[152:153], v248 offset:8192\n"
"  v_mfma_f32_16x16x32_bf16 v[196:199], v[156:159], v[52:55], v[196:199]\n"
"  ds_read_b64_tr_b16 v[154:155], v248 offset:10240\n"
"  v_pk_max_i16 v228, v228, 0\n"
"  v_mfma_f32_16x16x32_bf16 v[168:171], v[160:163], v[56:59], v[168:171]\n"
"  ds_read_b64_tr_b16 v[156:157], v249 offset:8192\n"
"  v_mfma_f32_16x16x32_bf16 v[172:175], v[160:163], v[60:63], v[172:175]\n"
"  ds_read_b64_tr_b16 v[158:159], v249 offset:10240\n"
"  v_cvt_pk_bf16_f32 v230, v236, v237\n"
"  s_waitcnt lgkmcnt(6)\n"
"  v_mfma_f32_16x16x32_bf16 v[176:179], v[164:167], v[56:59], v[176:179]\n"
"  ds_read_b64_tr_b16 v[160:161], v248 offset:8704\n"
"  v_mfma_f32_16x16x32_bf16 v[180:183], v[164:167], v[60:63], v[180:183]\n"
"  ds_read_b64_tr_b16 v[162:163], v248 offset:10752\n"
"  v_pk_max_i16 v229, v229, 0\n"
"  v_mfma_f32_16x16x32_bf16 v[184:187], v[144:147], v[56:59], v[184:187]\n"
"  ds_read_b64_tr_b16 v[164:165], v249 offset:8704\n"
"  v_mfma_f32_16x16x32_bf16 v[188:191], v[144:147], v[60:63], v[188:191]\n"
"  ds_read_b64_tr_b16 v[166:167], v249 offset:10752\n"
"  v_cvt_pk_bf16_f32 v231, v238, v239\n"
"  s_waitcnt lgkmcnt(6)\n"
"  v_mfma_f32_16x16x32_bf16 v[192:195], v[148:151], v[56:59], v[192:195]\n"
"  ds_read_b64_tr_b16 v[144:145], v248 offset:12288\n"
"  v_mfma_f32_16x16x32_bf16 v[196:199], v[148:151], v[60:63], v[196:199]\n"
"  ds_read_b64_tr_b16 v[146:147], v248 offset:14336\n"
"  v_pk_max_i16 v230, v230, 0\n"
"  v_mfma_f32_16x16x32_bf16 v[168:171], v[152:155], v[64:67], v[168:171]\n"
"  ds_read_b64_tr_b16 v[148:149], v249 offset:12288\n"
"  v_mfma_f32_16x16x32_bf16 v[172:175], v[152:155], v[68:71], v[172:175]\n"
"  ds_read_b64_tr_b16 v[150:151], v249 offset:14336\n"
"  v_pk_max_i16 v231, v231, 0\n"
"  s_waitcnt lgkmcnt(6)\n"
"  v_mfma_f32_16x16x32_bf16 v[176:179], v[156:159], v[64:67], v[176:179]\n"
"  ds_read_b64_tr_b16 v[152:153], v248 offset:12800\n"
"  v_mfma_f32_16x16x32_bf16 v[180:183], v[156:159], v[68:71], v[180:183]\n"
"  ds_read_b64_tr_b16 v[154:155], v248 offset:14848\n"
"  v_mfma_f32_16x16x32_bf16 v[184:187], v[160:163], v[64:67], v[184:187]\n"
"  ds_read_b64_tr_b16 v[156:157], v249 offset:12800\n"
"  v_mfma_f32_16x16x32_bf16 v[188:191], v[160:163], v[68:71], v[188:191]\n"
"  ds_read_b64_tr_b16 v[158:159], v249 offset:14848\n"
"  s_waitcnt lgkmcnt(6)\n"
"  v_mfma_f32_16x16x32_bf16 v[192:195], v[164:167], v[64:67], v[192:195]\n"
"  ds_read_b64_tr_b16 v[160:161], v248 offset:16384\n"
"  v_mfma_f32_16x16x32_bf16 v[196:199], v[164:167], v[68:71], v[196:199]\n"
"  ds_read_b64_tr_b16 v[162:163], v248 offset:18432\n"
"  v_mfma_f32_16x16x32_bf16 v[168:171], v[144:147], v[72:75], v[168:171]\n"
"  ds_read_b64_tr_b16 v[164:165], v249 offset:16384\n"
"  v_mfma_f32_16x16x32_bf16 v[172:175], v[144:147], v[76:79], v[172:175]\n"
"  ds_read_b64_tr_b16 v[166:167], v249 offset:18432\n"
"  s_waitcnt lgkmcnt(6)\n"
"  v_mfma_f32_16x16x32_bf16 v[176:179], v[148:151], v[72:75], v[176:179]\n"
"  ds_read_b64_tr_b16 v[144:145], v248 offset:16896\n"
"  v_mfma_f32_16x16x32_bf16 v[180:183], v[148:151], v[76:79], v[180:183]\n"
"  ds_read_b64_tr_b16 v[146:147], v248 offset:18944\n"
"  v_mfma_f32_16x16x32_bf16 v[184:187], v[152:155], v[72:75], v[184:187]\n"
"  ds_read_b64_tr_b16 v[148:149], v249 offset:16896\n"
"  v_mfma_f32_16x16x32_bf16 v[188:191], v[152:155], v[76:79], v[188:191]\n"
"  ds_read_b64_tr_b16 v[150:151], v249 offset:18944\n"
"  s_waitcnt lgkmcnt(6)\n"
"  v_mfma_f32_16x16x32_bf16 v[192:195], v[156:159], v[72:75], v[192:195]\n"
"  ds_read_b32 v253, v252\n"
"  ds_read_b64_tr_b16 v[152:153], v248 offset:20480\n"
"  v_mfma_f32_16x16x32_bf16 v[196:199], v[156:159], v[76:79], v[196:199]\n"
"  ds_read_b64_tr_b16 v[154:155], v248 offset:22528\n"
"  v_mfma_f32_16x16x32_bf16 v[168:171], v[160:163], v[80:83], v[168:171]\n"
"  ds_read_b64_tr_b16 v[156:157], v249 offset:20480\n"
"  v_mfma_f32_16x16x32_bf16 v[172:175], v[160:163], v[84:87], v[172:175]\n"
"  ds_read_b64_tr_b16 v[158:159], v249 offset:22528\n"
"  s_waitcnt lgkmcnt(7)\n"
"  v_mfma_f32_16x16x32_bf16 v[176:179], v[164:167], v[80:83], v[176:179]\n"
"  ds_read_b64_tr_b16 v[160:161], v248 offset:20992\n"
"  v_mfma_f32_16x16x32_bf16 v[180:183], v[164:167], v[84:87], v[180:183]\n"
"  ds_read_b64_tr_b16 v[162:163], v248 offset:23040\n"
"  v_mfma_f32_16x16x32_bf16 v[184:187], v[144:147], v[80:83], v[184:187]\n"
"  s_waitcnt lgkmcnt(5)\n"
"  v_cmp_gt_u32 vcc, 15, v253\n"
"  ds_read_b64_tr_b16 v[164:165], v249 offset:20992\n"
"  v_mfma_f32_16x16x32_bf16 v[188:191], v[144:147], v[84:87], v[188:191]\n"
"  ds_read_b64_tr_b16 v[166:167], v249 offset:23040\n"
"  ds_inc_u32 v252, v252 offset:1024\n"
"  s_cbranch_vccnz .Lc_sp14\n"
".Lc_bk14:\n"
"  v_mfma_f32_16x16x32_bf16 v[192:195], v[148:151], v[80:83], v[192:195]\n"
"  ds_read_b64_tr_b16 v[144:145], v248 offset:24576\n"
"  v_mfma_f32_16x16x32_bf16 v[196:199], v[148:151], v[84:87], v[196:199]\n"
"  ds_read_b64_tr_b16 v[146:147], v248 offset:26624\n"
"  s_waitcnt lgkmcnt(7)\n"
"  v_mfma_f32_16x16x32_bf16 v[168:171], v[152:155], v[88:91], v[168:171]\n"
"  ds_read_b64_tr_b16 v[148:149], v249 offset:24576\n"
"  v_mfma_f32_16x16x32_bf16 v[172:175], v[152:155], v[92:95], v[172:175]\n"
"  ds_read_b64_tr_b16 v[150:151], v249 offset:26624\n"
"  v_mfma_f32_16x16x32_bf16 v[176:179], v[156:159], v[88:91], v[176:179]\n"
"  ds_read_b64_tr_b16 v[152:153], v248 offset:25088\n"
"  v_mfma_f32_16x16x32_bf16 v[180:183], v[156:159], v[92:95], v[180:183]\n"
"  ds_read_b64_tr_b16 v[154:155], v248 offset:27136\n"
"  s_waitcnt lgkmcnt(6)\n"
"  v_mfma_f32_16x16x32_bf16 v[184:187], v[160:163], v[88:91], v[184:187]\n"
"  ds_read_b64_tr_b16 v[156:157], v249 offset:25088\n"
"  v_mfma_f32_16x16x32_bf16 v[188:191], v[160:163], v[92:95], v[188:191]\n"
"  ds_read_b64_tr_b16 v[158:159], v249 offset:27136\n"
"  v_mfma_f32_16x16x32_bf16 v[192:195], v[164:167], v[88:91], v[192:195]\n"
"  ds_read_b64_tr_b16 v[160:161], v248 offset:28672\n"
"  v_mfma_f32_16x16x32_bf16 v[196:199], v[164:167], v[92:95], v[196:199]\n"
"  ds_read_b64_tr_b16 v[162:163], v248 offset:30720\n"
"  s_waitcnt lgkmcnt(6)\n"
"  v_mfma_f32_16x16x32_bf16 v[168:171], v[144:147], v[96:99], v[168:171]\n"
"  ds_read_b64_tr_b16 v[164:165], v249 offset:28672\n"
"  v_mfma_f32_16x16x32_bf16 v[172:175], v[144:147], v[100:103], v[172:175]\n"
"  ds_read_b64_tr_b16 v[166:167], v249 offset:30720\n"
"  v_mfma_f32_16x16x32_bf16 v[176:179], v[148:151], v[96:99], v[176:179]\n"
"  ds_read_b64_tr_b16 v[144:145], v248 offset:29184\n"
"  v_mfma_f32_16x16x32_bf16 v[180:183], v[148:151], v[100:103], v[180:183]\n"
"  ds_read_b64_tr_b16 v[146:147], v248 offset:31232\n"
"  s_waitcnt lgkmcnt(6)\n"
"  v_mfma_f32_16x16x32_bf16 v[184:187], v[152:155], v[96:99], v[184:187]\n"
"  ds_read_b64_tr_b16 v[148:149], v249 offset:29184\n"
"  v_mfma_f32_16x16x32_bf16 v[188:191], v[152:155], v[100:103], v[188:191]\n"
"  ds_read_b64_tr_b16 v[150:151], v249 offset:31232\n"
"  v_mfma_f32_16x16x32_bf16 v[192:195], v[156:159], v[96:99], v[192:195]\n"
"  ds_read_b64_tr_b16 v[152:153], v248 offset:32768\n"
"  v_mfma_f32_16x16x32_bf16 v[196:199], v[156:159], v[100:103], v[196:199]\n"
"  ds_read_b64_tr_b16 v[154:155], v248 offset:34816\n"
"  s_waitcnt lgkmcnt(6)\n"
"  v_mfma_f32_16x16x32_bf16 v[168:171], v[160:163], v[104:107], v[168:171]\n"
"  ds_read_b64_tr_b16 v[156:157], v249 offset:32768\n"
"  v_mfma_f32_16x16x32_bf16 v[172:175], v[160:163], v[108:111], v[172:175]\n"
"  ds_read_b64_tr_b16 v[158:159], v249 offset:34816\n"
"  v_mfma_f32_16x16x32_bf16 v[176:179], v[164:167], v[104:107], v[176:179]\n"
"  ds_read_b64_tr_b16 v[160:161], v248 offset:33280\n"
"  v_mfma_f32_16x16x32_bf16 v[180:183], v[164:167], v[108:111], v[180:183]\n"
"  ds_read_b64_tr_b16 v[162:163], v248 offset:35328\n"
"  s_waitcnt lgkmcnt(6)\n"
"  v_mfma_f32_16x16x32_bf16 v[184:187], v[144:147], v[104:107], v[184:187]\n"
"  ds_read_b64_tr_b16 v[164:165], v249 offset:33280\n"
"  v_mfma_f32_16x16x32_bf16 v[188:191], v[144:147], v[108:111], v[188:191]\n"
"  ds_read_b64_tr_b16 v[166:167], v249 offset:35328\n"
"  v_mfma_f32_16x16x32_bf16 v[192:195], v[148:151], v[104:107], v[192:195]\n"
"  ds_read_b64_tr_b16 v[144:145], v248 offset:36864\n"
"  v_mfma_f32_16x16x32_bf16 v[196:199], v[148:151], v[108:111], v[196:199]\n"
"  ds_read_b64_tr_b16 v[146:147], v248 offset:38912\n"
"  s_waitcnt lgkmcnt(6)\n"
"  v_mfma_f32_16x16x32_bf16 v[168:171], v[152:155], v[112:115], v[168:171]\n"
"  ds_read_b64_tr_b16 v[148:149], v249 offset:36864\n"
"  v_mfma_f32_16x16x32_bf16 v[172:175], v[152:155], v[116:119], v[172:175]\n"
"  ds_read_b64_tr_b16 v[150:151], v249 offset:38912\n"
"  v_mfma_f32_16x16x32_bf16 v[176:179], v[156:159], v[112:115], v[176:179]\n"
"  ds_read_b64_tr_b16 v[152:153], v248 offset:37376\n"
"  v_mfma_f32_16x16x32_bf16 v[180:183], v[156:159], v[116:119], v[180:183]\n"
"  ds_read_b64_tr_b16 v[154:155], v248 offset:39424\n"
"  s_waitcnt lgkmcnt(6)\n"
"  v_mfma_f32_16x16x32_bf16 v[184:187], v[160:163], v[112:115], v[184:187]\n"
"  ds_read_b64_tr_b16 v[156:157], v249 offset:37376\n"
"  v_mfma_f32_16x16x32_bf16 v[188:191], v[160:163], v[116:119], v[188:191]\n"
"  ds_read_b64_tr_b16 v[158:159], v249 offset:39424\n"
"  v_mfma_f32_16x16x32_bf16 v[192:195], v[164:167], v[112:115], v[192:195]\n"
"  ds_read_b64_tr_b16 v[160:161], v248 offset:40960\n"
"  v_mfma_f32_16x16x32_bf16 v[196:199], v[164:167], v[116:119], v[196:199]\n"
"  ds_read_b64_tr_b16 v[162:163], v248 offset:43008\n"
"  s_waitcnt lgkmcnt(6)\n"
"  v_mfma_f32_16x16x32_bf16 v[168:171], v[144:147], v[120:123], v[168:171]\n"
"  ds_read_b64_tr_b16 v[164:165], v249 offset:40960\n"
"  v_mfma_f32_16x16x32_bf16 v[172:175], v[144:147], v[124:127], v[172:175]\n"
"  ds_read_b64_tr_b16 v[166:167], v249 offset:43008\n"
"  v_mfma_f32_16x16x32_bf16 v[176:179], v[148:151], v[120:123], v[176:179]\n"
"  ds_read_b64_tr_b16 v[144:145], v248 offset:41472\n"
"  v_mfma_f32_16x16x32_bf16 v[180:183], v[148:151], v[124:127], v[180:183]\n"
"  ds_read_b64_tr_b16 v[146:147], v248 offset:43520\n"
"  s_waitcnt lgkmcnt(6)\n"
"  v_mfma_f32_16x16x32_bf16 v[184:187], v[152:155], v[120:123], v[184:187]\n"
"  ds_read_b64_tr_b16 v[148:149], v249 offset:41472\n"
"  v_mfma_f32_16x16x32_bf16 v[188:191], v[152:155], v[124:127], v[188:191]\n"
"  ds_read_b64_tr_b16 v[150:151], v249 offset:43520\n"
"  v_mfma_f32_16x16x32_bf16 v[192:195], v[156:159], v[120:123], v[192:195]\n"
"  ds_read_b32 v253, v252\n"
"  ds_read_b64_tr_b16 v[152:153], v248 offset:45056\n"
"  v_mfma_f32_16x16x32_bf16 v[196:199], v[156:159], v[124:127], v[196:199]\n"
"  ds_read_b64_tr_b16 v[154:155], v248 offset:47104\n"
"  s_waitcnt lgkmcnt(7)\n"
"  v_mfma_f32_16x16x32_bf16 v[168:171], v[160:163], v[128:131], v[168:171]\n"
"  ds_read_b64_tr_b16 v[156:157], v249 offset:45056\n"
"  v_mfma_f32_16x16x32_bf16 v[172:175], v[160:163], v[132:135], v[172:175]\n"
"  ds_read_b64_tr_b16 v[158:159], v249 offset:47104\n"
"  v_mfma_f32_16x16x32_bf16 v[176:179], v[164:167], v[128:131], v[176:179]\n"
"  ds_read_b64_tr_b16 v[160:161], v248 offset:45568\n"
"  v_mfma_f32_16x16x32_bf16 v[180:183], v[164:167], v[132:135], v[180:183]\n"
"  ds_read_b64_tr_b16 v[162:163], v248 offset:47616\n"
"  s_waitcnt lgkmcnt(7)\n"
"  v_mfma_f32_16x16x32_bf16 v[184:187], v[144:147], v[128:131], v[184:187]\n"
"  s_waitcnt lgkmcnt(5)\n"
"  v_cmp_gt_u32 vcc, 16, v253\n"
"  ds_read_b64_tr_b16 v[164:165], v249 offset:45568\n"
"  v_mfma_f32_16x16x32_bf16 v[188:191], v[144:147], v[132:135], v[188:191]\n"
"  ds_read_b64_tr_b16 v[166:167], v249 offset:47616\n"
"  ds_inc_u32 v252, v252 offset:1024\n"
"  s_cbranch_vccnz .Lc_sp15\n"
".Lc_bk15:\n"
"  v_mfma_f32_16x16x32_bf16 v[192:195], v[148:151], v[128:131], v[192:195]\n"
"  ds_read_b64_tr_b16 v[144:145], v248 offset:49152\n"
"  v_mfma_f32_16x16x32_bf16 v[196:199], v[148:151], v[132:135], v[196:199]\n"
"  ds_read_b64_tr_b16 v[146:147], v248 offset:51200\n"
"  s_waitcnt lgkmcnt(7)\n"
"  v_mfma_f32_16x16x32_bf16 v[168:171], v[152:155], v[136:139], v[168:171]\n"
"  ds_read_b64_tr_b16 v[148:149], v249 offset:49152\n"
"  v_mfma_f32_16x16x32_bf16 v[172:175], v[152:155], v[140:143], v[172:175]\n"
"  ds_read_b64_tr_b16 v[150:151], v249 offset:51200\n"
"  v_mfma_f32_16x16x32_bf16 v[176:179], v[156:159], v[136:139], v[176:179]\n"
"  ds_read_b64_tr_b16 v[152:153], v248 offset:49664\n"
"  v_mfma_f32_16x16x32_bf16 v[180:183], v[156:159], v[140:143], v[180:183]\n"
"  ds_read_b64_tr_b16 v[154:155], v248 offset:51712\n"
"  s_waitcnt lgkmcnt(6)\n"
"  v_mfma_f32_16x16x32_bf16 v[184:187], v[160:163], v[136:139], v[184:187]\n"
"  ds_read_b64_tr_b16 v[156:157], v249 offset:49664\n"
"  v_mfma_f32_16x16x32_bf16 v[188:191], v[160:163], v[140:143], v[188:191]\n"
"  ds_read_b64_tr_b16 v[158:159], v249 offset:51712\n"
"  v_mfma_f32_16x16x32_bf16 v[192:195], v[164:167], v[136:139], v[192:195]\n"
"  ds_read_b64_tr_b16 v[160:161], v248 offset:53248\n"
"  v_mfma_f32_16x16x32_bf16 v[196:199], v[164:167], v[140:143], v[196:199]\n"
"  ds_read_b64_tr_b16 v[162:163], v248 offset:55296\n"
"  s_waitcnt lgkmcnt(6)\n"
"  v_mfma_f32_16x16x32_bf16 v[200:203], v[144:147], v[0:3], 0\n"
"  ds_read_b64_tr_b16 v[164:165], v249 offset:53248\n"
"  v_mfma_f32_16x16x32_bf16 v[204:207], v[144:147], v[4:7], 0\n"
"  ds_read_b64_tr_b16 v[166:167], v249 offset:55296\n"
"  v_mfma_f32_16x16x32_bf16 v[240:243], v[148:151], v[0:3], 0\n"
"  ds_read_b64_tr_b16 v[144:145], v248 offset:53760\n"
"  v_mfma_f32_16x16x32_bf16 v[244:247], v[148:151], v[4:7], 0\n"
"  ds_read_b64_tr_b16 v[146:147], v248 offset:55808\n"
"  v_cvt_pk_bf16_f32 v168, v168, v169\n"
"  s_waitcnt lgkmcnt(6)\n"
"  v_mfma_f32_16x16x32_bf16 v[216:219], v[152:155], v[0:3], 0\n"
"  ds_read_b64_tr_b16 v[148:149], v249 offset:53760\n"
"  v_mfma_f32_16x16x32_bf16 v[220:223], v[152:155], v[4:7], 0\n"
"  ds_read_b64_tr_b16 v[150:151], v249 offset:55808\n"
"  v_cvt_pk_bf16_f32 v169, v170, v171\n"
"  v_mfma_f32_16x16x32_bf16 v[232:235], v[156:159], v[0:3], 0\n"
"  ds_read_b64_tr_b16 v[152:153], v248 offset:57344\n"
"  v_mfma_f32_16x16x32_bf16 v[236:239], v[156:159], v[4:7], 0\n"
"  ds_read_b64_tr_b16 v[154:155], v248 offset:59392\n"
"  v_pk_max_i16 v168, v168, 0\n"
"  s_waitcnt lgkmcnt(6)\n"
"  v_mfma_f32_16x16x32_bf16 v[200:203], v[160:163], v[8:11], v[200:203]\n"
"  ds_read_b64_tr_b16 v[156:157], v249 offset:57344\n"
"  v_mfma_f32_16x16x32_bf16 v[204:207], v[160:163], v[12:15], v[204:207]\n"
"  ds_read_b64_tr_b16 v[158:159], v249 offset:59392\n"
"  v_cvt_pk_bf16_f32 v170, v176, v177\n"
"  v_mfma_f32_16x16x32_bf16 v[240:243], v[164:167], v[8:11], v[240:243]\n"
"  ds_read_b64_tr_b16 v[160:161], v248 offset:57856\n"
"  v_mfma_f32_16x16x32_bf16 v[244:247], v[164:167], v[12:15], v[244:247]\n"
"  ds_read_b64_tr_b16 v[162:163], v248 offset:59904\n"
"  v_pk_max_i16 v169, v169, 0\n"
"  s_waitcnt lgkmcnt(6)\n"
"  v_mfma_f32_16x16x32_bf16 v[216:219], v[144:147], v[8:11], v[216:219]\n"
"  ds_read_b64_tr_b16 v[164:165], v249 offset:57856\n"
"  v_mfma_f32_16x16x32_bf16 v[220:223], v[144:147], v[12:15], v[220:223]\n"
"  ds_read_b64_tr_b16 v[166:167], v249 offset:59904\n"
"  v_cvt_pk_bf16_f32 v171, v178, v179\n"
"  v_mfma_f32_16x16x32_bf16 v[232:235], v[148:151], v[8:11], v[232:235]\n"
"  ds_read_b64_tr_b16 v[144:145], v250 offset:0\n"
"  v_mfma_f32_16x16x32_bf16 v[236:239], v[148:151], v[12:15], v[236:239]\n"
"  ds_read_b64_tr_b16 v[146:147], v250 offset:2048\n"
"  v_pk_max_i16 v170, v170, 0\n"
"  s_waitcnt lgkmcnt(6)\n"
"  v_mfma_f32_16x16x32_bf16 v[200:203], v[152:155], v[16:19], v[200:203]\n"
"  ds_read_b64_tr_b16 v[148:149], v251 offset:0\n"
"  v_mfma_f32_16x16x32_bf16 v[204:207], v[152:155], v[20:23], v[204:207]\n"
"  ds_read_b64_tr_b16 v[150:151], v251 offset:2048\n"
"  v_pk_max_i16 v171, v171, 0\n"
"  v_mfma_f32_16x16x32_bf16 v[240:243], v[156:159], v[16:19], v[240:243]\n"
"  ds_read_b64_tr_b16 v[152:153], v250 offset:512\n"
"  v_mfma_f32_16x16x32_bf16 v[244:247], v[156:159], v[20:23], v[244:247]\n"
"  ds_read_b64_tr_b16 v[154:155], v250 offset:2560\n"
"  v_cvt_pk_bf16_f32 v172, v172, v173\n"
"  s_waitcnt lgkmcnt(6)\n"
"  v_mfma_f32_16x16x32_bf16 v[216:219], v[160:163], v[16:19], v[216:219]\n"
"  ds_read_b64_tr_b16 v[156:157], v251 offset:512\n"
"  v_mfma_f32_16x16x32_bf16 v[220:223], v[160:163], v[20:23], v[220:223]\n"
"  ds_read_b64_tr_b16 v[158:159], v251 offset:2560\n"
"  v_cvt_pk_bf16_f32 v173, v174, v175\n"
"  v_mfma_f32_16x16x32_bf16 v[232:235], v[164:167], v[16:19], v[232:235]\n"
"  ds_read_b64_tr_b16 v[160:161], v250 offset:4096\n"
"  v_mfma_f32_16x16x32_bf16 v[236:239], v[164:167], v[20:23], v[236:239]\n"
"  ds_read_b64_tr_b16 v[162:163], v250 offset:6144\n"
"  v_pk_max_i16 v172, v172, 0\n"
"  s_waitcnt lgkmcnt(6)\n"
"  v_mfma_f32_16x16x32_bf16 v[200:203], v[144:147], v[24:27], v[200:203]\n"
"  ds_read_b64_tr_b16 v[164:165], v251 offset:4096\n"
"  v_mfma_f32_16x16x32_bf16 v[204:207], v[144:147], v[28:31], v[204:207]\n"
"  ds_read_b64_tr_b16 v[166:167], v251 offset:6144\n"
"  v_cvt_pk_bf16_f32 v174, v180, v181\n"
"  v_mfma_f32_16x16x32_bf16 v[240:243], v[148:151], v[24:27], v[240:243]\n"
"  ds_read_b64_tr_b16 v[144:145], v250 offset:4608\n"
"  v_mfma_f32_16x16x32_bf16 v[244:247], v[148:151], v[28:31], v[244:247]\n"
"  ds_read_b64_tr_b16 v[146:147], v250 offset:6656\n"
"  v_pk_max_i16 v173, v173, 0\n"
"  s_waitcnt lgkmcnt(6)\n"
"  v_mfma_f32_16x16x32_bf16 v[216:219], v[152:155], v[24:27], v[216:219]\n"
"  ds_read_b64_tr_b16 v[148:149], v251 offset:4608\n"
"  v_mfma_f32_16x16x32_bf16 v[220:223], v[152:155], v[28:31], v[220:223]\n"
"  ds_read_b64_tr_b16 v[150:151], v251 offset:6656\n"
"  v_cvt_pk_bf16_f32 v175, v182, v183\n"
"  v_mfma_f32_16x16x32_bf16 v[232:235], v[156:159], v[24:27], v[232:235]\n"
"  ds_read_b32 v253, v252\n"
"  ds_read_b64_tr_b16 v[152:153], v250 offset:8192\n"
"  v_mfma_f32_16x16x32_bf16 v[236:239], v[156:159], v[28:31], v[236:239]\n"
"  ds_read_b64_tr_b16 v[154:155], v250 offset:10240\n"
"  v_pk_max_i16 v174, v174, 0\n"
"  s_waitcnt lgkmcnt(7)\n"
"  v_mfma_f32_16x16x32_bf16 v[200:203], v[160:163], v[32:35], v[200:203]\n"
"  ds_read_b64_tr_b16 v[156:157], v251 offset:8192\n"
"  v_mfma_f32_16x16x32_bf16 v[204:207], v[160:163], v[36:39], v[204:207]\n"
"  ds_read_b64_tr_b16 v[158:159], v251 offset:10240\n"
"  v_pk_max_i16 v175, v175, 0\n"
"  v_mfma_f32_16x16x32_bf16 v[240:243], v[164:167], v[32:35], v[240:243]\n"
"  ds_read_b64_tr_b16 v[160:161], v250 offset:8704\n"
"  v_mfma_f32_16x16x32_bf16 v[244:247], v[164:167], v[36:39], v[244:247]\n"
"  ds_read_b64_tr_b16 v[162:163], v250 offset:10752\n"
"  v_cvt_pk_bf16_f32 v184, v184, v185\n"
"  s_waitcnt lgkmcnt(7)\n"
"  v_mfma_f32_16x16x32_bf16 v[216:219], v[144:147], v[32:35], v[216:219]\n"
"  s_waitcnt lgkmcnt(5)\n"
"  v_cmp_gt_u32 vcc, 17, v253\n"
"  ds_read_b64_tr_b16 v[164:165], v251 offset:8704\n"
"  v_mfma_f32_16x16x32_bf16 v[220:223], v[144:147], v[36:39], v[220:223]\n"
"  ds_read_b64_tr_b16 v[166:167], v251 offset:10752\n"
"  ds_inc_u32 v252, v252 offset:1024\n"
"  v_cvt_pk_bf16_f32 v185, v186, v187\n"
"  s_cbranch_vccnz .Lc_sp16\n"
".Lc_bk16:\n"
"  v_mfma_f32_16x16x32_bf16 v[232:235], v[148:151], v[32:35], v[232:235]\n"
"  ds_read_b64_tr_b16 v[144:145], v250 offset:12288\n"
"  v_mfma_f32_16x16x32_bf16 v[236:239], v[148:151], v[36:39], v[236:239]\n"
"  ds_read_b64_tr_b16 v[146:147], v250 offset:14336\n"
"  v_pk_max_i16 v184, v184, 0\n"
"  s_waitcnt lgkmcnt(7)\n"
"  v_mfma_f32_16x16x32_bf16 v[200:203], v[152:155], v[40:43], v[200:203]\n"
"  ds_read_b64_tr_b16 v[148:149], v251 offset:12288\n"
"  v_mfma_f32_16x16x32_bf16 v[204:207], v[152:155], v[44:47], v[204:207]\n"
"  ds_read_b64_tr_b16 v[150:151], v251 offset:14336\n"
"  v_cvt_pk_bf16_f32 v186, v192, v193\n"
"  v_mfma_f32_16x16x32_bf16 v[240:243], v[156:159], v[40:43], v[240:243]\n"
"  ds_read_b64_tr_b16 v[152:153], v250 offset:12800\n"
"  v_mfma_f32_16x16x32_bf16 v[244:247], v[156:159], v[44:47], v[244:247]\n"
"  ds_read_b64_tr_b16 v[154:155], v250 offset:14848\n"
"  v_pk_max_i16 v185, v185, 0\n"
"  s_waitcnt lgkmcnt(6)\n"
"  v_mfma_f32_16x16x32_bf16 v[216:219], v[160:163], v[40:43], v[216:219]\n"
"  ds_read_b64_tr_b16 v[156:157], v251 offset:12800\n"
"  v_mfma_f32_16x16x32_bf16 v[220:223], v[160:163], v[44:47], v[220:223]\n"
"  ds_read_b64_tr_b16 v[158:159], v251 offset:14848\n"
"  v_cvt_pk_bf16_f32 v187, v194, v195\n"
"  v_mfma_f32_16x16x32_bf16 v[232:235], v[164:167], v[40:43], v[232:235]\n"
"  ds_read_b64_tr_b16 v[160:161], v250 offset:16384\n"
"  v_mfma_f32_16x16x32_bf16 v[236:239], v[164:167], v[44:47], v[236:239]\n"
"  ds_read_b64_tr_b16 v[162:163], v250 offset:18432\n"
"  v_pk_max_i16 v186, v186, 0\n"
"  s_waitcnt lgkmcnt(6)\n"
"  v_mfma_f32_16x16x32_bf16 v[200:203], v[144:147], v[48:51], v[200:203]\n"
"  ds_read_b64_tr_b16 v[164:165], v251 offset:16384\n"
"  v_mfma_f32_16x16x32_bf16 v[204:207], v[144:147], v[52:55], v[204:207]\n"
"  ds_read_b64_tr_b16 v[166:167], v251 offset:18432\n"
"  v_pk_max_i16 v187, v187, 0\n"
"  v_mfma_f32_16x16x32_bf16 v[240:243], v[148:151], v[48:51], v[240:243]\n"
"  ds_read_b64_tr_b16 v[144:145], v250 offset:16896\n"
"  v_mfma_f32_16x16x32_bf16 v[244:247], v[148:151], v[52:55], v[244:247]\n"
"  ds_read_b64_tr_b16 v[146:147], v250 offset:18944\n"
"  v_cvt_pk_bf16_f32 v188, v188, v189\n"
"  s_waitcnt lgkmcnt(6)\n"
"  v_mfma_f32_16x16x32_bf16 v[216:219], v[152:155], v[48:51], v[216:219]\n"
"  ds_read_b64_tr_b16 v[148:149], v251 offset:16896\n"
"  v_mfma_f32_16x16x32_bf16 v[220:223], v[152:155], v[52:55], v[220:223]\n"
"  ds_read_b64_tr_b16 v[150:151], v251 offset:18944\n"
"  v_cvt_pk_bf16_f32 v189, v190, v191\n"
"  v_mfma_f32_16x16x32_bf16 v[232:235], v[156:159], v[48:51], v[232:235]\n"
"  ds_read_b64_tr_b16 v[152:153], v250 offset:20480\n"
"  v_mfma_f32_16x16x32_bf16 v[236:239], v[156:159], v[52:55], v[236:239]\n"
"  ds_read_b64_tr_b16 v[154:155], v250 offset:22528\n"
"  v_pk_max_i16 v188, v188, 0\n"
"  s_waitcnt lgkmcnt(6)\n"
"  v_mfma_f32_16x16x32_bf16 v[200:203], v[160:163], v[56:59], v[200:203]\n"
"  ds_read_b64_tr_b16 v[156:157], v251 offset:20480\n"
"  v_mfma_f32_16x16x32_bf16 v[204:207], v[160:163], v[60:63], v[204:207]\n"
"  ds_read_b64_tr_b16 v[158:159], v251 offset:22528\n"
"  v_cvt_pk_bf16_f32 v190, v196, v197\n"
"  v_mfma_f32_16x16x32_bf16 v[240:243], v[164:167], v[56:59], v[240:243]\n"
"  ds_read_b64_tr_b16 v[160:161], v250 offset:20992\n"
"  v_mfma_f32_16x16x32_bf16 v[244:247], v[164:167], v[60:63], v[244:247]\n"
"  ds_read_b64_tr_b16 v[162:163], v250 offset:23040\n"
"  v_pk_max_i16 v189, v189, 0\n"
"  s_waitcnt lgkmcnt(6)\n"
"  v_mfma_f32_16x16x32_bf16 v[216:219], v[144:147], v[56:59], v[216:219]\n"
"  ds_read_b64_tr_b16 v[164:165], v251 offset:20992\n"
"  v_mfma_f32_16x16x32_bf16 v[220:223], v[144:147], v[60:63], v[220:223]\n"
"  ds_read_b64_tr_b16 v[166:167], v251 offset:23040\n"
"  v_cvt_pk_bf16_f32 v191, v198, v199\n"
"  v_mfma_f32_16x16x32_bf16 v[232:235], v[148:151], v[56:59], v[232:235]\n"
"  ds_read_b64_tr_b16 v[144:145], v250 offset:24576\n"
"  v_mfma_f32_16x16x32_bf16 v[236:239], v[148:151], v[60:63], v[236:239]\n"
"  ds_read_b64_tr_b16 v[146:147], v250 offset:26624\n"
"  v_pk_max_i16 v190, v190, 0\n"
"  s_waitcnt lgkmcnt(6)\n"
"  v_mfma_f32_16x16x32_bf16 v[200:203], v[152:155], v[64:67], v[200:203]\n"
"  ds_read_b64_tr_b16 v[148:149], v251 offset:24576\n"
"  v_mfma_f32_16x16x32_bf16 v[204:207], v[152:155], v[68:71], v[204:207]\n"
"  ds_read_b64_tr_b16 v[150:151], v251 offset:26624\n"
"  v_pk_max_i16 v191, v191, 0\n"
"  v_mfma_f32_16x16x32_bf16 v[240:243], v[156:159], v[64:67], v[240:243]\n"
"  ds_read_b64_tr_b16 v[152:153], v250 offset:25088\n"
"  v_mfma_f32_16x16x32_bf16 v[244:247], v[156:159], v[68:71], v[244:247]\n"
"  ds_read_b64_tr_b16 v[154:155], v250 offset:27136\n"
"  s_waitcnt lgkmcnt(6)\n"
"  v_mfma_f32_16x16x32_bf16 v[216:219], v[160:163], v[64:67], v[216:219]\n"
"  ds_read_b64_tr_b16 v[156:157], v251 offset:25088\n"
"  v_mfma_f32_16x16x32_bf16 v[220:223], v[160:163], v[68:71], v[220:223]\n"
"  ds_read_b64_tr_b16 v[158:159], v251 offset:27136\n"
"  v_mfma_f32_16x16x32_bf16 v[232:235], v[164:167], v[64:67], v[232:235]\n"
"  ds_read_b64_tr_b16 v[160:161], v250 offset:28672\n"
"  v_mfma_f32_16x16x32_bf16 v[236:239], v[164:167], v[68:71], v[236:239]\n"
"  ds_read_b64_tr_b16 v[162:163], v250 offset:30720\n"
"  s_waitcnt lgkmcnt(6)\n"
"  v_mfma_f32_16x16x32_bf16 v[200:203], v[144:147], v[72:75], v[200:203]\n"
"  ds_read_b64_tr_b16 v[164:165], v251 offset:28672\n"
"  v_mfma_f32_16x16x32_bf16 v[204:207], v[144:147], v[76:79], v[204:207]\n"
"  ds_read_b64_tr_b16 v[166:167], v251 offset:30720\n"
"  v_mfma_f32_16x16x32_bf16 v[240:243], v[148:151], v[72:75], v[240:243]\n"
"  ds_read_b64_tr_b16 v[144:145], v250 offset:29184\n"
"  v_mfma_f32_16x16x32_bf16 v[244:247], v[148:151], v[76:79], v[244:247]\n"
"  ds_read_b64_tr_b16 v[146:147], v250 offset:31232\n"
"  s_waitcnt lgkmcnt(6)\n"
"  v_mfma_f32_16x16x32_bf16 v[216:219], v[152:155], v[72:75], v[216:219]\n"
"  ds_read_b64_tr_b16 v[148:149], v251 offset:29184\n"
"  v_mfma_f32_16x16x32_bf16 v[220:223], v[152:155], v[76:79], v[220:223]\n"
"  ds_read_b64_tr_b16 v[150:151], v251 offset:31232\n"
"  v_mfma_f32_16x16x32_bf16 v[232:235], v[156:159], v[72:75], v[232:235]\n"
"  ds_read_b32 v253, v252\n"
"  ds_read_b64_tr_b16 v[152:153], v250 offset:32768\n"
"  v_mfma_f32_16x16x32_bf16 v[236:239], v[156:159], v[76:79], v[236:239]\n"
"  ds_read_b64_tr_b16 v[154:155], v250 offset:34816\n"
"  s_waitcnt lgkmcnt(7)\n"
"  v_mfma_f32_16x16x32_bf16 v[200:203], v[160:163], v[80:83], v[200:203]\n"
"  ds_read_b64_tr_b16 v[156:157], v251 offset:32768\n"
"  v_mfma_f32_16x16x32_bf16 v[204:207], v[160:163], v[84:87], v[204:207]\n"
"  ds_read_b64_tr_b16 v[158:159], v251 offset:34816\n"
"  v_mfma_f32_16x16x32_bf16 v[240:243], v[164:167], v[80:83], v[240:243]\n"
"  ds_read_b64_tr_b16 v[160:161], v250 offset:33280\n"
"  v_mfma_f32_16x16x32_bf16 v[244:247], v[164:167], v[84:87], v[244:247]\n"
"  ds_read_b64_tr_b16 v[162:163], v250 offset:35328\n"
"  s_waitcnt lgkmcnt(7)\n"
"  v_mfma_f32_16x16x32_bf16 v[216:219], v[144:147], v[80:83], v[216:219]\n"
"  s_waitcnt lgkmcnt(5)\n"
"  v_cmp_gt_u32 vcc, 18, v253\n"
"  ds_read_b64_tr_b16 v[164:165], v251 offset:33280\n"
"  v_mfma_f32_16x16x32_bf16 v[220:223], v[144:147], v[84:87], v[220:223]\n"
"  ds_read_b64_tr_b16 v[166:167], v251 offset:35328\n"
"  ds_inc_u32 v252, v252 offset:1024\n"
"  s_cbranch_vccnz .Lc_sp17\n"
".Lc_bk17:\n"
"  v_mfma_f32_16x16x32_bf16 v[232:235], v[148:151], v[80:83], v[232:235]\n"
"  ds_read_b64_tr_b16 v[144:145], v250 offset:36864\n"
"  v_mfma_f32_16x16x32_bf16 v[236:239], v[148:151], v[84:87], v[236:239]\n"
"  ds_read_b64_tr_b16 v[146:147], v250 offset:38912\n"
"  s_waitcnt lgkmcnt(7)\n"
"  v_mfma_f32_16x16x32_bf16 v[200:203], v[152:155], v[88:91], v[200:203]\n"
"  ds_read_b64_tr_b16 v[148:149], v251 offset:36864\n"
"  v_mfma_f32_16x16x32_bf16 v[204:207], v[152:155], v[92:95], v[204:207]\n"
"  ds_read_b64_tr_b16 v[150:151], v251 offset:38912\n"
"  v_mfma_f32_16x16x32_bf16 v[240:243], v[156:159], v[88:91], v[240:243]\n"
"  ds_read_b64_tr_b16 v[152:153], v250 offset:37376\n"
"  v_mfma_f32_16x16x32_bf16 v[244:247], v[156:159], v[92:95], v[244:247]\n"
"  ds_read_b64_tr_b16 v[154:155], v250 offset:39424\n"
"  s_waitcnt lgkmcnt(6)\n"
"  v_mfma_f32_16x16x32_bf16 v[216:219], v[160:163], v[88:91], v[216:219]\n"
"  ds_read_b64_tr_b16 v[156:157], v251 offset:37376\n"
"  v_mfma_f32_16x16x32_bf16 v[220:223], v[160:163], v[92:95], v[220:223]\n"
"  ds_read_b64_tr_b16 v[158:159], v251 offset:39424\n"
"  v_mfma_f32_16x16x32_bf16 v[232:235], v[164:167], v[88:91], v[232:235]\n"
"  ds_read_b64_tr_b16 v[160:161], v250 offset:40960\n"
"  v_mfma_f32_16x16x32_bf16 v[236:239], v[164:167], v[92:95], v[236:239]\n"
"  ds_read_b64_tr_b16 v[162:163], v250 offset:43008\n"
"  s_waitcnt lgkmcnt(6)\n"
"  v_mfma_f32_16x16x32_bf16 v[200:203], v[144:147], v[96:99], v[200:203]\n"
"  ds_read_b64_tr_b16 v[164:165], v251 offset:40960\n"
"  v_mfma_f32_16x16x32_bf16 v[204:207], v[144:147], v[100:103], v[204:207]\n"
"  ds_read_b64_tr_b16 v[166:167], v251 offset:43008\n"
"  v_mfma_f32_16x16x32_bf16 v[240:243], v[148:151], v[96:99], v[240:243]\n"
"  ds_read_b64_tr_b16 v[144:145], v250 offset:41472\n"
"  v_mfma_f32_16x16x32_bf16 v[244:247], v[148:151], v[100:103], v[244:247]\n"
"  ds_read_b64_tr_b16 v[146:147], v250 offset:43520\n"
"  s_waitcnt lgkmcnt(6)\n"
"  v_mfma_f32_16x16x32_bf16 v[216:219], v[152:155], v[96:99], v[216:219]\n"
"  ds_read_b64_tr_b16 v[148:149], v251 offset:41472\n"
"  v_mfma_f32_16x16x32_bf16 v[220:223], v[152:155], v[100:103], v[220:223]\n"
"  ds_read_b64_tr_b16 v[150:151], v251 offset:43520\n"
"  v_mfma_f32_16x16x32_bf16 v[232:235], v[156:159], v[96:99], v[232:235]\n"
"  ds_read_b64_tr_b16 v[152:153], v250 offset:45056\n"
"  v_mfma_f32_16x16x32_bf16 v[236:239], v[156:159], v[100:103], v[236:239]\n"
"  ds_read_b64_tr_b16 v[154:155], v250 offset:47104\n"
"  s_waitcnt lgkmcnt(6)\n"
"  v_mfma_f32_16x16x32_bf16 v[200:203], v[160:163], v[104:107], v[200:203]\n"
"  ds_read_b64_tr_b16 v[156:157], v251 offset:45056\n"
"  v_mfma_f32_16x16x32_bf16 v[204:207], v[160:163], v[108:111], v[204:207]\n"
"  ds_read_b64_tr_b16 v[158:159], v251 offset:47104\n"
"  v_mfma_f32_16x16x32_bf16 v[240:243], v[164:167], v[104:107], v[240:243]\n"
"  ds_read_b64_tr_b16 v[160:161], v250 offset:45568\n"
"  v_mfma_f32_16x16x32_bf16 v[244:247], v[164:167], v[108:111], v[244:247]\n"
"  ds_read_b64_tr_b16 v[162:163], v250 offset:47616\n"
"  s_waitcnt lgkmcnt(6)\n"
"  v_mfma_f32_16x16x32_bf16 v[216:219], v[144:147], v[104:107], v[216:219]\n"
"  ds_read_b64_tr_b16 v[164:165], v251 offset:45568\n"
"  v_mfma_f32_16x16x32_bf16 v[220:223], v[144:147], v[108:111], v[220:223]\n"
"  ds_read_b64_tr_b16 v[166:167], v251 offset:47616\n"
"  v_mfma_f32_16x16x32_bf16 v[232:235], v[148:151], v[104:107], v[232:235]\n"
"  ds_read_b64_tr_b16 v[144:145], v250 offset:49152\n"
"  v_mfma_f32_16x16x32_bf16 v[236:239], v[148:151], v[108:111], v[236:239]\n"
"  ds_read_b64_tr_b16 v[146:147], v250 offset:51200\n"
"  s_waitcnt lgkmcnt(6)\n"
"  v_mfma_f32_16x16x32_bf16 v[200:203], v[152:155], v[112:115], v[200:203]\n"
"  ds_read_b64_tr_b16 v[148:149], v251 offset:49152\n"
"  v_mfma_f32_16x16x32_bf16 v[204:207], v[152:155], v[116:119], v[204:207]\n"
"  ds_read_b64_tr_b16 v[150:151], v251 offset:51200\n"
"  v_mfma_f32_16x16x32_bf16 v[240:243], v[156:159], v[112:115], v[240:243]\n"
"  ds_read_b64_tr_b16 v[152:153], v250 offset:53248\n"
"  v_mfma_f32_16x16x32_bf16 v[244:247], v[156:159], v[116:119], v[244:247]\n"
"  ds_read_b64_tr_b16 v[154:155], v250 offset:55296\n"
"  s_waitcnt lgkmcnt(6)\n"
"  v_mfma_f32_16x16x32_bf16 v[216:219], v[160:163], v[112:115], v[216:219]\n"
"  ds_read_b64_tr_b16 v[156:157], v251 offset:53248\n"
"  v_mfma_f32_16x16x32_bf16 v[220:223], v[160:163], v[116:119], v[220:223]\n"
"  ds_read_b64_tr_b16 v[158:159], v251 offset:55296\n"
"  v_mfma_f32_16x16x32_bf16 v[232:235], v[164:167], v[112:115], v[232:235]\n"
"  ds_read_b64_tr_b16 v[160:161], v250 offset:57344\n"
"  v_mfma_f32_16x16x32_bf16 v[236:239], v[164:167], v[116:119], v[236:239]\n"
"  ds_read_b64_tr_b16 v[162:163], v250 offset:59392\n"
"  s_waitcnt lgkmcnt(6)\n"
"  v_mfma_f32_16x16x32_bf16 v[200:203], v[144:147], v[120:123], v[200:203]\n"
"  ds_read_b64_tr_b16 v[164:165], v251 offset:57344\n"
"  v_mfma_f32_16x16x32_bf16 v[204:207], v[144:147], v[124:127], v[204:207]\n"
"  ds_read_b64_tr_b16 v[166:167], v251 offset:59392\n"
"  v_mfma_f32_16x16x32_bf16 v[240:243], v[148:151], v[120:123], v[240:243]\n"
"  ds_read_b64_tr_b16 v[144:145], v250 offset:49664\n"
"  v_mfma_f32_16x16x32_bf16 v[244:247], v[148:151], v[124:127], v[244:247]\n"
"  ds_read_b64_tr_b16 v[146:147], v250 offset:51712\n"
"  s_waitcnt lgkmcnt(6)\n"
"  v_mfma_f32_16x16x32_bf16 v[200:203], v[152:155], v[128:131], v[200:203]\n"
"  ds_read_b64_tr_b16 v[148:149], v251 offset:49664\n"
"  v_mfma_f32_16x16x32_bf16 v[204:207], v[152:155], v[132:135], v[204:207]\n"
"  ds_read_b64_tr_b16 v[150:151], v251 offset:51712\n"
"  v_mfma_f32_16x16x32_bf16 v[240:243], v[156:159], v[128:131], v[240:243]\n"
"  ds_read_b32 v253, v252\n"
"  ds_read_b64_tr_b16 v[152:153], v250 offset:53760\n"
"  v_mfma_f32_16x16x32_bf16 v[244:247], v[156:159], v[132:135], v[244:247]\n"
"  ds_read_b64_tr_b16 v[154:155], v250 offset:55808\n"
"  s_waitcnt lgkmcnt(7)\n"
"  v_mfma_f32_16x16x32_bf16 v[200:203], v[160:163], v[136:139], v[200:203]\n"
"  ds_read_b64_tr_b16 v[156:157], v251 offset:53760\n"
"  v_mfma_f32_16x16x32_bf16 v[204:207], v[160:163], v[140:143], v[204:207]\n"
"  ds_read_b64_tr_b16 v[158:159], v251 offset:55808\n"
"  v_mfma_f32_16x16x32_bf16 v[240:243], v[164:167], v[136:139], v[240:243]\n"
"  ds_read_b64_tr_b16 v[160:161], v250 offset:57856\n"
"  v_mfma_f32_16x16x32_bf16 v[244:247], v[164:167], v[140:143], v[244:247]\n"
"  ds_read_b64_tr_b16 v[162:163], v250 offset:59904\n"
"  s_waitcnt lgkmcnt(7)\n"
"  v_mfma_f32_16x16x32_bf16 v[216:219], v[144:147], v[120:123], v[216:219]\n"
"  s_waitcnt lgkmcnt(5)\n"
"  v_cmp_gt_u32 vcc, 19, v253\n"
"  ds_read_b64_tr_b16 v[164:165], v251 offset:57856\n"
"  v_mfma_f32_16x16x32_bf16 v[220:223], v[144:147], v[124:127], v[220:223]\n"
"  ds_read_b64_tr_b16 v[166:167], v251 offset:59904\n"
"  ds_inc_u32 v252, v252 offset:1024\n"
"  s_cbranch_vccnz .Lc_sp18\n"
".Lc_bk18:\n"
"  v_mfma_f32_16x16x32_bf16 v[232:235], v[148:151], v[120:123], v[232:235]\n"
"  ds_read_b64_tr_b16 v[144:145], v248 offset:0\n"
"  v_cvt_pk_bf16_f32 v200, v200, v201\n"
"  v_cvt_pk_bf16_f32 v201, v202, v203\n"
"  v_mfma_f32_16x16x32_bf16 v[236:239], v[148:151], v[124:127], v[236:239]\n"
"  ds_read_b64_tr_b16 v[146:147], v248 offset:2048\n"
"  v_pk_max_i16 v200, v200, 0\n"
"  s_waitcnt lgkmcnt(7)\n"
"  v_mfma_f32_16x16x32_bf16 v[216:219], v[152:155], v[128:131], v[216:219]\n"
"  ds_read_b64_tr_b16 v[148:149], v249 offset:0\n"
"  v_cvt_pk_bf16_f32 v202, v240, v241\n"
"  v_mfma_f32_16x16x32_bf16 v[220:223], v[152:155], v[132:135], v[220:223]\n"
"  ds_read_b64_tr_b16 v[150:151], v249 offset:2048\n"
"  v_pk_max_i16 v201, v201, 0\n"
"  v_cvt_pk_bf16_f32 v203, v242, v243\n"
"  v_mfma_f32_16x16x32_bf16 v[232:235], v[156:159], v[128:131], v[232:235]\n"
"  ds_read_b64_tr_b16 v[152:153], v248 offset:4096\n"
"  v_pk_max_i16 v202, v202, 0\n"
"  v_mfma_f32_16x16x32_bf16 v[236:239], v[156:159], v[132:135], v[236:239]\n"
"  ds_read_b64_tr_b16 v[154:155], v248 offset:6144\n"
"  v_pk_max_i16 v203, v203, 0\n"
"  s_waitcnt lgkmcnt(6)\n"
"  v_mfma_f32_16x16x32_bf16 v[216:219], v[160:163], v[136:139], v[216:219]\n"
"  ds_read_b64_tr_b16 v[156:157], v249 offset:4096\n"
"  v_cvt_pk_bf16_f32 v204, v204, v205\n"
"  v_cvt_pk_bf16_f32 v205, v206, v207\n"
"  v_mfma_f32_16x16x32_bf16 v[220:223], v[160:163], v[140:143], v[220:223]\n"
"  ds_read_b64_tr_b16 v[158:159], v249 offset:6144\n"
"  v_pk_max_i16 v204, v204, 0\n"
"  v_mfma_f32_16x16x32_bf16 v[232:235], v[164:167], v[136:139], v[232:235]\n"
"  ds_read_b64_tr_b16 v[160:161], v248 offset:8192\n"
"  v_cvt_pk_bf16_f32 v206, v244, v245\n"
"  v_mfma_f32_16x16x32_bf16 v[236:239], v[164:167], v[140:143], v[236:239]\n"
"  ds_read_b64_tr_b16 v[162:163], v248 offset:10240\n"
"  v_pk_max_i16 v205, v205, 0\n"
"  v_cvt_pk_bf16_f32 v207, v246, v247\n"
"  s_waitcnt lgkmcnt(6)\n"
"  v_mfma_f32_16x16x32_bf16 v[176:179], v[144:147], v[0:3], 0\n"
"  ds_read_b64_tr_b16 v[164:165], v249 offset:8192\n"
"  v_pk_max_i16 v206, v206, 0\n"
"  v_mfma_f32_16x16x32_bf16 v[180:183], v[144:147], v[4:7], 0\n"
"  ds_read_b64_tr_b16 v[166:167], v249 offset:10240\n"
"  v_pk_max_i16 v207, v207, 0\n"
"  v_mfma_f32_16x16x32_bf16 v[192:195], v[148:151], v[0:3], 0\n"
"  ds_read_b64_tr_b16 v[144:145], v248 offset:512\n"
"  v_cvt_pk_bf16_f32 v216, v216, v217\n"
"  v_cvt_pk_bf16_f32 v217, v218, v219\n"
"  v_mfma_f32_16x16x32_bf16 v[196:199], v[148:151], v[4:7], 0\n"
"  ds_read_b64_tr_b16 v[146:147], v248 offset:2560\n"
"  v_pk_max_i16 v216, v216, 0\n"
"  s_waitcnt lgkmcnt(6)\n"
"  v_mfma_f32_16x16x32_bf16 v[176:179], v[152:155], v[8:11], v[176:179]\n"
"  ds_read_b64_tr_b16 v[148:149], v249 offset:512\n"
"  v_cvt_pk_bf16_f32 v218, v232, v233\n"
"  v_pk_max_i16 v217, v217, 0\n"
"  v_mfma_f32_16x16x32_bf16 v[180:183], v[152:155], v[12:15], v[180:183]\n"
"  ds_read_b64_tr_b16 v[150:151], v249 offset:2560\n"
"  v_cvt_pk_bf16_f32 v219, v234, v235\n"
"  v_mfma_f32_16x16x32_bf16 v[192:195], v[156:159], v[8:11], v[192:195]\n"
"  ds_read_b64_tr_b16 v[152:153], v248 offset:4608\n"
"  v_pk_max_i16 v218, v218, 0\n"
"  v_pk_max_i16 v219, v219, 0\n"
"  v_mfma_f32_16x16x32_bf16 v[196:199], v[156:159], v[12:15], v[196:199]\n"
"  ds_read_b64_tr_b16 v[154:155], v248 offset:6656\n"
"  v_cvt_pk_bf16_f32 v220, v220, v221\n"
"  s_waitcnt lgkmcnt(6)\n"
"  v_mfma_f32_16x16x32_bf16 v[176:179], v[160:163], v[16:19], v[176:179]\n"
"  ds_read_b64_tr_b16 v[156:157], v249 offset:4608\n"
"  v_cvt_pk_bf16_f32 v221, v222, v223\n"
"  v_mfma_f32_16x16x32_bf16 v[180:183], v[160:163], v[20:23], v[180:183]\n"
"  ds_read_b64_tr_b16 v[158:159], v249 offset:6656\n"
"  v_pk_max_i16 v220, v220, 0\n"
"  v_cvt_pk_bf16_f32 v222, v236, v237\n"
"  v_mfma_f32_16x16x32_bf16 v[192:195], v[164:167], v[16:19], v[192:195]\n"
"  ds_read_b64_tr_b16 v[160:161], v248 offset:8704\n"
"  v_pk_max_i16 v221, v221, 0\n"
"  v_mfma_f32_16x16x32_bf16 v[196:199], v[164:167], v[20:23], v[196:199]\n"
"  ds_read_b64_tr_b16 v[162:163], v248 offset:10752\n"
"  v_cvt_pk_bf16_f32 v223, v238, v239\n"
"  v_pk_max_i16 v222, v222, 0\n"
"  s_waitcnt lgkmcnt(6)\n"
"  v_mfma_f32_16x16x32_bf16 v[240:243], v[144:147], v[0:3], 0\n"
"  ds_read_b64_tr_b16 v[164:165], v249 offset:8704\n"
"  v_pk_max_i16 v223, v223, 0\n"
"  v_mfma_f32_16x16x32_bf16 v[244:247], v[144:147], v[4:7], 0\n"
"  ds_read_b64_tr_b16 v[166:167], v249 offset:10752\n"
"  v_mfma_f32_16x16x32_bf16 v[232:235], v[148:151], v[0:3], 0\n"
"  ds_read_b64_tr_b16 v[144:145], v248 offset:12288\n"
"  v_mfma_f32_16x16x32_bf16 v[236:239], v[148:151], v[4:7], 0\n"
"  ds_read_b64_tr_b16 v[146:147], v248 offset:14336\n"
"  s_waitcnt lgkmcnt(6)\n"
"  v_mfma_f32_16x16x32_bf16 v[240:243], v[152:155], v[8:11], v[240:243]\n"
"  ds_read_b64_tr_b16 v[148:149], v249 offset:12288\n"
"  v_mfma_f32_16x16x32_bf16 v[244:247], v[152:155], v[12:15], v[244:247]\n"
"  ds_read_b64_tr_b16 v[150:151], v249 offset:14336\n"
"  v_mfma_f32_16x16x32_bf16 v[232:235], v[156:159], v[8:11], v[232:235]\n"
"  ds_read_b64_tr_b16 v[152:153], v248 offset:12800\n"
"  v_mfma_f32_16x16x32_bf16 v[236:239], v[156:159], v[12:15], v[236:239]\n"
"  ds_read_b64_tr_b16 v[154:155], v248 offset:14848\n"
"  s_waitcnt lgkmcnt(6)\n"
"  v_mfma_f32_16x16x32_bf16 v[240:243], v[160:163], v[16:19], v[240:243]\n"
"  ds_read_b64_tr_b16 v[156:157], v249 offset:12800\n"
"  v_mfma_f32_16x16x32_bf16 v[244:247], v[160:163], v[20:23], v[244:247]\n"
"  ds_read_b64_tr_b16 v[158:159], v249 offset:14848\n"
"  v_mfma_f32_16x16x32_bf16 v[232:235], v[164:167], v[16:19], v[232:235]\n"
"  ds_read_b64_tr_b16 v[160:161], v248 offset:16384\n"
"  v_mfma_f32_16x16x32_bf16 v[236:239], v[164:167], v[20:23], v[236:239]\n"
"  ds_read_b64_tr_b16 v[162:163], v248 offset:18432\n"
"  s_waitcnt lgkmcnt(6)\n"
"  v_mfma_f32_16x16x32_bf16 v[176:179], v[144:147], v[24:27], v[176:179]\n"
"  ds_read_b64_tr_b16 v[164:165], v249 offset:16384\n"
"  v_mfma_f32_16x16x32_bf16 v[180:183], v[144:147], v[28:31], v[180:183]\n"
"  ds_read_b64_tr_b16 v[166:167], v249 offset:18432\n"
"  v_mfma_f32_16x16x32_bf16 v[192:195], v[148:151], v[24:27], v[192:195]\n"
"  ds_read_b64_tr_b16 v[144:145], v248 offset:16896\n"
"  v_mfma_f32_16x16x32_bf16 v[196:199], v[148:151], v[28:31], v[196:199]\n"
"  ds_read_b64_tr_b16 v[146:147], v248 offset:18944\n"
"  s_waitcnt lgkmcnt(6)\n"
"  v_mfma_f32_16x16x32_bf16 v[240:243], v[152:155], v[24:27], v[240:243]\n"
"  ds_read_b64_tr_b16 v[148:149], v249 offset:16896\n"
"  v_mfma_f32_16x16x32_bf16 v[244:247], v[152:155], v[28:31], v[244:247]\n"
"  ds_read_b64_tr_b16 v[150:151], v249 offset:18944\n"
"  v_mfma_f32_16x16x32_bf16 v[232:235], v[156:159], v[24:27], v[232:235]\n"
"  ds_read_b32 v253, v252\n"
"  ds_read_b64_tr_b16 v[152:153], v248 offset:20480\n"
"  v_mfma_f32_16x16x32_bf16 v[236:239], v[156:159], v[28:31], v[236:239]\n"
"  ds_read_b64_tr_b16 v[154:155], v248 offset:22528\n"
"  s_waitcnt lgkmcnt(7)\n"
"  v_mfma_f32_16x16x32_bf16 v[176:179], v[160:163], v[32:35], v[176:179]\n"
"  ds_read_b64_tr_b16 v[156:157], v249 offset:20480\n"
"  v_mfma_f32_16x16x32_bf16 v[180:183], v[160:163], v[36:39], v[180:183]\n"
"  ds_read_b64_tr_b16 v[158:159], v249 offset:22528\n"
"  v_mfma_f32_16x16x32_bf16 v[192:195], v[164:167], v[32:35], v[192:195]\n"
"  ds_read_b64_tr_b16 v[160:161], v248 offset:20992\n"
"  v_mfma_f32_16x16x32_bf16 v[196:199], v[164:167], v[36:39], v[196:199]\n"
"  ds_read_b64_tr_b16 v[162:163], v248 offset:23040\n"
"  s_waitcnt lgkmcnt(7)\n"
"  v_mfma_f32_16x16x32_bf16 v[240:243], v[144:147], v[32:35], v[240:243]\n"
"  s_waitcnt lgkmcnt(5)\n"
"  v_cmp_gt_u32 vcc, 20, v253\n"
"  ds_read_b64_tr_b16 v[164:165], v249 offset:20992\n"
"  v_mfma_f32_16x16x32_bf16 v[244:247], v[144:147], v[36:39], v[244:247]\n"
"  ds_read_b64_tr_b16 v[166:167], v249 offset:23040\n"
"  ds_inc_u32 v252, v252 offset:1024\n"
"  s_cbranch_vccnz .Lc_sp19\n"
".Lc_bk19:\n"
"  v_mfma_f32_16x16x32_bf16 v[232:235], v[148:151], v[32:35], v[232:235]\n"
"  ds_read_b64_tr_b16 v[144:145], v248 offset:24576\n"
"  v_mfma_f32_16x16x32_bf16 v[236:239], v[148:151], v[36:39], v[236:239]\n"
"  ds_read_b64_tr_b16 v[146:147], v248 offset:26624\n"
"  s_waitcnt lgkmcnt(7)\n"
"  v_mfma_f32_16x16x32_bf16 v[176:179], v[152:155], v[40:43], v[176:179]\n"
"  ds_read_b64_tr_b16 v[148:149], v249 offset:24576\n"
"  v_mfma_f32_16x16x32_bf16 v[180:183], v[152:155], v[44:47], v[180:183]\n"
"  ds_read_b64_tr_b16 v[150:151], v249 offset:26624\n"
"  v_mfma_f32_16x16x32_bf16 v[192:195], v[156:159], v[40:43], v[192:195]\n"
"  ds_read_b64_tr_b16 v[152:153], v248 offset:25088\n"
"  v_mfma_f32_16x16x32_bf16 v[196:199], v[156:159], v[44:47], v[196:199]\n"
"  ds_read_b64_tr_b16 v[154:155], v248 offset:27136\n"
"  s_waitcnt lgkmcnt(6)\n"
"  v_mfma_f32_16x16x32_bf16 v[240:243], v[160:163], v[40:43], v[240:243]\n"
"  ds_read_b64_tr_b16 v[156:157], v249 offset:25088\n"
"  v_mfma_f32_16x16x32_bf16 v[244:247], v[160:163], v[44:47], v[244:247]\n"
"  ds_read_b64_tr_b16 v[158:159], v249 offset:27136\n"
"  v_mfma_f32_16x16x32_bf16 v[232:235], v[164:167], v[40:43], v[232:235]\n"
"  ds_read_b64_tr_b16 v[160:161], v248 offset:28672\n"
"  v_mfma_f32_16x16x32_bf16 v[236:239], v[164:167], v[44:47], v[236:239]\n"
"  ds_read_b64_tr_b16 v[162:163], v248 offset:30720\n"
"  s_waitcnt lgkmcnt(6)\n"
"  v_mfma_f32_16x16x32_bf16 v[176:179], v[144:147], v[48:51], v[176:179]\n"
"  ds_read_b64_tr_b16 v[164:165], v249 offset:28672\n"
"  v_mfma_f32_16x16x32_bf16 v[180:183], v[144:147], v[52:55], v[180:183]\n"
"  ds_read_b64_tr_b16 v[166:167], v249 offset:30720\n"
"  v_mfma_f32_16x16x32_bf16 v[192:195], v[148:151], v[48:51], v[192:195]\n"
"  ds_read_b64_tr_b16 v[144:145], v248 offset:29184\n"
"  v_mfma_f32_16x16x32_bf16 v[196:199], v[148:151], v[52:55], v[196:199]\n"
"  ds_read_b64_tr_b16 v[146:147], v248 offset:31232\n"
"  s_waitcnt lgkmcnt(6)\n"
"  v_mfma_f32_16x16x32_bf16 v[240:243], v[152:155], v[48:51], v[240:243]\n"
"  ds_read_b64_tr_b16 v[148:149], v249 offset:29184\n"
"  v_mfma_f32_16x16x32_bf16 v[244:247], v[152:155], v[52:55], v[244:247]\n"
"  ds_read_b64_tr_b16 v[150:151], v249 offset:31232\n"
"  v_mfma_f32_16x16x32_bf16 v[232:235], v[156:159], v[48:51], v[232:235]\n"
"  ds_read_b64_tr_b16 v[152:153], v248 offset:32768\n"
"  v_mfma_f32_16x16x32_bf16 v[236:239], v[156:159], v[52:55], v[236:239]\n"
"  ds_read_b64_tr_b16 v[154:155], v248 offset:34816\n"
"  s_waitcnt lgkmcnt(6)\n"
"  v_mfma_f32_16x16x32_bf16 v[176:179], v[160:163], v[56:59], v[176:179]\n"
"  ds_read_b64_tr_b16 v[156:157], v249 offset:32768\n"
"  v_mfma_f32_16x16x32_bf16 v[180:183], v[160:163], v[60:63], v[180:183]\n"
"  ds_read_b64_tr_b16 v[158:159], v249 offset:34816\n"
"  v_mfma_f32_16x16x32_bf16 v[192:195], v[164:167], v[56:59], v[192:195]\n"
"  ds_read_b64_tr_b16 v[160:161], v248 offset:33280\n"
"  v_mfma_f32_16x16x32_bf16 v[196:199], v[164:167], v[60:63], v[196:199]\n"
"  ds_read_b64_tr_b16 v[162:163], v248 offset:35328\n"
"  s_waitcnt lgkmcnt(6)\n"
"  v_mfma_f32_16x16x32_bf16 v[240:243], v[144:147], v[56:59], v[240:243]\n"
"  ds_read_b64_tr_b16 v[164:165], v249 offset:33280\n"
"  v_mfma_f32_16x16x32_bf16 v[244:247], v[144:147], v[60:63], v[244:247]\n"
"  ds_read_b64_tr_b16 v[166:167], v249 offset:35328\n"
"  v_mfma_f32_16x16x32_bf16 v[232:235], v[148:151], v[56:59], v[232:235]\n"
"  ds_read_b64_tr_b16 v[144:145], v248 offset:36864\n"
"  v_mfma_f32_16x16x32_bf16 v[236:239], v[148:151], v[60:63], v[236:239]\n"
"  ds_read_b64_tr_b16 v[146:147], v248 offset:38912\n"
"  s_waitcnt lgkmcnt(6)\n"
"  v_mfma_f32_16x16x32_bf16 v[176:179], v[152:155], v[64:67], v[176:179]\n"
"  ds_read_b64_tr_b16 v[148:149], v249 offset:36864\n"
"  v_mfma_f32_16x16x32_bf16 v[180:183], v[152:155], v[68:71], v[180:183]\n"
"  ds_read_b64_tr_b16 v[150:151], v249 offset:38912\n"
"  v_mfma_f32_16x16x32_bf16 v[192:195], v[156:159], v[64:67], v[192:195]\n"
"  ds_read_b64_tr_b16 v[152:153], v248 offset:37376\n"
"  v_mfma_f32_16x16x32_bf16 v[196:199], v[156:159], v[68:71], v[196:199]\n"
"  ds_read_b64_tr_b16 v[154:155], v248 offset:39424\n"
"  s_waitcnt lgkmcnt(6)\n"
"  v_mfma_f32_16x16x32_bf16 v[240:243], v[160:163], v[64:67], v[240:243]\n"
"  ds_read_b64_tr_b16 v[156:157], v249 offset:37376\n"
"  v_mfma_f32_16x16x32_bf16 v[244:247], v[160:163], v[68:71], v[244:247]\n"
"  ds_read_b64_tr_b16 v[158:159], v249 offset:39424\n"
"  v_mfma_f32_16x16x32_bf16 v[232:235], v[164:167], v[64:67], v[232:235]\n"
"  ds_read_b64_tr_b16 v[160:161], v248 offset:40960\n"
"  v_mfma_f32_16x16x32_bf16 v[236:239], v[164:167], v[68:71], v[236:239]\n"
"  ds_read_b64_tr_b16 v[162:163], v248 offset:43008\n"
"  s_waitcnt lgkmcnt(6)\n"
"  v_mfma_f32_16x16x32_bf16 v[176:179], v[144:147], v[72:75], v[176:179]\n"
"  ds_read_b64_tr_b16 v[164:165], v249 offset:40960\n"
"  v_mfma_f32_16x16x32_bf16 v[180:183], v[144:147], v[76:79], v[180:183]\n"
"  ds_read_b64_tr_b16 v[166:167], v249 offset:43008\n"
"  v_mfma_f32_16x16x32_bf16 v[192:195], v[148:151], v[72:75], v[192:195]\n"
"  ds_read_b64_tr_b16 v[144:145], v248 offset:41472\n"
"  v_mfma_f32_16x16x32_bf16 v[196:199], v[148:151], v[76:79], v[196:199]\n"
"  ds_read_b64_tr_b16 v[146:147], v248 offset:43520\n"
"  s_waitcnt lgkmcnt(6)\n"
"  v_mfma_f32_16x16x32_bf16 v[240:243], v[152:155], v[72:75], v[240:243]\n"
"  ds_read_b64_tr_b16 v[148:149], v249 offset:41472\n"
"  v_mfma_f32_16x16x32_bf16 v[244:247], v[152:155], v[76:79], v[244:247]\n"
"  ds_read_b64_tr_b16 v[150:151], v249 offset:43520\n"
"  v_mfma_f32_16x16x32_bf16 v[232:235], v[156:159], v[72:75], v[232:235]\n"
"  ds_read_b32 v253, v252\n"
"  ds_read_b64_tr_b16 v[152:153], v248 offset:45056\n"
"  v_mfma_f32_16x16x32_bf16 v[236:239], v[156:159], v[76:79], v[236:239]\n"
"  ds_read_b64_tr_b16 v[154:155], v248 offset:47104\n"
"  s_waitcnt lgkmcnt(7)\n"
"  v_mfma_f32_16x16x32_bf16 v[176:179], v[160:163], v[80:83], v[176:179]\n"
"  ds_read_b64_tr_b16 v[156:157], v249 offset:45056\n"
"  v_mfma_f32_16x16x32_bf16 v[180:183], v[160:163], v[84:87], v[180:183]\n"
"  ds_read_b64_tr_b16 v[158:159], v249 offset:47104\n"
"  v_mfma_f32_16x16x32_bf16 v[192:195], v[164:167], v[80:83], v[192:195]\n"
"  ds_read_b64_tr_b16 v[160:161], v248 offset:45568\n"
"  v_mfma_f32_16x16x32_bf16 v[196:199], v[164:167], v[84:87], v[196:199]\n"
"  ds_read_b64_tr_b16 v[162:163], v248 offset:47616\n"
"  s_waitcnt lgkmcnt(7)\n"
"  v_mfma_f32_16x16x32_bf16 v[240:243], v[144:147], v[80:83], v[240:243]\n"
"  s_waitcnt lgkmcnt(5)\n"
"  v_cmp_gt_u32 vcc, 21, v253\n"
"  ds_read_b64_tr_b16 v[164:165], v249 offset:45568\n"
"  v_mfma_f32_16x16x32_bf16 v[244:247], v[144:147], v[84:87], v[244:247]\n"
"  ds_read_b64_tr_b16 v[166:167], v249 offset:47616\n"
"  ds_inc_u32 v252, v252 offset:1024\n"
"  s_cbranch_vccnz .Lc_sp20\n"
".Lc_bk20:\n"
"  v_mfma_f32_16x16x32_bf16 v[232:235], v[148:151], v[80:83], v[232:235]\n"
"  ds_read_b64_tr_b16 v[144:145], v248 offset:49152\n"
"  v_mfma_f32_16x16x32_bf16 v[236:239], v[148:151], v[84:87], v[236:239]\n"
"  ds_read_b64_tr_b16 v[146:147], v248 offset:51200\n"
"  s_waitcnt lgkmcnt(7)\n"
"  v_mfma_f32_16x16x32_bf16 v[176:179], v[152:155], v[88:91], v[176:179]\n"
"  ds_read_b64_tr_b16 v[148:149], v249 offset:49152\n"
"  v_mfma_f32_16x16x32_bf16 v[180:183], v[152:155], v[92:95], v[180:183]\n"
"  ds_read_b64_tr_b16 v[150:151], v249 offset:51200\n"
"  v_mfma_f32_16x16x32_bf16 v[192:195], v[156:159], v[88:91], v[192:195]\n"
"  ds_read_b64_tr_b16 v[152:153], v248 offset:49664\n"
"  v_mfma_f32_16x16x32_bf16 v[196:199], v[156:159], v[92:95], v[196:199]\n"
"  ds_read_b64_tr_b16 v[154:155], v248 offset:51712\n"
"  s_waitcnt lgkmcnt(6)\n"
"  v_mfma_f32_16x16x32_bf16 v[240:243], v[160:163], v[88:91], v[240:243]\n"
"  ds_read_b64_tr_b16 v[156:157], v249 offset:49664\n"
"  v_mfma_f32_16x16x32_bf16 v[244:247], v[160:163], v[92:95], v[244:247]\n"
"  ds_read_b64_tr_b16 v[158:159], v249 offset:51712\n"
"  v_mfma_f32_16x16x32_bf16 v[232:235], v[164:167], v[88:91], v[232:235]\n"
"  ds_read_b64_tr_b16 v[160:161], v248 offset:53248\n"
"  v_mfma_f32_16x16x32_bf16 v[236:239], v[164:167], v[92:95], v[236:239]\n"
"  ds_read_b64_tr_b16 v[162:163], v248 offset:55296\n"
"  s_waitcnt lgkmcnt(6)\n"
"  v_mfma_f32_16x16x32_bf16 v[176:179], v[144:147], v[96:99], v[176:179]\n"
"  ds_read_b64_tr_b16 v[164:165], v249 offset:53248\n"
"  v_mfma_f32_16x16x32_bf16 v[180:183], v[144:147], v[100:103], v[180:183]\n"
"  ds_read_b64_tr_b16 v[166:167], v249 offset:55296\n"
"  v_mfma_f32_16x16x32_bf16 v[192:195], v[148:151], v[96:99], v[192:195]\n"
"  ds_read_b64_tr_b16 v[144:145], v248 offset:53760\n"
"  v_mfma_f32_16x16x32_bf16 v[196:199], v[148:151], v[100:103], v[196:199]\n"
"  ds_read_b64_tr_b16 v[146:147], v248 offset:55808\n"
"  s_waitcnt lgkmcnt(6)\n"
"  v_mfma_f32_16x16x32_bf16 v[240:243], v[152:155], v[96:99], v[240:243]\n"
"  ds_read_b64_tr_b16 v[148:149], v249 offset:53760\n"
"  v_mfma_f32_16x16x32_bf16 v[244:247], v[152:155], v[100:103], v[244:247]\n"
"  ds_read_b64_tr_b16 v[150:151], v249 offset:55808\n"
"  v_mfma_f32_16x16x32_bf16 v[232:235], v[156:159], v[96:99], v[232:235]\n"
"  ds_read_b64_tr_b16 v[152:153], v248 offset:57344\n"
"  v_mfma_f32_16x16x32_bf16 v[236:239], v[156:159], v[100:103], v[236:239]\n"
"  ds_read_b64_tr_b16 v[154:155], v248 offset:59392\n"
"  s_waitcnt lgkmcnt(6)\n"
"  v_mfma_f32_16x16x32_bf16 v[176:179], v[160:163], v[104:107], v[176:179]\n"
"  ds_read_b64_tr_b16 v[156:157], v249 offset:57344\n"
"  v_mfma_f32_16x16x32_bf16 v[180:183], v[160:163], v[108:111], v[180:183]\n"
"  ds_read_b64_tr_b16 v[158:159], v249 offset:59392\n"
"  v_mfma_f32_16x16x32_bf16 v[192:195], v[164:167], v[104:107], v[192:195]\n"
"  ds_read_b64_tr_b16 v[160:161], v248 offset:57856\n"
"  v_mfma_f32_16x16x32_bf16 v[196:199], v[164:167], v[108:111], v[196:199]\n"
"  ds_read_b64_tr_b16 v[162:163], v248 offset:59904\n"
"  s_waitcnt lgkmcnt(6)\n"
"  v_mfma_f32_16x16x32_bf16 v[240:243], v[144:147], v[104:107], v[240:243]\n"
"  ds_read_b64_tr_b16 v[164:165], v249 offset:57856\n"
"  v_mfma_f32_16x16x32_bf16 v[244:247], v[144:147], v[108:111], v[244:247]\n"
"  ds_read_b64_tr_b16 v[166:167], v249 offset:59904\n"
"  v_mfma_f32_16x16x32_bf16 v[232:235], v[148:151], v[104:107], v[232:235]\n"
"  ds_read_b64_tr_b16 v[144:145], v250 offset:0\n"
"  v_mfma_f32_16x16x32_bf16 v[236:239], v[148:151], v[108:111], v[236:239]\n"
"  ds_read_b64_tr_b16 v[146:147], v250 offset:2048\n"
"  s_waitcnt lgkmcnt(6)\n"
"  v_mfma_f32_16x16x32_bf16 v[176:179], v[152:155], v[112:115], v[176:179]\n"
"  ds_read_b64_tr_b16 v[148:149], v251 offset:0\n"
"  v_mfma_f32_16x16x32_bf16 v[180:183], v[152:155], v[116:119], v[180:183]\n"
"  ds_read_b64_tr_b16 v[150:151], v251 offset:2048\n"
"  v_mfma_f32_16x16x32_bf16 v[192:195], v[156:159], v[112:115], v[192:195]\n"
"  ds_read_b64_tr_b16 v[152:153], v250 offset:4096\n"
"  v_mfma_f32_16x16x32_bf16 v[196:199], v[156:159], v[116:119], v[196:199]\n"
"  ds_read_b64_tr_b16 v[154:155], v250 offset:6144\n"
"  s_waitcnt lgkmcnt(6)\n"
"  v_mfma_f32_16x16x32_bf16 v[240:243], v[160:163], v[112:115], v[240:243]\n"
"  ds_read_b64_tr_b16 v[156:157], v251 offset:4096\n"
"  v_mfma_f32_16x16x32_bf16 v[244:247], v[160:163], v[116:119], v[244:247]\n"
"  ds_read_b64_tr_b16 v[158:159], v251 offset:6144\n"
"  v_mfma_f32_16x16x32_bf16 v[232:235], v[164:167], v[112:115], v[232:235]\n"
"  ds_read_b64_tr_b16 v[160:161], v250 offset:8192\n"
"  v_mfma_f32_16x16x32_bf16 v[236:239], v[164:167], v[116:119], v[236:239]\n"
"  ds_read_b64_tr_b16 v[162:163], v250 offset:10240\n"
"  s_waitcnt lgkmcnt(6)\n"
"  v_mfma_f32_16x16x32_bf16 v[176:179], v[144:147], v[120:123], v[176:179]\n"
"  ds_read_b64_tr_b16 v[164:165], v251 offset:8192\n"
"  v_mfma_f32_16x16x32_bf16 v[180:183], v[144:147], v[124:127], v[180:183]\n"
"  ds_read_b64_tr_b16 v[166:167], v251 offset:10240\n"
"  v_mfma_f32_16x16x32_bf16 v[192:195], v[148:151], v[120:123], v[192:195]\n"
"  ds_read_b64_tr_b16 v[144:145], v250 offset:512\n"
"  v_mfma_f32_16x16x32_bf16 v[196:199], v[148:151], v[124:127], v[196:199]\n"
"  ds_read_b64_tr_b16 v[146:147], v250 offset:2560\n"
"  s_waitcnt lgkmcnt(6)\n"
"  v_mfma_f32_16x16x32_bf16 v[176:179], v[152:155], v[128:131], v[176:179]\n"
"  ds_read_b64_tr_b16 v[148:149], v251 offset:512\n"
"  v_mfma_f32_16x16x32_bf16 v[180:183], v[152:155], v[132:135], v[180:183]\n"
"  ds_read_b64_tr_b16 v[150:151], v251 offset:2560\n"
"  v_mfma_f32_16x16x32_bf16 v[192:195], v[156:159], v[128:131], v[192:195]\n"
"  ds_read_b32 v253, v252\n"
"  ds_read_b64_tr_b16 v[152:153], v250 offset:4608\n"
"  v_mfma_f32_16x16x32_bf16 v[196:199], v[156:159], v[132:135], v[196:199]\n"
"  ds_read_b64_tr_b16 v[154:155], v250 offset:6656\n"
"  s_waitcnt lgkmcnt(7)\n"
"  v_mfma_f32_16x16x32_bf16 v[176:179], v[160:163], v[136:139], v[176:179]\n"
"  ds_read_b64_tr_b16 v[156:157], v251 offset:4608\n"
"  v_mfma_f32_16x16x32_bf16 v[180:183], v[160:163], v[140:143], v[180:183]\n"
"  ds_read_b64_tr_b16 v[158:159], v251 offset:6656\n"
"  v_mfma_f32_16x16x32_bf16 v[192:195], v[164:167], v[136:139], v[192:195]\n"
"  ds_read_b64_tr_b16 v[160:161], v250 offset:8704\n"
"  v_mfma_f32_16x16x32_bf16 v[196:199], v[164:167], v[140:143], v[196:199]\n"
"  ds_read_b64_tr_b16 v[162:163], v250 offset:10752\n"
"  s_waitcnt lgkmcnt(7)\n"
"  v_mfma_f32_16x16x32_bf16 v[240:243], v[144:147], v[120:123], v[240:243]\n"
"  s_waitcnt lgkmcnt(5)\n"
"  v_cmp_gt_u32 vcc, 22, v253\n"
"  ds_read_b64_tr_b16 v[164:165], v251 offset:8704\n"
"  v_mfma_f32_16x16x32_bf16 v[244:247], v[144:147], v[124:127], v[244:247]\n"
"  ds_read_b64_tr_b16 v[166:167], v251 offset:10752\n"
"  ds_inc_u32 v252, v252 offset:1024\n"
"  s_cbranch_vccnz .Lc_sp21\n"
".Lc_bk21:\n"
"  v_mfma_f32_16x16x32_bf16 v[232:235], v[148:151], v[120:123], v[232:235]\n"
"  ds_read_b64_tr_b16 v[144:145], v250 offset:12288\n"
"  v_cvt_pk_bf16_f32 v176, v176, v177\n"
"  v_cvt_pk_bf16_f32 v177, v178, v179\n"
"  v_mfma_f32_16x16x32_bf16 v[236:239], v[148:151], v[124:127], v[236:239]\n"
"  ds_read_b64_tr_b16 v[146:147], v250 offset:14336\n"
"  v_pk_max_i16 v176, v176, 0\n"
"  v_cvt_pk_bf16_f32 v178, v192, v193\n"
"  s_waitcnt lgkmcnt(7)\n"
"  v_mfma_f32_16x16x32_bf16 v[240:243], v[152:155], v[128:131], v[240:243]\n"
"  ds_read_b64_tr_b16 v[148:149], v250 offset:12800\n"
"  v_pk_max_i16 v177, v177, 0\n"
"  v_mfma_f32_16x16x32_bf16 v[244:247], v[152:155], v[132:135], v[244:247]\n"
"  ds_read_b64_tr_b16 v[150:151], v250 offset:14848\n"
"  v_cvt_pk_bf16_f32 v179, v194, v195\n"
"  v_pk_max_i16 v178, v178, 0\n"
"  v_mfma_f32_16x16x32_bf16 v[232:235], v[156:159], v[128:131], v[232:235]\n"
"  ds_read_b64_tr_b16 v[152:153], v250 offset:16384\n"
"  v_pk_max_i16 v179, v179, 0\n"
"  v_cvt_pk_bf16_f32 v180, v180, v181\n"
"  v_mfma_f32_16x16x32_bf16 v[236:239], v[156:159], v[132:135], v[236:239]\n"
"  ds_read_b64_tr_b16 v[154:155], v250 offset:18432\n"
"  v_cvt_pk_bf16_f32 v181, v182, v183\n"
"  s_waitcnt lgkmcnt(6)\n"
"  v_mfma_f32_16x16x32_bf16 v[240:243], v[160:163], v[136:139], v[240:243]\n"
"  ds_read_b64_tr_b16 v[156:157], v250 offset:16896\n"
"  v_pk_max_i16 v180, v180, 0\n"
"  v_cvt_pk_bf16_f32 v182, v196, v197\n"
"  v_mfma_f32_16x16x32_bf16 v[244:247], v[160:163], v[140:143], v[244:247]\n"
"  ds_read_b64_tr_b16 v[158:159], v250 offset:18944\n"
"  v_pk_max_i16 v181, v181, 0\n"
"  v_cvt_pk_bf16_f32 v183, v198, v199\n"
"  v_mfma_f32_16x16x32_bf16 v[232:235], v[164:167], v[136:139], v[232:235]\n"
"  ds_read_b64_tr_b16 v[160:161], v250 offset:20480\n"
"  v_pk_max_i16 v182, v182, 0\n"
"  v_mfma_f32_16x16x32_bf16 v[236:239], v[164:167], v[140:143], v[236:239]\n"
"  ds_read_b64_tr_b16 v[162:163], v250 offset:22528\n"
"  v_pk_max_i16 v183, v183, 0\n"
"  s_waitcnt lgkmcnt(6)\n"
"  v_mfma_f32_16x16x32_bf16 v[192:195], v[144:147], v[0:3], 0\n"
"  ds_read_b64_tr_b16 v[164:165], v250 offset:20992\n"
"  v_mfma_f32_16x16x32_bf16 v[196:199], v[144:147], v[4:7], 0\n"
"  ds_read_b64_tr_b16 v[166:167], v250 offset:23040\n"
"  v_mfma_f32_16x16x32_bf16 v[192:195], v[148:151], v[8:11], v[192:195]\n"
"  ds_read_b64_tr_b16 v[144:145], v251 offset:12288\n"
"  v_cvt_pk_bf16_f32 v240, v240, v241\n"
"  v_cvt_pk_bf16_f32 v241, v242, v243\n"
"  v_mfma_f32_16x16x32_bf16 v[196:199], v[148:151], v[12:15], v[196:199]\n"
"  ds_read_b64_tr_b16 v[146:147], v251 offset:14336\n"
"  v_pk_max_i16 v240, v240, 0\n"
"  v_cvt_pk_bf16_f32 v242, v232, v233\n"
"  s_waitcnt lgkmcnt(6)\n"
"  v_mfma_f32_16x16x32_bf16 v[192:195], v[152:155], v[16:19], v[192:195]\n"
"  ds_read_b64_tr_b16 v[148:149], v251 offset:12800\n"
"  v_pk_max_i16 v241, v241, 0\n"
"  v_mfma_f32_16x16x32_bf16 v[196:199], v[152:155], v[20:23], v[196:199]\n"
"  ds_read_b64_tr_b16 v[150:151], v251 offset:14848\n"
"  v_cvt_pk_bf16_f32 v243, v234, v235\n"
"  v_pk_max_i16 v242, v242, 0\n"
"  v_mfma_f32_16x16x32_bf16 v[192:195], v[156:159], v[24:27], v[192:195]\n"
"  ds_read_b64_tr_b16 v[152:153], v251 offset:16384\n"
"  v_pk_max_i16 v243, v243, 0\n"
"  v_cvt_pk_bf16_f32 v244, v244, v245\n"
"  v_mfma_f32_16x16x32_bf16 v[196:199], v[156:159], v[28:31], v[196:199]\n"
"  ds_read_b64_tr_b16 v[154:155], v251 offset:18432\n"
"  v_cvt_pk_bf16_f32 v245, v246, v247\n"
"  s_waitcnt lgkmcnt(6)\n"
"  v_mfma_f32_16x16x32_bf16 v[192:195], v[160:163], v[32:35], v[192:195]\n"
"  ds_read_b64_tr_b16 v[156:157], v251 offset:16896\n"
"  v_pk_max_i16 v244, v244, 0\n"
"  v_cvt_pk_bf16_f32 v246, v236, v237\n"
"  v_mfma_f32_16x16x32_bf16 v[196:199], v[160:163], v[36:39], v[196:199]\n"
"  ds_read_b64_tr_b16 v[158:159], v251 offset:18944\n"
"  v_pk_max_i16 v245, v245, 0\n"
"  v_cvt_pk_bf16_f32 v247, v238, v239\n"
"  v_mfma_f32_16x16x32_bf16 v[192:195], v[164:167], v[40:43], v[192:195]\n"
"  ds_read_b64_tr_b16 v[160:161], v251 offset:20480\n"
"  v_pk_max_i16 v246, v246, 0\n"
"  v_mfma_f32_16x16x32_bf16 v[196:199], v[164:167], v[44:47], v[196:199]\n"
"  ds_read_b64_tr_b16 v[162:163], v251 offset:22528\n"
"  v_pk_max_i16 v247, v247, 0\n"
"  s_waitcnt lgkmcnt(6)\n"
"  v_mfma_f32_16x16x32_bf16 v[232:235], v[144:147], v[0:3], 0\n"
"  ds_read_b64_tr_b16 v[164:165], v251 offset:20992\n"
"  v_mfma_f32_16x16x32_bf16 v[236:239], v[144:147], v[4:7], 0\n"
"  ds_read_b64_tr_b16 v[166:167], v251 offset:23040\n"
"  v_mfma_f32_16x16x32_bf16 v[232:235], v[148:151], v[8:11], v[232:235]\n"
"  ds_read_b64_tr_b16 v[144:145], v250 offset:24576\n"
"  v_mfma_f32_16x16x32_bf16 v[236:239], v[148:151], v[12:15], v[236:239]\n"
"  ds_read_b64_tr_b16 v[146:147], v250 offset:26624\n"
"  s_waitcnt lgkmcnt(6)\n"
"  v_mfma_f32_16x16x32_bf16 v[232:235], v[152:155], v[16:19], v[232:235]\n"
"  ds_read_b64_tr_b16 v[148:149], v251 offset:24576\n"
"  v_mfma_f32_16x16x32_bf16 v[236:239], v[152:155], v[20:23], v[236:239]\n"
"  ds_read_b64_tr_b16 v[150:151], v251 offset:26624\n"
"  v_mfma_f32_16x16x32_bf16 v[232:235], v[156:159], v[24:27], v[232:235]\n"
"  ds_read_b64_tr_b16 v[152:153], v250 offset:25088\n"
"  v_mfma_f32_16x16x32_bf16 v[236:239], v[156:159], v[28:31], v[236:239]\n"
"  ds_read_b64_tr_b16 v[154:155], v250 offset:27136\n"
"  s_waitcnt lgkmcnt(6)\n"
"  v_mfma_f32_16x16x32_bf16 v[232:235], v[160:163], v[32:35], v[232:235]\n"
"  ds_read_b64_tr_b16 v[156:157], v251 offset:25088\n"
"  v_mfma_f32_16x16x32_bf16 v[236:239], v[160:163], v[36:39], v[236:239]\n"
"  ds_read_b64_tr_b16 v[158:159], v251 offset:27136\n"
"  v_mfma_f32_16x16x32_bf16 v[232:235], v[164:167], v[40:43], v[232:235]\n"
"  ds_read_b32 v253, v252\n"
"  ds_read_b64_tr_b16 v[160:161], v250 offset:28672\n"
"  v_mfma_f32_16x16x32_bf16 v[236:239], v[164:167], v[44:47], v[236:239]\n"
"  ds_read_b64_tr_b16 v[162:163], v250 offset:30720\n"
"  s_waitcnt lgkmcnt(7)\n"
"  v_mfma_f32_16x16x32_bf16 v[192:195], v[144:147], v[48:51], v[192:195]\n"
"  ds_read_b64_tr_b16 v[164:165], v251 offset:28672\n"
"  v_mfma_f32_16x16x32_bf16 v[196:199], v[144:147], v[52:55], v[196:199]\n"
"  ds_read_b64_tr_b16 v[166:167], v251 offset:30720\n"
"  v_mfma_f32_16x16x32_bf16 v[232:235], v[148:151], v[48:51], v[232:235]\n"
"  ds_read_b64_tr_b16 v[144:145], v250 offset:29184\n"
"  v_mfma_f32_16x16x32_bf16 v[236:239], v[148:151], v[52:55], v[236:239]\n"
"  ds_read_b64_tr_b16 v[146:147], v250 offset:31232\n"
"  s_waitcnt lgkmcnt(7)\n"
"  v_mfma_f32_16x16x32_bf16 v[192:195], v[152:155], v[56:59], v[192:195]\n"
"  s_waitcnt lgkmcnt(5)\n"
"  v_cmp_gt_u32 vcc, 23, v253\n"
"  ds_read_b64_tr_b16 v[148:149], v251 offset:29184\n"
"  v_mfma_f32_16x16x32_bf16 v[196:199], v[152:155], v[60:63], v[196:199]\n"
"  ds_read_b64_tr_b16 v[150:151], v251 offset:31232\n"
"  ds_inc_u32 v252, v252 offset:1024\n"
"  s_cbranch_vccnz .Lc_sp22\n"
".Lc_bk22:\n"
"  v_mfma_f32_16x16x32_bf16 v[232:235], v[156:159], v[56:59], v[232:235]\n"
"  ds_read_b64_tr_b16 v[152:153], v250 offset:36864\n"
"  v_mfma_f32_16x16x32_bf16 v[236:239], v[156:159], v[60:63], v[236:239]\n"
"  ds_read_b64_tr_b16 v[154:155], v250 offset:38912\n"
"  s_waitcnt lgkmcnt(7)\n"
"  v_mfma_f32_16x16x32_bf16 v[192:195], v[160:163], v[64:67], v[192:195]\n"
"  ds_read_b64_tr_b16 v[156:157], v251 offset:36864\n"
"  v_mfma_f32_16x16x32_bf16 v[196:199], v[160:163], v[68:71], v[196:199]\n"
"  ds_read_b64_tr_b16 v[158:159], v251 offset:38912\n"
"  v_mfma_f32_16x16x32_bf16 v[232:235], v[164:167], v[64:67], v[232:235]\n"
"  ds_read_b64_tr_b16 v[160:161], v250 offset:37376\n"
"  v_mfma_f32_16x16x32_bf16 v[236:239], v[164:167], v[68:71], v[236:239]\n"
"  ds_read_b64_tr_b16 v[162:163], v250 offset:39424\n"
"  s_waitcnt lgkmcnt(6)\n"
"  v_mfma_f32_16x16x32_bf16 v[192:195], v[144:147], v[72:75], v[192:195]\n"
"  ds_read_b64_tr_b16 v[164:165], v251 offset:37376\n"
"  v_mfma_f32_16x16x32_bf16 v[196:199], v[144:147], v[76:79], v[196:199]\n"
"  ds_read_b64_tr_b16 v[166:167], v251 offset:39424\n"
"  v_mfma_f32_16x16x32_bf16 v[232:235], v[148:151], v[72:75], v[232:235]\n"
"  ds_read_b64_tr_b16 v[144:145], v250 offset:40960\n"
"  v_mfma_f32_16x16x32_bf16 v[236:239], v[148:151], v[76:79], v[236:239]\n"
"  ds_read_b64_tr_b16 v[146:147], v250 offset:43008\n"
"  s_waitcnt lgkmcnt(6)\n"
"  v_mfma_f32_16x16x32_bf16 v[192:195], v[152:155], v[80:83], v[192:195]\n"
"  ds_read_b64_tr_b16 v[148:149], v251 offset:40960\n"
"  v_mfma_f32_16x16x32_bf16 v[196:199], v[152:155], v[84:87], v[196:199]\n"
"  ds_read_b64_tr_b16 v[150:151], v251 offset:43008\n"
"  ds_read_b128 v[8:11], v255 offset:0\n"
"  v_mfma_f32_16x16x32_bf16 v[232:235], v[156:159], v[80:83], v[232:235]\n"
"  ds_read_b64_tr_b16 v[152:153], v250 offset:41472\n"
"  v_mfma_f32_16x16x32_bf16 v[236:239], v[156:159], v[84:87], v[236:239]\n"
"  ds_read_b64_tr_b16 v[154:155], v250 offset:43520\n"
"  ds_read_b128 v[12:15], v255 offset:2048\n"
"  s_waitcnt lgkmcnt(8)\n"
"  v_mfma_f32_16x16x32_bf16 v[192:195], v[160:163], v[88:91], v[192:195]\n"
"  ds_read_b64_tr_b16 v[156:157], v251 offset:41472\n"
"  v_mfma_f32_16x16x32_bf16 v[196:199], v[160:163], v[92:95], v[196:199]\n"
"  ds_read_b64_tr_b16 v[158:159], v251 offset:43520\n"
"  ds_read_b128 v[16:19], v255 offset:512\n"
"  v_mfma_f32_16x16x32_bf16 v[232:235], v[164:167], v[88:91], v[232:235]\n"
"  ds_read_b64_tr_b16 v[160:161], v250 offset:45056\n"
"  v_mfma_f32_16x16x32_bf16 v[236:239], v[164:167], v[92:95], v[236:239]\n"
"  ds_read_b64_tr_b16 v[162:163], v250 offset:47104\n"
"  ds_read_b128 v[20:23], v255 offset:2560\n"
"  s_waitcnt lgkmcnt(9)\n"
"  v_mfma_f32_16x16x32_bf16 v[192:195], v[144:147], v[96:99], v[192:195]\n"
"  ds_read_b64_tr_b16 v[164:165], v251 offset:45056\n"
"  v_mfma_f32_16x16x32_bf16 v[196:199], v[144:147], v[100:103], v[196:199]\n"
"  ds_read_b64_tr_b16 v[166:167], v251 offset:47104\n"
"  ds_read_b128 v[24:27], v255 offset:22528\n"
"  v_mfma_f32_16x16x32_bf16 v[232:235], v[148:151], v[96:99], v[232:235]\n"
"  ds_read_b64_tr_b16 v[144:145], v250 offset:45568\n"
"  v_mfma_f32_16x16x32_bf16 v[236:239], v[148:151], v[100:103], v[236:239]\n"
"  ds_read_b64_tr_b16 v[146:147], v250 offset:47616\n"
"  ds_read_b128 v[28:31], v255 offset:24576\n"
"  s_waitcnt lgkmcnt(9)\n"
"  v_mfma_f32_16x16x32_bf16 v[192:195], v[152:155], v[104:107], v[192:195]\n"
"  ds_read_b64_tr_b16 v[148:149], v251 offset:45568\n"
"  v_mfma_f32_16x16x32_bf16 v[196:199], v[152:155], v[108:111], v[196:199]\n"
"  ds_read_b64_tr_b16 v[150:151], v251 offset:47616\n"
"  ds_read_b128 v[32:35], v255 offset:23040\n"
"  v_mfma_f32_16x16x32_bf16 v[232:235], v[156:159], v[104:107], v[232:235]\n"
"  ds_read_b32 v253, v252\n"
"  ds_read_b64_tr_b16 v[152:153], v250 offset:49152\n"
"  v_mfma_f32_16x16x32_bf16 v[236:239], v[156:159], v[108:111], v[236:239]\n"
"  ds_read_b64_tr_b16 v[154:155], v250 offset:51200\n"
"  ds_read_b128 v[36:39], v255 offset:25088\n"
"  s_waitcnt lgkmcnt(10)\n"
"  v_mfma_f32_16x16x32_bf16 v[192:195], v[160:163], v[112:115], v[192:195]\n"
"  ds_read_b64_tr_b16 v[156:157], v251 offset:49152\n"
"  v_mfma_f32_16x16x32_bf16 v[196:199], v[160:163], v[116:119], v[196:199]\n"
"  ds_read_b64_tr_b16 v[158:159], v251 offset:51200\n"
"  v_mfma_f32_16x16x32_bf16 v[232:235], v[164:167], v[112:115], v[232:235]\n"
"  ds_read_b64_tr_b16 v[160:161], v250 offset:49664\n"
"  v_mfma_f32_16x16x32_bf16 v[236:239], v[164:167], v[116:119], v[236:239]\n"
"  ds_read_b64_tr_b16 v[162:163], v250 offset:51712\n"
"  s_waitcnt lgkmcnt(8)\n"
"  v_mfma_f32_16x16x32_bf16 v[192:195], v[144:147], v[120:123], v[192:195]\n"
"  s_waitcnt lgkmcnt(6)\n"
"  v_cmp_gt_u32 vcc, 24, v253\n"
"  ds_read_b64_tr_b16 v[164:165], v251 offset:49664\n"
"  v_mfma_f32_16x16x32_bf16 v[196:199], v[144:147], v[124:127], v[196:199]\n"
"  ds_read_b64_tr_b16 v[166:167], v251 offset:51712\n"
"  ds_inc_u32 v252, v252 offset:1024\n"
"  s_cbranch_vccnz .Lc_sp23\n"
".Lc_bk23:\n"
"  v_mfma_f32_16x16x32_bf16 v[232:235], v[148:151], v[120:123], v[232:235]\n"
"  ds_read_b64_tr_b16 v[0:1], v248 offset:0\n"
"  v_mfma_f32_16x16x32_bf16 v[236:239], v[148:151], v[124:127], v[236:239]\n"
"  ds_read_b64_tr_b16 v[2:3], v248 offset:2048\n"
"  s_waitcnt lgkmcnt(7)\n"
"  v_mfma_f32_16x16x32_bf16 v[192:195], v[152:155], v[128:131], v[192:195]\n"
"  ds_read_b64_tr_b16 v[4:5], v249 offset:0\n"
"  v_mfma_f32_16x16x32_bf16 v[196:199], v[152:155], v[132:135], v[196:199]\n"
"  ds_read_b64_tr_b16 v[6:7], v249 offset:2048\n"
"  v_mfma_f32_16x16x32_bf16 v[232:235], v[156:159], v[128:131], v[232:235]\n"
"  ds_read_b64_tr_b16 v[144:145], v248 offset:512\n"
"  v_mfma_f32_16x16x32_bf16 v[236:239], v[156:159], v[132:135], v[236:239]\n"
"  ds_read_b64_tr_b16 v[146:147], v248 offset:2560\n"
"  s_waitcnt lgkmcnt(6)\n"
"  v_mfma_f32_16x16x32_bf16 v[192:195], v[160:163], v[136:139], v[192:195]\n"
"  ds_read_b64_tr_b16 v[148:149], v249 offset:512\n"
"  v_mfma_f32_16x16x32_bf16 v[196:199], v[160:163], v[140:143], v[196:199]\n"
"  ds_read_b64_tr_b16 v[150:151], v249 offset:2560\n"
"  v_mfma_f32_16x16x32_bf16 v[232:235], v[164:167], v[136:139], v[232:235]\n"
"  ds_read_b64_tr_b16 v[152:153], v248 offset:4096\n"
"  v_mfma_f32_16x16x32_bf16 v[236:239], v[164:167], v[140:143], v[236:239]\n"
"  ds_read_b64_tr_b16 v[154:155], v248 offset:6144\n"
"  s_waitcnt lgkmcnt(6)\n"
"  v_mfma_f32_16x16x32_bf16 v[40:43], v[0:3], v[208:211], 0\n"
"  ds_read_b64_tr_b16 v[156:157], v249 offset:4096\n"
"  v_mfma_f32_16x16x32_bf16 v[44:47], v[0:3], v[212:215], 0\n"
"  ds_read_b64_tr_b16 v[158:159], v249 offset:6144\n"
"  v_mfma_f32_16x16x32_bf16 v[48:51], v[4:7], v[208:211], 0\n"
"  ds_read_b64_tr_b16 v[160:161], v248 offset:4608\n"
"  v_mfma_f32_16x16x32_bf16 v[52:55], v[4:7], v[212:215], 0\n"
"  ds_read_b64_tr_b16 v[162:163], v248 offset:6656\n"
"  v_cvt_pk_bf16_f32 v192, v192, v193\n"
"  s_waitcnt lgkmcnt(6)\n"
"  v_mfma_f32_16x16x32_bf16 v[56:59], v[144:147], v[208:211], 0\n"
"  ds_read_b64_tr_b16 v[164:165], v249 offset:4608\n"
"  v_mfma_f32_16x16x32_bf16 v[60:63], v[144:147], v[212:215], 0\n"
"  ds_read_b64_tr_b16 v[166:167], v249 offset:6656\n"
"  v_cvt_pk_bf16_f32 v193, v194, v195\n"
"  v_mfma_f32_16x16x32_bf16 v[64:67], v[148:151], v[208:211], 0\n"
"  ds_read_b64_tr_b16 v[0:1], v248 offset:8192\n"
"  v_mfma_f32_16x16x32_bf16 v[68:71], v[148:151], v[212:215], 0\n"
"  ds_read_b64_tr_b16 v[2:3], v248 offset:10240\n"
"  v_pk_max_i16 v192, v192, 0\n"
"  s_waitcnt lgkmcnt(6)\n"
"  v_mfma_f32_16x16x32_bf16 v[40:43], v[152:155], v[224:227], v[40:43]\n"
"  ds_read_b64_tr_b16 v[4:5], v249 offset:8192\n"
"  v_mfma_f32_16x16x32_bf16 v[44:47], v[152:155], v[228:231], v[44:47]\n"
"  ds_read_b64_tr_b16 v[6:7], v249 offset:10240\n"
"  v_cvt_pk_bf16_f32 v194, v232, v233\n"
"  v_mfma_f32_16x16x32_bf16 v[48:51], v[156:159], v[224:227], v[48:51]\n"
"  ds_read_b64_tr_b16 v[144:145], v248 offset:8704\n"
"  v_mfma_f32_16x16x32_bf16 v[52:55], v[156:159], v[228:231], v[52:55]\n"
"  ds_read_b64_tr_b16 v[146:147], v248 offset:10752\n"
"  v_pk_max_i16 v193, v193, 0\n"
"  s_waitcnt lgkmcnt(6)\n"
"  v_mfma_f32_16x16x32_bf16 v[56:59], v[160:163], v[224:227], v[56:59]\n"
"  ds_read_b64_tr_b16 v[148:149], v249 offset:8704\n"
"  v_mfma_f32_16x16x32_bf16 v[60:63], v[160:163], v[228:231], v[60:63]\n"
"  ds_read_b64_tr_b16 v[150:151], v249 offset:10752\n"
"  v_cvt_pk_bf16_f32 v195, v234, v235\n"
"  v_mfma_f32_16x16x32_bf16 v[64:67], v[164:167], v[224:227], v[64:67]\n"
"  ds_read_b64_tr_b16 v[152:153], v248 offset:12288\n"
"  v_mfma_f32_16x16x32_bf16 v[68:71], v[164:167], v[228:231], v[68:71]\n"
"  ds_read_b64_tr_b16 v[154:155], v248 offset:14336\n"
"  v_pk_max_i16 v194, v194, 0\n"
"  s_waitcnt lgkmcnt(6)\n"
"  v_mfma_f32_16x16x32_bf16 v[40:43], v[0:3], v[168:171], v[40:43]\n"
"  ds_read_b64_tr_b16 v[156:157], v249 offset:12288\n"
"  v_mfma_f32_16x16x32_bf16 v[44:47], v[0:3], v[172:175], v[44:47]\n"
"  ds_read_b64_tr_b16 v[158:159], v249 offset:14336\n"
"  v_pk_max_i16 v195, v195, 0\n"
"  v_mfma_f32_16x16x32_bf16 v[48:51], v[4:7], v[168:171], v[48:51]\n"
"  ds_read_b64_tr_b16 v[160:161], v248 offset:12800\n"
"  v_mfma_f32_16x16x32_bf16 v[52:55], v[4:7], v[172:175], v[52:55]\n"
"  ds_read_b64_tr_b16 v[162:163], v248 offset:14848\n"
"  v_cvt_pk_bf16_f32 v196, v196, v197\n"
"  s_waitcnt lgkmcnt(6)\n"
"  v_mfma_f32_16x16x32_bf16 v[56:59], v[144:147], v[168:171], v[56:59]\n"
"  ds_read_b64_tr_b16 v[164:165], v249 offset:12800\n"
"  v_mfma_f32_16x16x32_bf16 v[60:63], v[144:147], v[172:175], v[60:63]\n"
"  ds_read_b64_tr_b16 v[166:167], v249 offset:14848\n"
"  v_cvt_pk_bf16_f32 v197, v198, v199\n"
"  v_mfma_f32_16x16x32_bf16 v[64:67], v[148:151], v[168:171], v[64:67]\n"
"  ds_read_b64_tr_b16 v[0:1], v248 offset:16384\n"
"  v_mfma_f32_16x16x32_bf16 v[68:71], v[148:151], v[172:175], v[68:71]\n"
"  ds_read_b64_tr_b16 v[2:3], v248 offset:18432\n"
"  v_pk_max_i16 v196, v196, 0\n"
"  s_waitcnt lgkmcnt(6)\n"
"  v_mfma_f32_16x16x32_bf16 v[40:43], v[152:155], v[184:187], v[40:43]\n"
"  ds_read_b64_tr_b16 v[4:5], v249 offset:16384\n"
"  v_mfma_f32_16x16x32_bf16 v[44:47], v[152:155], v[188:191], v[44:47]\n"
"  ds_read_b64_tr_b16 v[6:7], v249 offset:18432\n"
"  v_cvt_pk_bf16_f32 v198, v236, v237\n"
"  v_mfma_f32_16x16x32_bf16 v[48:51], v[156:159], v[184:187], v[48:51]\n"
"  ds_read_b64_tr_b16 v[144:145], v248 offset:16896\n"
"  v_mfma_f32_16x16x32_bf16 v[52:55], v[156:159], v[188:191], v[52:55]\n"
"  ds_read_b64_tr_b16 v[146:147], v248 offset:18944\n"
"  v_pk_max_i16 v197, v197, 0\n"
"  s_waitcnt lgkmcnt(6)\n"
"  v_mfma_f32_16x16x32_bf16 v[56:59], v[160:163], v[184:187], v[56:59]\n"
"  ds_read_b64_tr_b16 v[148:149], v249 offset:16896\n"
"  v_mfma_f32_16x16x32_bf16 v[60:63], v[160:163], v[188:191], v[60:63]\n"
"  ds_read_b64_tr_b16 v[150:151], v249 offset:18944\n"
"  v_cvt_pk_bf16_f32 v199, v238, v239\n"
"  v_mfma_f32_16x16x32_bf16 v[64:67], v[164:167], v[184:187], v[64:67]\n"
"  ds_read_b32 v253, v252\n"
"  ds_read_b64_tr_b16 v[152:153], v248 offset:20480\n"
"  v_mfma_f32_16x16x32_bf16 v[68:71], v[164:167], v[188:191], v[68:71]\n"
"  ds_read_b64_tr_b16 v[154:155], v248 offset:22528\n"
"  v_pk_max_i16 v198, v198, 0\n"
"  s_waitcnt lgkmcnt(7)\n"
"  v_mfma_f32_16x16x32_bf16 v[40:43], v[0:3], v[200:203], v[40:43]\n"
"  ds_read_b64_tr_b16 v[156:157], v249 offset:20480\n"
"  v_mfma_f32_16x16x32_bf16 v[44:47], v[0:3], v[204:207], v[44:47]\n"
"  ds_read_b64_tr_b16 v[158:159], v249 offset:22528\n"
"  v_pk_max_i16 v199, v199, 0\n"
"  v_mfma_f32_16x16x32_bf16 v[48:51], v[4:7], v[200:203], v[48:51]\n"
"  ds_read_b64_tr_b16 v[160:161], v248 offset:20992\n"
"  v_mfma_f32_16x16x32_bf16 v[52:55], v[4:7], v[204:207], v[52:55]\n"
"  ds_read_b64_tr_b16 v[162:163], v248 offset:23040\n"
"  s_waitcnt lgkmcnt(7)\n"
"  v_mfma_f32_16x16x32_bf16 v[56:59], v[144:147], v[200:203], v[56:59]\n"
"  s_waitcnt lgkmcnt(5)\n"
"  v_cmp_gt_u32 vcc, 25, v253\n"
"  ds_read_b64_tr_b16 v[164:165], v249 offset:20992\n"
"  v_mfma_f32_16x16x32_bf16 v[60:63], v[144:147], v[204:207], v[60:63]\n"
"  ds_read_b64_tr_b16 v[166:167], v249 offset:23040\n"
"  ds_inc_u32 v252, v252 offset:1024\n"
"  s_cbranch_vccnz .Lc_sp24\n"
".Lc_bk24:\n"
"  v_mfma_f32_16x16x32_bf16 v[64:67], v[148:151], v[200:203], v[64:67]\n"
"  ds_read_b64_tr_b16 v[0:1], v248 offset:24576\n"
"  v_mfma_f32_16x16x32_bf16 v[68:71], v[148:151], v[204:207], v[68:71]\n"
"  ds_read_b64_tr_b16 v[2:3], v248 offset:26624\n"
"  s_waitcnt lgkmcnt(7)\n"
"  v_mfma_f32_16x16x32_bf16 v[40:43], v[152:155], v[216:219], v[40:43]\n"
"  ds_read_b64_tr_b16 v[4:5], v249 offset:24576\n"
"  v_add_u32 v72, 69632, v254\n"
"  v_mfma_f32_16x16x32_bf16 v[44:47], v[152:155], v[220:223], v[44:47]\n"
"  ds_read_b64_tr_b16 v[6:7], v249 offset:26624\n"
"  v_mfma_f32_16x16x32_bf16 v[48:51], v[156:159], v[216:219], v[48:51]\n"
"  ds_read_b64_tr_b16 v[144:145], v248 offset:25088\n"
"  v_mfma_f32_16x16x32_bf16 v[52:55], v[156:159], v[220:223], v[52:55]\n"
"  ds_read_b64_tr_b16 v[146:147], v248 offset:27136\n"
"  s_waitcnt lgkmcnt(6)\n"
"  v_mfma_f32_16x16x32_bf16 v[56:59], v[160:163], v[216:219], v[56:59]\n"
"  ds_read_b64_tr_b16 v[148:149], v249 offset:25088\n"
"  v_mfma_f32_16x16x32_bf16 v[60:63], v[160:163], v[220:223], v[60:63]\n"
"  ds_read_b64_tr_b16 v[150:151], v249 offset:27136\n"
"  v_mfma_f32_16x16x32_bf16 v[64:67], v[164:167], v[216:219], v[64:67]\n"
"  ds_read_b64_tr_b16 v[152:153], v248 offset:28672\n"
"  v_mfma_f32_16x16x32_bf16 v[68:71], v[164:167], v[220:223], v[68:71]\n"
"  ds_read_b64_tr_b16 v[154:155], v248 offset:30720\n"
"  s_waitcnt lgkmcnt(6)\n"
"  v_mfma_f32_16x16x32_bf16 v[40:43], v[0:3], v[176:179], v[40:43]\n"
"  ds_read_b64_tr_b16 v[156:157], v249 offset:28672\n"
"  v_mfma_f32_16x16x32_bf16 v[44:47], v[0:3], v[180:183], v[44:47]\n"
"  ds_read_b64_tr_b16 v[158:159], v249 offset:30720\n"
"  v_mfma_f32_16x16x32_bf16 v[48:51], v[4:7], v[176:179], v[48:51]\n"
"  ds_read_b64_tr_b16 v[160:161], v248 offset:29184\n"
"  v_mfma_f32_16x16x32_bf16 v[52:55], v[4:7], v[180:183], v[52:55]\n"
"  ds_read_b64_tr_b16 v[162:163], v248 offset:31232\n"
"  s_waitcnt lgkmcnt(6)\n"
"  v_mfma_f32_16x16x32_bf16 v[56:59], v[144:147], v[176:179], v[56:59]\n"
"  ds_read_b64_tr_b16 v[164:165], v249 offset:29184\n"
"  v_mfma_f32_16x16x32_bf16 v[60:63], v[144:147], v[180:183], v[60:63]\n"
"  ds_read_b64_tr_b16 v[166:167], v249 offset:31232\n"
"  v_mfma_f32_16x16x32_bf16 v[64:67], v[148:151], v[176:179], v[64:67]\n"
"  ds_read_b64_tr_b16 v[0:1], v248 offset:32768\n"
"  v_mfma_f32_16x16x32_bf16 v[68:71], v[148:151], v[180:183], v[68:71]\n"
"  ds_read_b64_tr_b16 v[2:3], v248 offset:34816\n"
"  s_waitcnt lgkmcnt(6)\n"
"  v_mfma_f32_16x16x32_bf16 v[40:43], v[152:155], v[240:243], v[40:43]\n"
"  ds_read_b64_tr_b16 v[4:5], v249 offset:32768\n"
"  v_mfma_f32_16x16x32_bf16 v[44:47], v[152:155], v[244:247], v[44:47]\n"
"  ds_read_b64_tr_b16 v[6:7], v249 offset:34816\n"
"  v_mfma_f32_16x16x32_bf16 v[48:51], v[156:159], v[240:243], v[48:51]\n"
"  ds_read_b64_tr_b16 v[144:145], v248 offset:33280\n"
"  v_mfma_f32_16x16x32_bf16 v[52:55], v[156:159], v[244:247], v[52:55]\n"
"  ds_read_b64_tr_b16 v[146:147], v248 offset:35328\n"
"  s_waitcnt lgkmcnt(6)\n"
"  v_mfma_f32_16x16x32_bf16 v[56:59], v[160:163], v[240:243], v[56:59]\n"
"  ds_read_b64_tr_b16 v[148:149], v249 offset:33280\n"
"  v_mfma_f32_16x16x32_bf16 v[60:63], v[160:163], v[244:247], v[60:63]\n"
"  ds_read_b64_tr_b16 v[150:151], v249 offset:35328\n"
"  v_mfma_f32_16x16x32_bf16 v[64:67], v[164:167], v[240:243], v[64:67]\n"
"  v_mfma_f32_16x16x32_bf16 v[68:71], v[164:167], v[244:247], v[68:71]\n"
"  ds_read_b128 v[152:155], v254 offset:0\n"
"  s_waitcnt lgkmcnt(5)\n"
"  v_mfma_f32_16x16x32_bf16 v[40:43], v[0:3], v[192:195], v[40:43]\n"
"  ds_read_b128 v[156:159], v254 offset:1024\n"
"  v_mfma_f32_16x16x32_bf16 v[44:47], v[0:3], v[196:199], v[44:47]\n"
"  v_mfma_f32_16x16x32_bf16 v[48:51], v[4:7], v[192:195], v[48:51]\n"
"  v_mfma_f32_16x16x32_bf16 v[52:55], v[4:7], v[196:199], v[52:55]\n"
"  ds_read_b128 v[160:163], v254 offset:2048\n"
"  s_waitcnt lgkmcnt(3)\n"
"  v_mfma_f32_16x16x32_bf16 v[56:59], v[144:147], v[192:195], v[56:59]\n"
"  ds_read_b128 v[164:167], v254 offset:3072\n"
"  v_mfma_f32_16x16x32_bf16 v[60:63], v[144:147], v[196:199], v[60:63]\n"
"  v_mfma_f32_16x16x32_bf16 v[64:67], v[148:151], v[192:195], v[64:67]\n"
"  ds_read_b32 v253, v252\n"
"  v_mfma_f32_16x16x32_bf16 v[68:71], v[148:151], v[196:199], v[68:71]\n"
"  ds_read_b128 v[0:3], v254 offset:4096\n"
"  s_waitcnt lgkmcnt(4)\n"
"  v_mfma_f32_16x16x32_bf16 v[40:43], v[152:155], v[8:11], v[40:43]\n"
"  ds_read_b128 v[4:7], v254 offset:5120\n"
"  v_mfma_f32_16x16x32_bf16 v[44:47], v[152:155], v[12:15], v[44:47]\n"
"  v_mfma_f32_16x16x32_bf16 v[48:51], v[156:159], v[8:11], v[48:51]\n"
"  v_mfma_f32_16x16x32_bf16 v[52:55], v[156:159], v[12:15], v[52:55]\n"
"  ds_read_b128 v[144:147], v254 offset:6144\n"
"  s_waitcnt lgkmcnt(4)\n"
"  v_mfma_f32_16x16x32_bf16 v[56:59], v[160:163], v[8:11], v[56:59]\n"
"  ds_read_b128 v[148:151], v254 offset:7168\n"
"  v_mfma_f32_16x16x32_bf16 v[60:63], v[160:163], v[12:15], v[60:63]\n"
"  v_mfma_f32_16x16x32_bf16 v[64:67], v[164:167], v[8:11], v[64:67]\n"
"  s_waitcnt lgkmcnt(2)\n"
"  v_cmp_gt_u32 vcc, 27, v253\n"
"  v_mfma_f32_16x16x32_bf16 v[68:71], v[164:167], v[12:15], v[68:71]\n"
"  ds_read_b128 v[152:155], v254 offset:8192\n"
"  s_cbranch_vccnz .Lc_spci\n"
".Lc_bkci:\n"
"  v_mfma_f32_16x16x32_bf16 v[40:43], v[0:3], v[16:19], v[40:43]\n"
"  ds_read_b128 v[156:159], v254 offset:9216\n"
"  ds_read_b128 v[80:83], v255 offset:0\n"
"  v_mfma_f32_16x16x32_bf16 v[44:47], v[0:3], v[20:23], v[44:47]\n"
"  ds_read_b128 v[84:87], v255 offset:2048\n"
"  v_mfma_f32_16x16x32_bf16 v[48:51], v[4:7], v[16:19], v[48:51]\n"
"  ds_read_b128 v[88:91], v255 offset:512\n"
"  v_mfma_f32_16x16x32_bf16 v[52:55], v[4:7], v[20:23], v[52:55]\n"
"  ds_read_b128 v[160:163], v254 offset:10240\n"
"  ds_read_b128 v[92:95], v255 offset:2560\n"
"  s_waitcnt lgkmcnt(7)\n"
"  v_mfma_f32_16x16x32_bf16 v[56:59], v[144:147], v[16:19], v[56:59]\n"
"  ds_read_b128 v[164:167], v254 offset:11264\n"
"  ds_read_b128 v[96:99], v255 offset:22528\n"
"  v_mfma_f32_16x16x32_bf16 v[60:63], v[144:147], v[20:23], v[60:63]\n"
"  ds_inc_u32 v252, v252 offset:1024\n"
"  ds_read_b128 v[100:103], v255 offset:24576\n"
"  v_mfma_f32_16x16x32_bf16 v[64:67], v[148:151], v[16:19], v[64:67]\n"
"  ds_read_b128 v[104:107], v255 offset:23040\n"
"  v_mfma_f32_16x16x32_bf16 v[68:71], v[148:151], v[20:23], v[68:71]\n"
"  ds_read_b128 v[0:3], v254 offset:12288\n"
"  ds_read_b128 v[108:111], v255 offset:25088\n"
"  s_waitcnt lgkmcnt(10)\n"
"  v_mfma_f32_16x16x32_bf16 v[40:43], v[152:155], v[24:27], v[40:43]\n"
"  ds_read_b128 v[4:7], v254 offset:13312\n"
"  v_mfma_f32_16x16x32_bf16 v[44:47], v[152:155], v[28:31], v[44:47]\n"
"  v_mfma_f32_16x16x32_bf16 v[48:51], v[156:159], v[24:27], v[48:51]\n"
"  v_mfma_f32_16x16x32_bf16 v[52:55], v[156:159], v[28:31], v[52:55]\n"
"  ds_read_b128 v[144:147], v254 offset:14336\n"
"  s_waitcnt lgkmcnt(5)\n"
"  v_mfma_f32_16x16x32_bf16 v[56:59], v[160:163], v[24:27], v[56:59]\n"
"  ds_read_b128 v[148:151], v254 offset:15360\n"
"  v_mfma_f32_16x16x32_bf16 v[60:63], v[160:163], v[28:31], v[60:63]\n"
"  v_mfma_f32_16x16x32_bf16 v[64:67], v[164:167], v[24:27], v[64:67]\n"
"  v_mfma_f32_16x16x32_bf16 v[68:71], v[164:167], v[28:31], v[68:71]\n"
"  ds_read_b128 v[152:155], v254 offset:16384\n"
"  s_waitcnt lgkmcnt(3)\n"
"  v_mfma_f32_16x16x32_bf16 v[40:43], v[0:3], v[32:35], v[40:43]\n"
"  ds_read_b128 v[156:159], v254 offset:17408\n"
"  v_mfma_f32_16x16x32_bf16 v[44:47], v[0:3], v[36:39], v[44:47]\n"
"  v_mfma_f32_16x16x32_bf16 v[48:51], v[4:7], v[32:35], v[48:51]\n"
"  v_mfma_f32_16x16x32_bf16 v[52:55], v[4:7], v[36:39], v[52:55]\n"
"  ds_read_b128 v[160:163], v254 offset:18432\n"
"  s_waitcnt lgkmcnt(3)\n"
"  v_mfma_f32_16x16x32_bf16 v[56:59], v[144:147], v[32:35], v[56:59]\n"
"  ds_read_b128 v[164:167], v254 offset:19456\n"
"  v_mfma_f32_16x16x32_bf16 v[60:63], v[144:147], v[36:39], v[60:63]\n"
"  v_mfma_f32_16x16x32_bf16 v[64:67], v[148:151], v[32:35], v[64:67]\n"
"  v_mfma_f32_16x16x32_bf16 v[68:71], v[148:151], v[36:39], v[68:71]\n"
"  ds_read_b128 v[0:3], v254 offset:20480\n"
"  s_waitcnt lgkmcnt(3)\n"
"  v_mfma_f32_16x16x32_bf16 v[40:43], v[152:155], v[80:83], v[40:43]\n"
"  ds_read_b128 v[4:7], v254 offset:21504\n"
"  v_mfma_f32_16x16x32_bf16 v[44:47], v[152:155], v[84:87], v[44:47]\n"
"  v_mfma_f32_16x16x32_bf16 v[48:51], v[156:159], v[80:83], v[48:51]\n"
"  v_mfma_f32_16x16x32_bf16 v[52:55], v[156:159], v[84:87], v[52:55]\n"
"  ds_read_b128 v[144:147], v254 offset:22528\n"
"  s_waitcnt lgkmcnt(3)\n"
"  v_mfma_f32_16x16x32_bf16 v[56:59], v[160:163], v[80:83], v[56:59]\n"
"  ds_read_b128 v[148:151], v254 offset:23552\n"
"  v_mfma_f32_16x16x32_bf16 v[60:63], v[160:163], v[84:87], v[60:63]\n"
"  v_mfma_f32_16x16x32_bf16 v[64:67], v[164:167], v[80:83], v[64:67]\n"
"  v_mfma_f32_16x16x32_bf16 v[68:71], v[164:167], v[84:87], v[68:71]\n"
"  ds_read_b128 v[152:155], v254 offset:24576\n"
"  s_waitcnt lgkmcnt(3)\n"
"  v_mfma_f32_16x16x32_bf16 v[40:43], v[0:3], v[88:91], v[40:43]\n"
"  ds_read_b128 v[156:159], v254 offset:25600\n"
"  v_mfma_f32_16x16x32_bf16 v[44:47], v[0:3], v[92:95], v[44:47]\n"
"  v_mfma_f32_16x16x32_bf16 v[48:51], v[4:7], v[88:91], v[48:51]\n"
"  v_mfma_f32_16x16x32_bf16 v[52:55], v[4:7], v[92:95], v[52:55]\n"
"  ds_read_b128 v[160:163], v254 offset:26624\n"
"  s_waitcnt lgkmcnt(3)\n"
"  v_mfma_f32_16x16x32_bf16 v[56:59], v[144:147], v[88:91], v[56:59]\n"
"  ds_read_b128 v[164:167], v254 offset:27648\n"
"  v_mfma_f32_16x16x32_bf16 v[60:63], v[144:147], v[92:95], v[60:63]\n"
"  v_mfma_f32_16x16x32_bf16 v[64:67], v[148:151], v[88:91], v[64:67]\n"
"  ds_read_b32 v253, v252\n"
"  v_mfma_f32_16x16x32_bf16 v[68:71], v[148:151], v[92:95], v[68:71]\n"
"  ds_read_b128 v[0:3], v254 offset:28672\n"
"  s_waitcnt lgkmcnt(4)\n"
"  v_mfma_f32_16x16x32_bf16 v[40:43], v[152:155], v[96:99], v[40:43]\n"
"  ds_read_b128 v[4:7], v254 offset:29696\n"
"  v_mfma_f32_16x16x32_bf16 v[44:47], v[152:155], v[100:103], v[44:47]\n"
"  v_mfma_f32_16x16x32_bf16 v[48:51], v[156:159], v[96:99], v[48:51]\n"
"  v_mfma_f32_16x16x32_bf16 v[52:55], v[156:159], v[100:103], v[52:55]\n"
"  ds_read_b128 v[144:147], v254 offset:30720\n"
"  s_waitcnt lgkmcnt(4)\n"
"  v_mfma_f32_16x16x32_bf16 v[56:59], v[160:163], v[96:99], v[56:59]\n"
"  s_waitcnt lgkmcnt(3)\n"
"  v_cmp_gt_u32 vcc, 28, v253\n"
"  ds_read_b128 v[148:151], v254 offset:31744\n"
"  v_mfma_f32_16x16x32_bf16 v[60:63], v[160:163], v[100:103], v[60:63]\n"
"  ds_inc_u32 v252, v252 offset:1024\n"
"  s_cbranch_vccnz .Lc_sp26\n"
".Lc_bk26:\n"
"  v_mfma_f32_16x16x32_bf16 v[64:67], v[164:167], v[96:99], v[64:67]\n"
"  ds_read_b64_tr_b16 v[152:153], v250 offset:8192\n"
"  v_mfma_f32_16x16x32_bf16 v[68:71], v[164:167], v[100:103], v[68:71]\n"
"  ds_read_b64_tr_b16 v[154:155], v250 offset:10240\n"
"  s_waitcnt lgkmcnt(5)\n"
"  v_mfma_f32_16x16x32_bf16 v[40:43], v[0:3], v[104:107], v[40:43]\n"
"  ds_read_b64_tr_b16 v[156:157], v251 offset:8192\n"
"  v_mfma_f32_16x16x32_bf16 v[44:47], v[0:3], v[108:111], v[44:47]\n"
"  ds_read_b64_tr_b16 v[158:159], v251 offset:10240\n"
"  v_mfma_f32_16x16x32_bf16 v[48:51], v[4:7], v[104:107], v[48:51]\n"
"  ds_read_b64_tr_b16 v[160:161], v250 offset:8704\n"
"  v_mfma_f32_16x16x32_bf16 v[52:55], v[4:7], v[108:111], v[52:55]\n"
"  ds_read_b64_tr_b16 v[162:163], v250 offset:10752\n"
"  s_waitcnt lgkmcnt(6)\n"
"  v_mfma_f32_16x16x32_bf16 v[56:59], v[144:147], v[104:107], v[56:59]\n"
"  ds_read_b64_tr_b16 v[164:165], v251 offset:8704\n"
"  v_mfma_f32_16x16x32_bf16 v[60:63], v[144:147], v[108:111], v[60:63]\n"
"  ds_read_b64_tr_b16 v[166:167], v251 offset:10752\n"
"  v_mfma_f32_16x16x32_bf16 v[64:67], v[148:151], v[104:107], v[64:67]\n"
"  ds_read_b64_tr_b16 v[0:1], v250 offset:12288\n"
"  v_mfma_f32_16x16x32_bf16 v[68:71], v[148:151], v[108:111], v[68:71]\n"
"  ds_read_b64_tr_b16 v[2:3], v250 offset:14336\n"
".Lc_loop:\n"
"  s_waitcnt lgkmcnt(6)\n"
"  v_mfma_f32_16x16x32_bf16 v[112:115], v[152:155], v[208:211], 0\n"
"  ds_read_b64_tr_b16 v[4:5], v251 offset:12288\n"
"  v_mfma_f32_16x16x32_bf16 v[116:119], v[152:155], v[212:215], 0\n"
"  ds_read_b64_tr_b16 v[6:7], v251 offset:14336\n"
"  v_mfma_f32_16x16x32_bf16 v[120:123], v[156:159], v[208:211], 0\n"
"  ds_read_b64_tr_b16 v[144:145], v250 offset:12800\n"
"  v_cvt_pk_bf16_f32 v40, v40, v41\n"
"  v_mfma_f32_16x16x32_bf16 v[124:127], v[156:159], v[212:215], 0\n"
"  ds_read_b64_tr_b16 v[146:147], v250 offset:14848\n"
"  v_cvt_pk_bf16_f32 v41, v42, v43\n"
"  s_waitcnt lgkmcnt(6)\n"
"  v_mfma_f32_16x16x32_bf16 v[128:131], v[160:163], v[208:211], 0\n"
"  ds_read_b64_tr_b16 v[148:149], v251 offset:12800\n"
"  v_cvt_pk_bf16_f32 v42, v48, v49\n"
"  v_mfma_f32_16x16x32_bf16 v[132:135], v[160:163], v[212:215], 0\n"
"  ds_read_b64_tr_b16 v[150:151], v251 offset:14848\n"
"  v_cvt_pk_bf16_f32 v43, v50, v51\n"
"  v_mfma_f32_16x16x32_bf16 v[136:139], v[164:167], v[208:211], 0\n"
"  ds_read_b64_tr_b16 v[152:153], v250 offset:16384\n"
"  buffer_store_dwordx4 v[40:43], v254, s[40:43], s47 offen sc1\n"
"  v_mfma_f32_16x16x32_bf16 v[140:143], v[164:167], v[212:215], 0\n"
"  ds_read_b64_tr_b16 v[154:155], v250 offset:18432\n"
"  s_add_u32 s47, s47, 16384\n"
"  s_waitcnt lgkmcnt(6)\n"
"  v_mfma_f32_16x16x32_bf16 v[112:115], v[0:3], v[224:227], v[112:115]\n"
"  ds_read_b64_tr_b16 v[156:157], v251 offset:16384\n"
"  v_cvt_pk_bf16_f32 v44, v44, v45\n"
"  v_mfma_f32_16x16x32_bf16 v[116:119], v[0:3], v[228:231], v[116:119]\n"
"  ds_read_b64_tr_b16 v[158:159], v251 offset:18432\n"
"  v_cvt_pk_bf16_f32 v45, v46, v47\n"
"  v_mfma_f32_16x16x32_bf16 v[120:123], v[4:7], v[224:227], v[120:123]\n"
"  ds_read_b64_tr_b16 v[160:161], v250 offset:16896\n"
"  v_cvt_pk_bf16_f32 v46, v52, v53\n"
"  v_mfma_f32_16x16x32_bf16 v[124:127], v[4:7], v[228:231], v[124:127]\n"
"  ds_read_b64_tr_b16 v[162:163], v250 offset:18944\n"
"  v_cvt_pk_bf16_f32 v47, v54, v55\n"
"  s_waitcnt lgkmcnt(6)\n"
"  v_mfma_f32_16x16x32_bf16 v[128:131], v[144:147], v[224:227], v[128:131]\n"
"  ds_read_b64_tr_b16 v[164:165], v251 offset:16896\n"
"  buffer_store_dwordx4 v[44:47], v254, s[40:43], s47 offen sc1\n"
"  v_mfma_f32_16x16x32_bf16 v[132:135], v[144:147], v[228:231], v[132:135]\n"
"  ds_read_b64_tr_b16 v[166:167], v251 offset:18944\n"
"  s_add_u32 s47, s47, 16384\n"
"  v_mfma_f32_16x16x32_bf16 v[136:139], v[148:151], v[224:227], v[136:139]\n"
"  ds_read_b64_tr_b16 v[0:1], v250 offset:20480\n"
"  v_cvt_pk_bf16_f32 v56, v56, v57\n"
"  v_mfma_f32_16x16x32_bf16 v[140:143], v[148:151], v[228:231], v[140:143]\n"
"  ds_read_b64_tr_b16 v[2:3], v250 offset:22528\n"
"  v_cvt_pk_bf16_f32 v57, v58, v59\n"
"  s_waitcnt lgkmcnt(6)\n"
"  v_mfma_f32_16x16x32_bf16 v[112:115], v[152:155], v[168:171], v[112:115]\n"
"  ds_read_b64_tr_b16 v[4:5], v251 offset:20480\n"
"  v_cvt_pk_bf16_f32 v58, v64, v65\n"
"  v_mfma_f32_16x16x32_bf16 v[116:119], v[152:155], v[172:175], v[116:119]\n"
"  ds_read_b64_tr_b16 v[6:7], v251 offset:22528\n"
"  v_cvt_pk_bf16_f32 v59, v66, v67\n"
"  v_mfma_f32_16x16x32_bf16 v[120:123], v[156:159], v[168:171], v[120:123]\n"
"  ds_read_b64_tr_b16 v[144:145], v250 offset:20992\n"
"  buffer_store_dwordx4 v[56:59], v254, s[40:43], s47 offen sc1\n"
"  v_mfma_f32_16x16x32_bf16 v[124:127], v[156:159], v[172:175], v[124:127]\n"
"  ds_read_b64_tr_b16 v[146:147], v250 offset:23040\n"
"  s_add_u32 s47, s47, 16384\n"
"  s_waitcnt lgkmcnt(6)\n"
"  v_mfma_f32_16x16x32_bf16 v[128:131], v[160:163], v[168:171], v[128:131]\n"
"  ds_read_b64_tr_b16 v[148:149], v251 offset:20992\n"
"  v_cvt_pk_bf16_f32 v60, v60, v61\n"
"  v_mfma_f32_16x16x32_bf16 v[132:135], v[160:163], v[172:175], v[132:135]\n"
"  ds_read_b64_tr_b16 v[150:151], v251 offset:23040\n"
"  v_cvt_pk_bf16_f32 v61, v62, v63\n"
"  v_mfma_f32_16x16x32_bf16 v[136:139], v[164:167], v[168:171], v[136:139]\n"
"  ds_read_b64_tr_b16 v[152:153], v250 offset:24576\n"
"  v_cvt_pk_bf16_f32 v62, v68, v69\n"
"  v_mfma_f32_16x16x32_bf16 v[140:143], v[164:167], v[172:175], v[140:143]\n"
"  ds_read_b64_tr_b16 v[154:155], v250 offset:26624\n"
"  v_cvt_pk_bf16_f32 v63, v70, v71\n"
"  s_waitcnt lgkmcnt(6)\n"
"  v_mfma_f32_16x16x32_bf16 v[112:115], v[0:3], v[184:187], v[112:115]\n"
"  ds_read_b64_tr_b16 v[156:157], v251 offset:24576\n"
"  buffer_store_dwordx4 v[60:63], v254, s[40:43], s47 offen sc1\n"
"  v_mfma_f32_16x16x32_bf16 v[116:119], v[0:3], v[188:191], v[116:119]\n"
"  ds_read_b64_tr_b16 v[158:159], v251 offset:26624\n"
"  s_add_u32 s47, s47, 212992\n"
"  v_mfma_f32_16x16x32_bf16 v[120:123], v[4:7], v[184:187], v[120:123]\n"
"  ds_read_b64_tr_b16 v[160:161], v250 offset:25088\n"
"  v_mfma_f32_16x16x32_bf16 v[124:127], v[4:7], v[188:191], v[124:127]\n"
"  ds_read_b64_tr_b16 v[162:163], v250 offset:27136\n"
"  s_waitcnt lgkmcnt(6)\n"
"  v_mfma_f32_16x16x32_bf16 v[128:131], v[144:147], v[184:187], v[128:131]\n"
"  ds_read_b64_tr_b16 v[164:165], v251 offset:25088\n"
"  v_mfma_f32_16x16x32_bf16 v[132:135], v[144:147], v[188:191], v[132:135]\n"
"  ds_read_b64_tr_b16 v[166:167], v251 offset:27136\n"
"  v_mfma_f32_16x16x32_bf16 v[136:139], v[148:151], v[184:187], v[136:139]\n"
"  ds_read_b32 v253, v252\n"
"  ds_read_b64_tr_b16 v[0:1], v250 offset:28672\n"
"  v_mfma_f32_16x16x32_bf16 v[140:143], v[148:151], v[188:191], v[140:143]\n"
"  ds_read_b64_tr_b16 v[2:3], v250 offset:30720\n"
"  s_waitcnt lgkmcnt(7)\n"
"  v_mfma_f32_16x16x32_bf16 v[112:115], v[152:155], v[200:203], v[112:115]\n"
"  ds_read_b64_tr_b16 v[4:5], v251 offset:28672\n"
"  v_mfma_f32_16x16x32_bf16 v[116:119], v[152:155], v[204:207], v[116:119]\n"
"  ds_read_b64_tr_b16 v[6:7], v251 offset:30720\n"
"  v_mfma_f32_16x16x32_bf16 v[120:123], v[156:159], v[200:203], v[120:123]\n"
"  ds_read_b64_tr_b16 v[144:145], v250 offset:29184\n"
"  v_mfma_f32_16x16x32_bf16 v[124:127], v[156:159], v[204:207], v[124:127]\n"
"  ds_read_b64_tr_b16 v[146:147], v250 offset:31232\n"
"  s_waitcnt lgkmcnt(7)\n"
"  v_mfma_f32_16x16x32_bf16 v[128:131], v[160:163], v[200:203], v[128:131]\n"
"  s_add_u32 s48, s48, 1\n"
"  s_waitcnt lgkmcnt(5)\n"
"  v_cmp_gt_u32 vcc, s48, v253\n"
"  ds_read_b64_tr_b16 v[148:149], v251 offset:29184\n"
"  v_mfma_f32_16x16x32_bf16 v[132:135], v[160:163], v[204:207], v[132:135]\n"
"  ds_read_b64_tr_b16 v[150:151], v251 offset:31232\n"
"  ds_inc_u32 v252, v252 offset:1024\n"
"  s_cbranch_vccnz .Lc_sp27\n"
".Lc_bk27:\n"
"  v_mfma_f32_16x16x32_bf16 v[136:139], v[164:167], v[200:203], v[136:139]\n"
"  ds_read_b64_tr_b16 v[152:153], v250 offset:32768\n"
"  v_mfma_f32_16x16x32_bf16 v[140:143], v[164:167], v[204:207], v[140:143]\n"
"  ds_read_b64_tr_b16 v[154:155], v250 offset:34816\n"
"  s_waitcnt lgkmcnt(7)\n"
"  v_mfma_f32_16x16x32_bf16 v[112:115], v[0:3], v[216:219], v[112:115]\n"
"  ds_read_b64_tr_b16 v[156:157], v251 offset:32768\n"
"  v_mfma_f32_16x16x32_bf16 v[116:119], v[0:3], v[220:223], v[116:119]\n"
"  ds_read_b64_tr_b16 v[158:159], v251 offset:34816\n"
"  v_mfma_f32_16x16x32_bf16 v[120:123], v[4:7], v[216:219], v[120:123]\n"
"  ds_read_b64_tr_b16 v[160:161], v250 offset:33280\n"
"  v_mfma_f32_16x16x32_bf16 v[124:127], v[4:7], v[220:223], v[124:127]\n"
"  ds_read_b64_tr_b16 v[162:163], v250 offset:35328\n"
"  s_waitcnt lgkmcnt(6)\n"
"  v_mfma_f32_16x16x32_bf16 v[128:131], v[144:147], v[216:219], v[128:131]\n"
"  ds_read_b64_tr_b16 v[164:165], v251 offset:33280\n"
"  v_mfma_f32_16x16x32_bf16 v[132:135], v[144:147], v[220:223], v[132:135]\n"
"  ds_read_b64_tr_b16 v[166:167], v251 offset:35328\n"
"  v_mfma_f32_16x16x32_bf16 v[136:139], v[148:151], v[216:219], v[136:139]\n"
"  ds_read_b64_tr_b16 v[0:1], v250 offset:36864\n"
"  v_mfma_f32_16x16x32_bf16 v[140:143], v[148:151], v[220:223], v[140:143]\n"
"  ds_read_b64_tr_b16 v[2:3], v250 offset:38912\n"
"  s_waitcnt lgkmcnt(6)\n"
"  v_mfma_f32_16x16x32_bf16 v[112:115], v[152:155], v[176:179], v[112:115]\n"
"  ds_read_b64_tr_b16 v[4:5], v251 offset:36864\n"
"  v_mfma_f32_16x16x32_bf16 v[116:119], v[152:155], v[180:183], v[116:119]\n"
"  ds_read_b64_tr_b16 v[6:7], v251 offset:38912\n"
"  v_mfma_f32_16x16x32_bf16 v[120:123], v[156:159], v[176:179], v[120:123]\n"
"  ds_read_b64_tr_b16 v[144:145], v250 offset:37376\n"
"  v_mfma_f32_16x16x32_bf16 v[124:127], v[156:159], v[180:183], v[124:127]\n"
"  ds_read_b64_tr_b16 v[146:147], v250 offset:39424\n"
"  s_waitcnt lgkmcnt(6)\n"
"  v_mfma_f32_16x16x32_bf16 v[128:131], v[160:163], v[176:179], v[128:131]\n"
"  ds_read_b64_tr_b16 v[148:149], v251 offset:37376\n"
"  v_mfma_f32_16x16x32_bf16 v[132:135], v[160:163], v[180:183], v[132:135]\n"
"  ds_read_b64_tr_b16 v[150:151], v251 offset:39424\n"
"  v_mfma_f32_16x16x32_bf16 v[136:139], v[164:167], v[176:179], v[136:139]\n"
"  ds_read_b64_tr_b16 v[152:153], v250 offset:40960\n"
"  v_mfma_f32_16x16x32_bf16 v[140:143], v[164:167], v[180:183], v[140:143]\n"
"  ds_read_b64_tr_b16 v[154:155], v250 offset:43008\n"
"  s_waitcnt lgkmcnt(6)\n"
"  v_mfma_f32_16x16x32_bf16 v[112:115], v[0:3], v[240:243], v[112:115]\n"
"  ds_read_b64_tr_b16 v[156:157], v251 offset:40960\n"
"  v_mfma_f32_16x16x32_bf16 v[116:119], v[0:3], v[244:247], v[116:119]\n"
"  ds_read_b64_tr_b16 v[158:159], v251 offset:43008\n"
"  v_mfma_f32_16x16x32_bf16 v[120:123], v[4:7], v[240:243], v[120:123]\n"
"  ds_read_b64_tr_b16 v[160:161], v250 offset:41472\n"
"  v_mfma_f32_16x16x32_bf16 v[124:127], v[4:7], v[244:247], v[124:127]\n"
"  ds_read_b64_tr_b16 v[162:163], v250 offset:43520\n"
"  s_waitcnt lgkmcnt(6)\n"
"  v_mfma_f32_16x16x32_bf16 v[128:131], v[144:147], v[240:243], v[128:131]\n"
"  ds_read_b64_tr_b16 v[164:165], v251 offset:41472\n"
"  v_mfma_f32_16x16x32_bf16 v[132:135], v[144:147], v[244:247], v[132:135]\n"
"  ds_read_b64_tr_b16 v[166:167], v251 offset:43520\n"
"  v_mfma_f32_16x16x32_bf16 v[136:139], v[148:151], v[240:243], v[136:139]\n"
"  v_mfma_f32_16x16x32_bf16 v[140:143], v[148:151], v[244:247], v[140:143]\n"
"  ds_read_b128 v[0:3], v72 offset:0\n"
"  s_waitcnt lgkmcnt(5)\n"
"  v_mfma_f32_16x16x32_bf16 v[112:115], v[152:155], v[192:195], v[112:115]\n"
"  ds_read_b128 v[4:7], v72 offset:1024\n"
"  v_mfma_f32_16x16x32_bf16 v[116:119], v[152:155], v[196:199], v[116:119]\n"
"  v_mfma_f32_16x16x32_bf16 v[120:123], v[156:159], v[192:195], v[120:123]\n"
"  v_mfma_f32_16x16x32_bf16 v[124:127], v[156:159], v[196:199], v[124:127]\n"
"  ds_read_b128 v[144:147], v72 offset:2048\n"
"  s_waitcnt lgkmcnt(3)\n"
"  v_mfma_f32_16x16x32_bf16 v[128:131], v[160:163], v[192:195], v[128:131]\n"
"  ds_read_b128 v[148:151], v72 offset:3072\n"
"  v_mfma_f32_16x16x32_bf16 v[132:135], v[160:163], v[196:199], v[132:135]\n"
"  v_mfma_f32_16x16x32_bf16 v[136:139], v[164:167], v[192:195], v[136:139]\n"
"  v_mfma_f32_16x16x32_bf16 v[140:143], v[164:167], v[196:199], v[140:143]\n"
"  ds_read_b128 v[152:155], v72 offset:4096\n"
"  s_waitcnt lgkmcnt(3)\n"
"  v_mfma_f32_16x16x32_bf16 v[112:115], v[0:3], v[8:11], v[112:115]\n"
"  ds_read_b128 v[156:159], v72 offset:5120\n"
"  v_mfma_f32_16x16x32_bf16 v[116:119], v[0:3], v[12:15], v[116:119]\n"
"  v_mfma_f32_16x16x32_bf16 v[120:123], v[4:7], v[8:11], v[120:123]\n"
"  v_mfma_f32_16x16x32_bf16 v[124:127], v[4:7], v[12:15], v[124:127]\n"
"  ds_read_b128 v[160:163], v72 offset:6144\n"
"  s_waitcnt lgkmcnt(3)\n"
"  v_mfma_f32_16x16x32_bf16 v[128:131], v[144:147], v[8:11], v[128:131]\n"
"  ds_read_b128 v[164:167], v72 offset:7168\n"
"  v_mfma_f32_16x16x32_bf16 v[132:135], v[144:147], v[12:15], v[132:135]\n"
"  v_mfma_f32_16x16x32_bf16 v[136:139], v[148:151], v[8:11], v[136:139]\n"
"  ds_read_b32 v253, v252\n"
"  v_mfma_f32_16x16x32_bf16 v[140:143], v[148:151], v[12:15], v[140:143]\n"
"  ds_read_b128 v[0:3], v72 offset:8192\n"
"  s_waitcnt lgkmcnt(4)\n"
"  v_mfma_f32_16x16x32_bf16 v[112:115], v[152:155], v[16:19], v[112:115]\n"
"  ds_read_b128 v[4:7], v72 offset:9216\n"
"  v_mfma_f32_16x16x32_bf16 v[116:119], v[152:155], v[20:23], v[116:119]\n"
"  v_mfma_f32_16x16x32_bf16 v[120:123], v[156:159], v[16:19], v[120:123]\n"
"  v_mfma_f32_16x16x32_bf16 v[124:127], v[156:159], v[20:23], v[124:127]\n"
"  ds_read_b128 v[144:147], v72 offset:10240\n"
"  s_waitcnt lgkmcnt(4)\n"
"  v_mfma_f32_16x16x32_bf16 v[128:131], v[160:163], v[16:19], v[128:131]\n"
"  s_add_u32 s48, s48, 1\n"
"  s_waitcnt lgkmcnt(3)\n"
"  v_cmp_gt_u32 vcc, s48, v253\n"
"  ds_read_b128 v[148:151], v72 offset:11264\n"
"  s_waitcnt vmcnt(0)\n"
"  ds_inc_rtn_u32 v73, v72, v252 offset:34820\n"
"  v_mfma_f32_16x16x32_bf16 v[132:135], v[160:163], v[20:23], v[132:135]\n"
"  ds_inc_u32 v252, v252 offset:1024\n"
"  s_cbranch_vccnz .Lc_sp28\n"
".Lc_bk28:\n"
"  v_mfma_f32_16x16x32_bf16 v[136:139], v[164:167], v[16:19], v[136:139]\n"
"  v_mfma_f32_16x16x32_bf16 v[140:143], v[164:167], v[20:23], v[140:143]\n"
"  ds_read_b128 v[152:155], v72 offset:12288\n"
"  s_waitcnt lgkmcnt(5)\n"
"  v_mfma_f32_16x16x32_bf16 v[112:115], v[0:3], v[24:27], v[112:115]\n"
"  ds_read_b128 v[156:159], v72 offset:13312\n"
"  v_lshlrev_b32 v74, 4, v254\n"
"  v_mfma_f32_16x16x32_bf16 v[116:119], v[0:3], v[28:31], v[116:119]\n"
"  v_mfma_f32_16x16x32_bf16 v[120:123], v[4:7], v[24:27], v[120:123]\n"
"  v_mfma_f32_16x16x32_bf16 v[124:127], v[4:7], v[28:31], v[124:127]\n"
"  ds_read_b128 v[160:163], v72 offset:14336\n"
"  s_waitcnt lgkmcnt(3)\n"
"  v_mfma_f32_16x16x32_bf16 v[128:131], v[144:147], v[24:27], v[128:131]\n"
"  ds_read_b128 v[164:167], v72 offset:15360\n"
"  v_lshl_add_u32 v74, v73, 30, v74\n"
"  v_mfma_f32_16x16x32_bf16 v[132:135], v[144:147], v[28:31], v[132:135]\n"
"  buffer_store_dword v73, v74, s[96:99], 0 offen sc1\n"
"  v_mfma_f32_16x16x32_bf16 v[136:139], v[148:151], v[24:27], v[136:139]\n"
"  v_mfma_f32_16x16x32_bf16 v[140:143], v[148:151], v[28:31], v[140:143]\n"
"  ds_read_b128 v[0:3], v72 offset:16384\n"
"  s_add_u32 s96, s96, 16384\n"
"  s_addc_u32 s97, s97, 0\n"
"  s_waitcnt lgkmcnt(3)\n"
"  v_mfma_f32_16x16x32_bf16 v[112:115], v[152:155], v[32:35], v[112:115]\n"
"  ds_read_b128 v[4:7], v72 offset:17408\n"
"  v_mfma_f32_16x16x32_bf16 v[116:119], v[152:155], v[36:39], v[116:119]\n"
"  v_mfma_f32_16x16x32_bf16 v[120:123], v[156:159], v[32:35], v[120:123]\n"
"  v_mfma_f32_16x16x32_bf16 v[124:127], v[156:159], v[36:39], v[124:127]\n"
"  ds_read_b128 v[144:147], v72 offset:18432\n"
"  s_waitcnt lgkmcnt(3)\n"
"  v_mfma_f32_16x16x32_bf16 v[128:131], v[160:163], v[32:35], v[128:131]\n"
"  ds_read_b128 v[148:151], v72 offset:19456\n"
"  v_mfma_f32_16x16x32_bf16 v[132:135], v[160:163], v[36:39], v[132:135]\n"
"  v_mfma_f32_16x16x32_bf16 v[136:139], v[164:167], v[32:35], v[136:139]\n"
"  v_mfma_f32_16x16x32_bf16 v[140:143], v[164:167], v[36:39], v[140:143]\n"
"  ds_read_b128 v[152:155], v72 offset:20480\n"
"  s_waitcnt lgkmcnt(3)\n"
"  v_mfma_f32_16x16x32_bf16 v[112:115], v[0:3], v[80:83], v[112:115]\n"
"  ds_read_b128 v[156:159], v72 offset:21504\n"
"  v_mfma_f32_16x16x32_bf16 v[116:119], v[0:3], v[84:87], v[116:119]\n"
"  v_mfma_f32_16x16x32_bf16 v[120:123], v[4:7], v[80:83], v[120:123]\n"
"  v_mfma_f32_16x16x32_bf16 v[124:127], v[4:7], v[84:87], v[124:127]\n"
"  ds_read_b128 v[160:163], v72 offset:22528\n"
"  s_waitcnt lgkmcnt(3)\n"
"  v_mfma_f32_16x16x32_bf16 v[128:131], v[144:147], v[80:83], v[128:131]\n"
"  ds_read_b128 v[164:167], v72 offset:23552\n"
"  v_mfma_f32_16x16x32_bf16 v[132:135], v[144:147], v[84:87], v[132:135]\n"
"  v_mfma_f32_16x16x32_bf16 v[136:139], v[148:151], v[80:83], v[136:139]\n"
"  v_mfma_f32_16x16x32_bf16 v[140:143], v[148:151], v[84:87], v[140:143]\n"
"  ds_read_b128 v[0:3], v72 offset:24576\n"
"  s_waitcnt lgkmcnt(3)\n"
"  v_mfma_f32_16x16x32_bf16 v[112:115], v[152:155], v[88:91], v[112:115]\n"
"  ds_read_b128 v[4:7], v72 offset:25600\n"
"  v_mfma_f32_16x16x32_bf16 v[116:119], v[152:155], v[92:95], v[116:119]\n"
"  v_mfma_f32_16x16x32_bf16 v[120:123], v[156:159], v[88:91], v[120:123]\n"
"  v_mfma_f32_16x16x32_bf16 v[124:127], v[156:159], v[92:95], v[124:127]\n"
"  ds_read_b128 v[144:147], v72 offset:26624\n"
"  s_waitcnt lgkmcnt(3)\n"
"  v_mfma_f32_16x16x32_bf16 v[128:131], v[160:163], v[88:91], v[128:131]\n"
"  ds_read_b128 v[148:151], v72 offset:27648\n"
"  v_mfma_f32_16x16x32_bf16 v[132:135], v[160:163], v[92:95], v[132:135]\n"
"  v_mfma_f32_16x16x32_bf16 v[136:139], v[164:167], v[88:91], v[136:139]\n"
"  ds_read_b32 v253, v252\n"
"  v_mfma_f32_16x16x32_bf16 v[140:143], v[164:167], v[92:95], v[140:143]\n"
"  ds_read_b128 v[152:155], v72 offset:28672\n"
"  s_waitcnt lgkmcnt(4)\n"
"  v_mfma_f32_16x16x32_bf16 v[112:115], v[0:3], v[96:99], v[112:115]\n"
"  ds_read_b128 v[156:159], v72 offset:29696\n"
"  v_mfma_f32_16x16x32_bf16 v[116:119], v[0:3], v[100:103], v[116:119]\n"
"  v_mfma_f32_16x16x32_bf16 v[120:123], v[4:7], v[96:99], v[120:123]\n"
"  v_mfma_f32_16x16x32_bf16 v[124:127], v[4:7], v[100:103], v[124:127]\n"
"  ds_read_b128 v[160:163], v72 offset:30720\n"
"  s_waitcnt lgkmcnt(4)\n"
"  v_mfma_f32_16x16x32_bf16 v[128:131], v[144:147], v[96:99], v[128:131]\n"
"  s_add_u32 s48, s48, 1\n"
"  s_waitcnt lgkmcnt(3)\n"
"  v_cmp_gt_u32 vcc, s48, v253\n"
"  ds_read_b128 v[164:167], v72 offset:31744\n"
"  v_mfma_f32_16x16x32_bf16 v[132:135], v[144:147], v[100:103], v[132:135]\n"
"  ds_inc_u32 v252, v252 offset:1024\n"
"  s_cbranch_vccnz .Lc_sp29\n"
".Lc_bk29:\n"
"  v_mfma_f32_16x16x32_bf16 v[136:139], v[148:151], v[96:99], v[136:139]\n"
"  ds_read_b64_tr_b16 v[0:1], v248 offset:0\n"
"  v_mfma_f32_16x16x32_bf16 v[140:143], v[148:151], v[100:103], v[140:143]\n"
"  ds_read_b64_tr_b16 v[2:3], v248 offset:2048\n"
"  s_waitcnt lgkmcnt(5)\n"
"  v_mfma_f32_16x16x32_bf16 v[112:115], v[152:155], v[104:107], v[112:115]\n"
"  ds_read_b64_tr_b16 v[4:5], v249 offset:0\n"
"  v_mfma_f32_16x16x32_bf16 v[116:119], v[152:155], v[108:111], v[116:119]\n"
"  ds_read_b64_tr_b16 v[6:7], v249 offset:2048\n"
"  v_mfma_f32_16x16x32_bf16 v[120:123], v[156:159], v[104:107], v[120:123]\n"
"  ds_read_b64_tr_b16 v[144:145], v248 offset:512\n"
"  v_mfma_f32_16x16x32_bf16 v[124:127], v[156:159], v[108:111], v[124:127]\n"
"  ds_read_b64_tr_b16 v[146:147], v248 offset:2560\n"
"  s_waitcnt lgkmcnt(6)\n"
"  v_mfma_f32_16x16x32_bf16 v[128:131], v[160:163], v[104:107], v[128:131]\n"
"  ds_read_b64_tr_b16 v[148:149], v249 offset:512\n"
"  v_mfma_f32_16x16x32_bf16 v[132:135], v[160:163], v[108:111], v[132:135]\n"
"  ds_read_b64_tr_b16 v[150:151], v249 offset:2560\n"
"  v_mfma_f32_16x16x32_bf16 v[136:139], v[164:167], v[104:107], v[136:139]\n"
"  ds_read_b64_tr_b16 v[152:153], v248 offset:4096\n"
"  v_mfma_f32_16x16x32_bf16 v[140:143], v[164:167], v[108:111], v[140:143]\n"
"  ds_read_b64_tr_b16 v[154:155], v248 offset:6144\n"
"  s_waitcnt lgkmcnt(6)\n"
"  v_mfma_f32_16x16x32_bf16 v[40:43], v[0:3], v[208:211], 0\n"
"  ds_read_b64_tr_b16 v[156:157], v249 offset:4096\n"
"  v_mfma_f32_16x16x32_bf16 v[44:47], v[0:3], v[212:215], 0\n"
"  ds_read_b64_tr_b16 v[158:159], v249 offset:6144\n"
"  v_mfma_f32_16x16x32_bf16 v[48:51], v[4:7], v[208:211], 0\n"
"  ds_read_b64_tr_b16 v[160:161], v248 offset:4608\n"
"  v_cvt_pk_bf16_f32 v112, v112, v113\n"
"  v_mfma_f32_16x16x32_bf16 v[52:55], v[4:7], v[212:215], 0\n"
"  ds_read_b64_tr_b16 v[162:163], v248 offset:6656\n"
"  v_cvt_pk_bf16_f32 v113, v114, v115\n"
"  s_waitcnt lgkmcnt(6)\n"
"  v_mfma_f32_16x16x32_bf16 v[56:59], v[144:147], v[208:211], 0\n"
"  ds_read_b64_tr_b16 v[164:165], v249 offset:4608\n"
"  v_cvt_pk_bf16_f32 v114, v120, v121\n"
"  v_mfma_f32_16x16x32_bf16 v[60:63], v[144:147], v[212:215], 0\n"
"  ds_read_b64_tr_b16 v[166:167], v249 offset:6656\n"
"  v_cvt_pk_bf16_f32 v115, v122, v123\n"
"  v_mfma_f32_16x16x32_bf16 v[64:67], v[148:151], v[208:211], 0\n"
"  ds_read_b64_tr_b16 v[0:1], v248 offset:8192\n"
"  buffer_store_dwordx4 v[112:115], v254, s[40:43], s47 offen sc1\n"
"  v_mfma_f32_16x16x32_bf16 v[68:71], v[148:151], v[212:215], 0\n"
"  ds_read_b64_tr_b16 v[2:3], v248 offset:10240\n"
"  s_add_u32 s47, s47, 16384\n"
"  s_waitcnt lgkmcnt(6)\n"
"  v_mfma_f32_16x16x32_bf16 v[40:43], v[152:155], v[224:227], v[40:43]\n"
"  ds_read_b64_tr_b16 v[4:5], v249 offset:8192\n"
"  v_cvt_pk_bf16_f32 v116, v116, v117\n"
"  v_mfma_f32_16x16x32_bf16 v[44:47], v[152:155], v[228:231], v[44:47]\n"
"  ds_read_b64_tr_b16 v[6:7], v249 offset:10240\n"
"  v_cvt_pk_bf16_f32 v117, v118, v119\n"
"  v_mfma_f32_16x16x32_bf16 v[48:51], v[156:159], v[224:227], v[48:51]\n"
"  ds_read_b64_tr_b16 v[144:145], v248 offset:8704\n"
"  v_cvt_pk_bf16_f32 v118, v124, v125\n"
"  v_mfma_f32_16x16x32_bf16 v[52:55], v[156:159], v[228:231], v[52:55]\n"
"  ds_read_b64_tr_b16 v[146:147], v248 offset:10752\n"
"  v_cvt_pk_bf16_f32 v119, v126, v127\n"
"  s_waitcnt lgkmcnt(6)\n"
"  v_mfma_f32_16x16x32_bf16 v[56:59], v[160:163], v[224:227], v[56:59]\n"
"  ds_read_b64_tr_b16 v[148:149], v249 offset:8704\n"
"  buffer_store_dwordx4 v[116:119], v254, s[40:43], s47 offen sc1\n"
"  v_mfma_f32_16x16x32_bf16 v[60:63], v[160:163], v[228:231], v[60:63]\n"
"  ds_read_b64_tr_b16 v[150:151], v249 offset:10752\n"
"  s_add_u32 s47, s47, 16384\n"
"  v_mfma_f32_16x16x32_bf16 v[64:67], v[164:167], v[224:227], v[64:67]\n"
"  ds_read_b64_tr_b16 v[152:153], v248 offset:12288\n"
"  v_cvt_pk_bf16_f32 v128, v128, v129\n"
"  v_mfma_f32_16x16x32_bf16 v[68:71], v[164:167], v[228:231], v[68:71]\n"
"  ds_read_b64_tr_b16 v[154:155], v248 offset:14336\n"
"  v_cvt_pk_bf16_f32 v129, v130, v131\n"
"  s_waitcnt lgkmcnt(6)\n"
"  v_mfma_f32_16x16x32_bf16 v[40:43], v[0:3], v[168:171], v[40:43]\n"
"  ds_read_b64_tr_b16 v[156:157], v249 offset:12288\n"
"  v_cvt_pk_bf16_f32 v130, v136, v137\n"
"  v_mfma_f32_16x16x32_bf16 v[44:47], v[0:3], v[172:175], v[44:47]\n"
"  ds_read_b64_tr_b16 v[158:159], v249 offset:14336\n"
"  v_cvt_pk_bf16_f32 v131, v138, v139\n"
"  v_mfma_f32_16x16x32_bf16 v[48:51], v[4:7], v[168:171], v[48:51]\n"
"  ds_read_b64_tr_b16 v[160:161], v248 offset:12800\n"
"  buffer_store_dwordx4 v[128:131], v254, s[40:43], s47 offen sc1\n"
"  v_mfma_f32_16x16x32_bf16 v[52:55], v[4:7], v[172:175], v[52:55]\n"
"  ds_read_b64_tr_b16 v[162:163], v248 offset:14848\n"
"  s_add_u32 s47, s47, 16384\n"
"  s_waitcnt lgkmcnt(6)\n"
"  v_mfma_f32_16x16x32_bf16 v[56:59], v[144:147], v[168:171], v[56:59]\n"
"  ds_read_b64_tr_b16 v[164:165], v249 offset:12800\n"
"  v_cvt_pk_bf16_f32 v132, v132, v133\n"
"  v_mfma_f32_16x16x32_bf16 v[60:63], v[144:147], v[172:175], v[60:63]\n"
"  ds_read_b64_tr_b16 v[166:167], v249 offset:14848\n"
"  v_cvt_pk_bf16_f32 v133, v134, v135\n"
"  v_mfma_f32_16x16x32_bf16 v[64:67], v[148:151], v[168:171], v[64:67]\n"
"  ds_read_b64_tr_b16 v[0:1], v248 offset:16384\n"
"  v_cvt_pk_bf16_f32 v134, v140, v141\n"
"  v_mfma_f32_16x16x32_bf16 v[68:71], v[148:151], v[172:175], v[68:71]\n"
"  ds_read_b64_tr_b16 v[2:3], v248 offset:18432\n"
"  v_cvt_pk_bf16_f32 v135, v142, v143\n"
"  s_waitcnt lgkmcnt(6)\n"
"  v_mfma_f32_16x16x32_bf16 v[40:43], v[152:155], v[184:187], v[40:43]\n"
"  ds_read_b64_tr_b16 v[4:5], v249 offset:16384\n"
"  buffer_store_dwordx4 v[132:135], v254, s[40:43], s47 offen sc1\n"
"  v_mfma_f32_16x16x32_bf16 v[44:47], v[152:155], v[188:191], v[44:47]\n"
"  ds_read_b64_tr_b16 v[6:7], v249 offset:18432\n"
"  s_add_u32 s47, s47, 212992\n"
"  v_mfma_f32_16x16x32_bf16 v[48:51], v[156:159], v[184:187], v[48:51]\n"
"  ds_read_b64_tr_b16 v[144:145], v248 offset:16896\n"
"  v_mfma_f32_16x16x32_bf16 v[52:55], v[156:159], v[188:191], v[52:55]\n"
"  ds_read_b64_tr_b16 v[146:147], v248 offset:18944\n"
"  s_waitcnt lgkmcnt(6)\n"
"  v_mfma_f32_16x16x32_bf16 v[56:59], v[160:163], v[184:187], v[56:59]\n"
"  ds_read_b64_tr_b16 v[148:149], v249 offset:16896\n"
"  v_mfma_f32_16x16x32_bf16 v[60:63], v[160:163], v[188:191], v[60:63]\n"
"  ds_read_b64_tr_b16 v[150:151], v249 offset:18944\n"
"  v_mfma_f32_16x16x32_bf16 v[64:67], v[164:167], v[184:187], v[64:67]\n"
"  ds_read_b32 v253, v252\n"
"  ds_read_b64_tr_b16 v[152:153], v248 offset:20480\n"
"  v_mfma_f32_16x16x32_bf16 v[68:71], v[164:167], v[188:191], v[68:71]\n"
"  ds_read_b64_tr_b16 v[154:155], v248 offset:22528\n"
"  s_waitcnt lgkmcnt(7)\n"
"  v_mfma_f32_16x16x32_bf16 v[40:43], v[0:3], v[200:203], v[40:43]\n"
"  ds_read_b64_tr_b16 v[156:157], v249 offset:20480\n"
"  v_mfma_f32_16x16x32_bf16 v[44:47], v[0:3], v[204:207], v[44:47]\n"
"  ds_read_b64_tr_b16 v[158:159], v249 offset:22528\n"
"  v_mfma_f32_16x16x32_bf16 v[48:51], v[4:7], v[200:203], v[48:51]\n"
"  ds_read_b64_tr_b16 v[160:161], v248 offset:20992\n"
"  v_mfma_f32_16x16x32_bf16 v[52:55], v[4:7], v[204:207], v[52:55]\n"
"  ds_read_b64_tr_b16 v[162:163], v248 offset:23040\n"
"  s_waitcnt lgkmcnt(7)\n"
"  v_mfma_f32_16x16x32_bf16 v[56:59], v[144:147], v[200:203], v[56:59]\n"
"  s_add_u32 s48, s48, 1\n"
"  s_waitcnt lgkmcnt(5)\n"
"  v_cmp_gt_u32 vcc, s48, v253\n"
"  ds_read_b64_tr_b16 v[164:165], v249 offset:20992\n"
"  v_mfma_f32_16x16x32_bf16 v[60:63], v[144:147], v[204:207], v[60:63]\n"
"  ds_read_b64_tr_b16 v[166:167], v249 offset:23040\n"
"  ds_inc_u32 v252, v252 offset:1024\n"
"  s_cbranch_vccnz .Lc_sp30\n"
".Lc_bk30:\n"
"  v_mfma_f32_16x16x32_bf16 v[64:67], v[148:151], v[200:203], v[64:67]\n"
"  ds_read_b64_tr_b16 v[0:1], v248 offset:24576\n"
"  v_mfma_f32_16x16x32_bf16 v[68:71], v[148:151], v[204:207], v[68:71]\n"
"  ds_read_b64_tr_b16 v[2:3], v248 offset:26624\n"
"  s_waitcnt lgkmcnt(7)\n"
"  v_mfma_f32_16x16x32_bf16 v[40:43], v[152:155], v[216:219], v[40:43]\n"
"  ds_read_b64_tr_b16 v[4:5], v249 offset:24576\n"
"  v_mfma_f32_16x16x32_bf16 v[44:47], v[152:155], v[220:223], v[44:47]\n"
"  ds_read_b64_tr_b16 v[6:7], v249 offset:26624\n"
"  v_mfma_f32_16x16x32_bf16 v[48:51], v[156:159], v[216:219], v[48:51]\n"
"  ds_read_b64_tr_b16 v[144:145], v248 offset:25088\n"
"  v_mfma_f32_16x16x32_bf16 v[52:55], v[156:159], v[220:223], v[52:55]\n"
"  ds_read_b64_tr_b16 v[146:147], v248 offset:27136\n"
"  s_waitcnt lgkmcnt(6)\n"
"  v_mfma_f32_16x16x32_bf16 v[56:59], v[160:163], v[216:219], v[56:59]\n"
"  ds_read_b64_tr_b16 v[148:149], v249 offset:25088\n"
"  v_mfma_f32_16x16x32_bf16 v[60:63], v[160:163], v[220:223], v[60:63]\n"
"  ds_read_b64_tr_b16 v[150:151], v249 offset:27136\n"
"  v_mfma_f32_16x16x32_bf16 v[64:67], v[164:167], v[216:219], v[64:67]\n"
"  ds_read_b64_tr_b16 v[152:153], v248 offset:28672\n"
"  v_mfma_f32_16x16x32_bf16 v[68:71], v[164:167], v[220:223], v[68:71]\n"
"  ds_read_b64_tr_b16 v[154:155], v248 offset:30720\n"
"  s_waitcnt lgkmcnt(6)\n"
"  v_mfma_f32_16x16x32_bf16 v[40:43], v[0:3], v[176:179], v[40:43]\n"
"  ds_read_b64_tr_b16 v[156:157], v249 offset:28672\n"
"  v_mfma_f32_16x16x32_bf16 v[44:47], v[0:3], v[180:183], v[44:47]\n"
"  ds_read_b64_tr_b16 v[158:159], v249 offset:30720\n"
"  v_mfma_f32_16x16x32_bf16 v[48:51], v[4:7], v[176:179], v[48:51]\n"
"  ds_read_b64_tr_b16 v[160:161], v248 offset:29184\n"
"  v_mfma_f32_16x16x32_bf16 v[52:55], v[4:7], v[180:183], v[52:55]\n"
"  ds_read_b64_tr_b16 v[162:163], v248 offset:31232\n"
"  s_waitcnt lgkmcnt(6)\n"
"  v_mfma_f32_16x16x32_bf16 v[56:59], v[144:147], v[176:179], v[56:59]\n"
"  ds_read_b64_tr_b16 v[164:165], v249 offset:29184\n"
"  v_mfma_f32_16x16x32_bf16 v[60:63], v[144:147], v[180:183], v[60:63]\n"
"  ds_read_b64_tr_b16 v[166:167], v249 offset:31232\n"
"  v_mfma_f32_16x16x32_bf16 v[64:67], v[148:151], v[176:179], v[64:67]\n"
"  ds_read_b64_tr_b16 v[0:1], v248 offset:32768\n"
"  v_mfma_f32_16x16x32_bf16 v[68:71], v[148:151], v[180:183], v[68:71]\n"
"  ds_read_b64_tr_b16 v[2:3], v248 offset:34816\n"
"  s_waitcnt lgkmcnt(6)\n"
"  v_mfma_f32_16x16x32_bf16 v[40:43], v[152:155], v[240:243], v[40:43]\n"
"  ds_read_b64_tr_b16 v[4:5], v249 offset:32768\n"
"  v_mfma_f32_16x16x32_bf16 v[44:47], v[152:155], v[244:247], v[44:47]\n"
"  ds_read_b64_tr_b16 v[6:7], v249 offset:34816\n"
"  v_mfma_f32_16x16x32_bf16 v[48:51], v[156:159], v[240:243], v[48:51]\n"
"  ds_read_b64_tr_b16 v[144:145], v248 offset:33280\n"
"  v_mfma_f32_16x16x32_bf16 v[52:55], v[156:159], v[244:247], v[52:55]\n"
"  ds_read_b64_tr_b16 v[146:147], v248 offset:35328\n"
"  s_waitcnt lgkmcnt(6)\n"
"  v_mfma_f32_16x16x32_bf16 v[56:59], v[160:163], v[240:243], v[56:59]\n"
"  ds_read_b64_tr_b16 v[148:149], v249 offset:33280\n"
"  v_mfma_f32_16x16x32_bf16 v[60:63], v[160:163], v[244:247], v[60:63]\n"
"  ds_read_b64_tr_b16 v[150:151], v249 offset:35328\n"
"  v_mfma_f32_16x16x32_bf16 v[64:67], v[164:167], v[240:243], v[64:67]\n"
"  v_mfma_f32_16x16x32_bf16 v[68:71], v[164:167], v[244:247], v[68:71]\n"
"  ds_read_b128 v[152:155], v254 offset:0\n"
"  s_waitcnt lgkmcnt(5)\n"
"  v_mfma_f32_16x16x32_bf16 v[40:43], v[0:3], v[192:195], v[40:43]\n"
"  ds_read_b128 v[156:159], v254 offset:1024\n"
"  v_mfma_f32_16x16x32_bf16 v[44:47], v[0:3], v[196:199], v[44:47]\n"
"  v_mfma_f32_16x16x32_bf16 v[48:51], v[4:7], v[192:195], v[48:51]\n"
"  v_mfma_f32_16x16x32_bf16 v[52:55], v[4:7], v[196:199], v[52:55]\n"
"  ds_read_b128 v[160:163], v254 offset:2048\n"
"  s_waitcnt lgkmcnt(3)\n"
"  v_mfma_f32_16x16x32_bf16 v[56:59], v[144:147], v[192:195], v[56:59]\n"
"  ds_read_b128 v[164:167], v254 offset:3072\n"
"  v_mfma_f32_16x16x32_bf16 v[60:63], v[144:147], v[196:199], v[60:63]\n"
"  v_mfma_f32_16x16x32_bf16 v[64:67], v[148:151], v[192:195], v[64:67]\n"
"  v_mfma_f32_16x16x32_bf16 v[68:71], v[148:151], v[196:199], v[68:71]\n"
"  ds_read_b128 v[0:3], v254 offset:4096\n"
"  s_waitcnt lgkmcnt(3)\n"
"  v_mfma_f32_16x16x32_bf16 v[40:43], v[152:155], v[8:11], v[40:43]\n"
"  ds_read_b128 v[4:7], v254 offset:5120\n"
"  v_mfma_f32_16x16x32_bf16 v[44:47], v[152:155], v[12:15], v[44:47]\n"
"  v_mfma_f32_16x16x32_bf16 v[48:51], v[156:159], v[8:11], v[48:51]\n"
"  v_mfma_f32_16x16x32_bf16 v[52:55], v[156:159], v[12:15], v[52:55]\n"
"  ds_read_b128 v[144:147], v254 offset:6144\n"
"  s_waitcnt lgkmcnt(3)\n"
"  v_mfma_f32_16x16x32_bf16 v[56:59], v[160:163], v[8:11], v[56:59]\n"
"  ds_read_b128 v[148:151], v254 offset:7168\n"
"  v_mfma_f32_16x16x32_bf16 v[60:63], v[160:163], v[12:15], v[60:63]\n"
"  v_mfma_f32_16x16x32_bf16 v[64:67], v[164:167], v[8:11], v[64:67]\n"
"  ds_read_b32 v253, v252\n"
"  v_mfma_f32_16x16x32_bf16 v[68:71], v[164:167], v[12:15], v[68:71]\n"
"  ds_read_b128 v[152:155], v254 offset:8192\n"
"  s_waitcnt lgkmcnt(4)\n"
"  v_mfma_f32_16x16x32_bf16 v[40:43], v[0:3], v[16:19], v[40:43]\n"
"  ds_read_b128 v[156:159], v254 offset:9216\n"
"  v_mfma_f32_16x16x32_bf16 v[44:47], v[0:3], v[20:23], v[44:47]\n"
"  v_mfma_f32_16x16x32_bf16 v[48:51], v[4:7], v[16:19], v[48:51]\n"
"  v_mfma_f32_16x16x32_bf16 v[52:55], v[4:7], v[20:23], v[52:55]\n"
"  ds_read_b128 v[160:163], v254 offset:10240\n"
"  s_waitcnt lgkmcnt(4)\n"
"  v_mfma_f32_16x16x32_bf16 v[56:59], v[144:147], v[16:19], v[56:59]\n"
"  s_add_u32 s48, s48, 1\n"
"  s_waitcnt lgkmcnt(3)\n"
"  v_cmp_gt_u32 vcc, s48, v253\n"
"  ds_read_b128 v[164:167], v254 offset:11264\n"
"  s_waitcnt vmcnt(0)\n"
"  ds_inc_rtn_u32 v73, v72, v252 offset:34816\n"
"  v_mfma_f32_16x16x32_bf16 v[60:63], v[144:147], v[20:23], v[60:63]\n"
"  ds_inc_u32 v252, v252 offset:1024\n"
"  s_cbranch_vccnz .Lc_sp31\n"
".Lc_bk31:\n"
"  v_mfma_f32_16x16x32_bf16 v[64:67], v[148:151], v[16:19], v[64:67]\n"
"  v_mfma_f32_16x16x32_bf16 v[68:71], v[148:151], v[20:23], v[68:71]\n"
"  ds_read_b128 v[0:3], v254 offset:12288\n"
"  s_waitcnt lgkmcnt(5)\n"
"  v_mfma_f32_16x16x32_bf16 v[40:43], v[152:155], v[24:27], v[40:43]\n"
"  ds_read_b128 v[4:7], v254 offset:13312\n"
"  v_lshlrev_b32 v74, 4, v254\n"
"  v_mfma_f32_16x16x32_bf16 v[44:47], v[152:155], v[28:31], v[44:47]\n"
"  v_mfma_f32_16x16x32_bf16 v[48:51], v[156:159], v[24:27], v[48:51]\n"
"  v_mfma_f32_16x16x32_bf16 v[52:55], v[156:159], v[28:31], v[52:55]\n"
"  ds_read_b128 v[144:147], v254 offset:14336\n"
"  s_waitcnt lgkmcnt(3)\n"
"  v_mfma_f32_16x16x32_bf16 v[56:59], v[160:163], v[24:27], v[56:59]\n"
"  ds_read_b128 v[148:151], v254 offset:15360\n"
"  v_lshl_add_u32 v74, v73, 30, v74\n"
"  v_mfma_f32_16x16x32_bf16 v[60:63], v[160:163], v[28:31], v[60:63]\n"
"  buffer_store_dword v73, v74, s[96:99], 0 offen sc1\n"
"  v_mfma_f32_16x16x32_bf16 v[64:67], v[164:167], v[24:27], v[64:67]\n"
"  v_mfma_f32_16x16x32_bf16 v[68:71], v[164:167], v[28:31], v[68:71]\n"
"  ds_read_b128 v[152:155], v254 offset:16384\n"
"  s_add_u32 s96, s96, 16384\n"
"  s_addc_u32 s97, s97, 0\n"
"  s_waitcnt lgkmcnt(3)\n"
"  v_mfma_f32_16x16x32_bf16 v[40:43], v[0:3], v[32:35], v[40:43]\n"
"  ds_read_b128 v[156:159], v254 offset:17408\n"
"  v_mfma_f32_16x16x32_bf16 v[44:47], v[0:3], v[36:39], v[44:47]\n"
"  v_mfma_f32_16x16x32_bf16 v[48:51], v[4:7], v[32:35], v[48:51]\n"
"  v_mfma_f32_16x16x32_bf16 v[52:55], v[4:7], v[36:39], v[52:55]\n"
"  ds_read_b128 v[160:163], v254 offset:18432\n"
"  s_waitcnt lgkmcnt(3)\n"
"  v_mfma_f32_16x16x32_bf16 v[56:59], v[144:147], v[32:35], v[56:59]\n"
"  ds_read_b128 v[164:167], v254 offset:19456\n"
"  v_mfma_f32_16x16x32_bf16 v[60:63], v[144:147], v[36:39], v[60:63]\n"
"  v_mfma_f32_16x16x32_bf16 v[64:67], v[148:151], v[32:35], v[64:67]\n"
"  v_mfma_f32_16x16x32_bf16 v[68:71], v[148:151], v[36:39], v[68:71]\n"
"  ds_read_b128 v[0:3], v254 offset:20480\n"
"  s_waitcnt lgkmcnt(3)\n"
"  v_mfma_f32_16x16x32_bf16 v[40:43], v[152:155], v[80:83], v[40:43]\n"
"  ds_read_b128 v[4:7], v254 offset:21504\n"
"  v_mfma_f32_16x16x32_bf16 v[44:47], v[152:155], v[84:87], v[44:47]\n"
"  v_mfma_f32_16x16x32_bf16 v[48:51], v[156:159], v[80:83], v[48:51]\n"
"  v_mfma_f32_16x16x32_bf16 v[52:55], v[156:159], v[84:87], v[52:55]\n"
"  ds_read_b128 v[144:147], v254 offset:22528\n"
"  s_waitcnt lgkmcnt(3)\n"
"  v_mfma_f32_16x16x32_bf16 v[56:59], v[160:163], v[80:83], v[56:59]\n"
"  ds_read_b128 v[148:151], v254 offset:23552\n"
"  v_mfma_f32_16x16x32_bf16 v[60:63], v[160:163], v[84:87], v[60:63]\n"
"  v_mfma_f32_16x16x32_bf16 v[64:67], v[164:167], v[80:83], v[64:67]\n"
"  v_mfma_f32_16x16x32_bf16 v[68:71], v[164:167], v[84:87], v[68:71]\n"
"  ds_read_b128 v[152:155], v254 offset:24576\n"
"  s_waitcnt lgkmcnt(3)\n"
"  v_mfma_f32_16x16x32_bf16 v[40:43], v[0:3], v[88:91], v[40:43]\n"
"  ds_read_b128 v[156:159], v254 offset:25600\n"
"  v_mfma_f32_16x16x32_bf16 v[44:47], v[0:3], v[92:95], v[44:47]\n"
"  v_mfma_f32_16x16x32_bf16 v[48:51], v[4:7], v[88:91], v[48:51]\n"
"  v_mfma_f32_16x16x32_bf16 v[52:55], v[4:7], v[92:95], v[52:55]\n"
"  ds_read_b128 v[160:163], v254 offset:26624\n"
"  s_waitcnt lgkmcnt(3)\n"
"  v_mfma_f32_16x16x32_bf16 v[56:59], v[144:147], v[88:91], v[56:59]\n"
"  ds_read_b128 v[164:167], v254 offset:27648\n"
"  v_mfma_f32_16x16x32_bf16 v[60:63], v[144:147], v[92:95], v[60:63]\n"
"  v_mfma_f32_16x16x32_bf16 v[64:67], v[148:151], v[88:91], v[64:67]\n"
"  ds_read_b32 v253, v252\n"
"  v_mfma_f32_16x16x32_bf16 v[68:71], v[148:151], v[92:95], v[68:71]\n"
"  ds_read_b128 v[0:3], v254 offset:28672\n"
"  s_waitcnt lgkmcnt(4)\n"
"  v_mfma_f32_16x16x32_bf16 v[40:43], v[152:155], v[96:99], v[40:43]\n"
"  ds_read_b128 v[4:7], v254 offset:29696\n"
"  v_mfma_f32_16x16x32_bf16 v[44:47], v[152:155], v[100:103], v[44:47]\n"
"  v_mfma_f32_16x16x32_bf16 v[48:51], v[156:159], v[96:99], v[48:51]\n"
"  v_mfma_f32_16x16x32_bf16 v[52:55], v[156:159], v[100:103], v[52:55]\n"
"  ds_read_b128 v[144:147], v254 offset:30720\n"
"  s_waitcnt lgkmcnt(4)\n"
"  v_mfma_f32_16x16x32_bf16 v[56:59], v[160:163], v[96:99], v[56:59]\n"
"  s_add_u32 s48, s48, 1\n"
"  s_waitcnt lgkmcnt(3)\n"
"  v_cmp_gt_u32 vcc, s48, v253\n"
"  ds_read_b128 v[148:151], v254 offset:31744\n"
"  v_mfma_f32_16x16x32_bf16 v[60:63], v[160:163], v[100:103], v[60:63]\n"
"  ds_inc_u32 v252, v252 offset:1024\n"
"  s_cbranch_vccnz .Lc_sp32\n"
".Lc_bk32:\n"
"  v_mfma_f32_16x16x32_bf16 v[64:67], v[164:167], v[96:99], v[64:67]\n"
"  ds_read_b64_tr_b16 v[152:153], v250 offset:8192\n"
"  v_mfma_f32_16x16x32_bf16 v[68:71], v[164:167], v[100:103], v[68:71]\n"
"  ds_read_b64_tr_b16 v[154:155], v250 offset:10240\n"
"  s_waitcnt lgkmcnt(5)\n"
"  v_mfma_f32_16x16x32_bf16 v[40:43], v[0:3], v[104:107], v[40:43]\n"
"  ds_read_b64_tr_b16 v[156:157], v251 offset:8192\n"
"  v_mfma_f32_16x16x32_bf16 v[44:47], v[0:3], v[108:111], v[44:47]\n"
"  ds_read_b64_tr_b16 v[158:159], v251 offset:10240\n"
"  v_mfma_f32_16x16x32_bf16 v[48:51], v[4:7], v[104:107], v[48:51]\n"
"  ds_read_b64_tr_b16 v[160:161], v250 offset:8704\n"
"  v_mfma_f32_16x16x32_bf16 v[52:55], v[4:7], v[108:111], v[52:55]\n"
"  ds_read_b64_tr_b16 v[162:163], v250 offset:10752\n"
"  s_waitcnt lgkmcnt(6)\n"
"  v_mfma_f32_16x16x32_bf16 v[56:59], v[144:147], v[104:107], v[56:59]\n"
"  ds_read_b64_tr_b16 v[164:165], v251 offset:8704\n"
"  v_mfma_f32_16x16x32_bf16 v[60:63], v[144:147], v[108:111], v[60:63]\n"
"  ds_read_b64_tr_b16 v[166:167], v251 offset:10752\n"
"  v_mfma_f32_16x16x32_bf16 v[64:67], v[148:151], v[104:107], v[64:67]\n"
"  s_sub_u32 s49, s49, 1\n"
"  ds_read_b64_tr_b16 v[0:1], v250 offset:12288\n"
"  v_mfma_f32_16x16x32_bf16 v[68:71], v[148:151], v[108:111], v[68:71]\n"
"  ds_read_b64_tr_b16 v[2:3], v250 offset:14336\n"
"  s_cbranch_scc0 .Lc_loop\n"
"  v_cvt_pk_bf16_f32 v40, v40, v41\n"
"  v_cvt_pk_bf16_f32 v41, v42, v43\n"
"  v_cvt_pk_bf16_f32 v42, v48, v49\n"
"  v_cvt_pk_bf16_f32 v43, v50, v51\n"
"  buffer_store_dwordx4 v[40:43], v254, s[40:43], s47 offen sc1\n"
"  s_add_u32 s47, s47, 16384\n"
"  v_cvt_pk_bf16_f32 v44, v44, v45\n"
"  v_cvt_pk_bf16_f32 v45, v46, v47\n"
"  v_cvt_pk_bf16_f32 v46, v52, v53\n"
"  v_cvt_pk_bf16_f32 v47, v54, v55\n"
"  buffer_store_dwordx4 v[44:47], v254, s[40:43], s47 offen sc1\n"
"  s_add_u32 s47, s47, 16384\n"
"  v_cvt_pk_bf16_f32 v56, v56, v57\n"
"  v_cvt_pk_bf16_f32 v57, v58, v59\n"
"  v_cvt_pk_bf16_f32 v58, v64, v65\n"
"  v_cvt_pk_bf16_f32 v59, v66, v67\n"
"  buffer_store_dwordx4 v[56:59], v254, s[40:43], s47 offen sc1\n"
"  s_add_u32 s47, s47, 16384\n"
"  v_cvt_pk_bf16_f32 v60, v60, v61\n"
"  v_cvt_pk_bf16_f32 v61, v62, v63\n"
"  v_cvt_pk_bf16_f32 v62, v68, v69\n"
"  v_cvt_pk_bf16_f32 v63, v70, v71\n"
"  buffer_store_dwordx4 v[60:63], v254, s[40:43], s47 offen sc1\n"
"  s_add_u32 s47, s47, 212992\n"
"  s_branch .Lc_end\n"
".Lc_sp1:\n"
"  ds_read_b32 v253, v252\n"
"  s_sub_u32 s50, s50, 1\n"
"  s_cbranch_scc1 .Lc_end\n"
"  s_waitcnt lgkmcnt(0)\n"
"  v_cmp_gt_u32 vcc, 2, v253\n"
"  s_cbranch_vccnz .Lc_sp1\n"
"  s_branch .Lc_bk1\n"
".Lc_sp2:\n"
"  ds_read_b32 v253, v252\n"
"  s_sub_u32 s50, s50, 1\n"
"  s_cbranch_scc1 .Lc_end\n"
"  s_waitcnt lgkmcnt(0)\n"
"  v_cmp_gt_u32 vcc, 3, v253\n"
"  s_cbranch_vccnz .Lc_sp2\n"
"  s_branch .Lc_bk2\n"
".Lc_sp3:\n"
"  ds_read_b32 v253, v252\n"
"  s_sub_u32 s50, s50, 1\n"
"  s_cbranch_scc1 .Lc_end\n"
"  s_waitcnt lgkmcnt(0)\n"
"  v_cmp_gt_u32 vcc, 4, v253\n"
"  s_cbranch_vccnz .Lc_sp3\n"
"  s_branch .Lc_bk3\n"
".Lc_sp4:\n"
"  ds_read_b32 v253, v252\n"
"  s_sub_u32 s50, s50, 1\n"
"  s_cbranch_scc1 .Lc_end\n"
"  s_waitcnt lgkmcnt(0)\n"
"  v_cmp_gt_u32 vcc, 5, v253\n"
"  s_cbranch_vccnz .Lc_sp4\n"
"  s_branch .Lc_bk4\n"
".Lc_sp5:\n"
"  ds_read_b32 v253, v252\n"
"  s_sub_u32 s50, s50, 1\n"
"  s_cbranch_scc1 .Lc_end\n"
"  s_waitcnt lgkmcnt(0)\n"
"  v_cmp_gt_u32 vcc, 6, v253\n"
"  s_cbranch_vccnz .Lc_sp5\n"
"  s_branch .Lc_bk5\n"
".Lc_sp6:\n"
"  ds_read_b32 v253, v252\n"
"  s_sub_u32 s50, s50, 1\n"
"  s_cbranch_scc1 .Lc_end\n"
"  s_waitcnt lgkmcnt(0)\n"
"  v_cmp_gt_u32 vcc, 7, v253\n"
"  s_cbranch_vccnz .Lc_sp6\n"
"  s_branch .Lc_bk6\n"
".Lc_sp7:\n"
"  ds_read_b32 v253, v252\n"
"  s_sub_u32 s50, s50, 1\n"
"  s_cbranch_scc1 .Lc_end\n"
"  s_waitcnt lgkmcnt(0)\n"
"  v_cmp_gt_u32 vcc, 8, v253\n"
"  s_cbranch_vccnz .Lc_sp7\n"
"  s_branch .Lc_bk7\n"
".Lc_sp8:\n"
"  ds_read_b32 v253, v252\n"
"  s_sub_u32 s50, s50, 1\n"
"  s_cbranch_scc1 .Lc_end\n"
"  s_waitcnt lgkmcnt(0)\n"
"  v_cmp_gt_u32 vcc, 9, v253\n"
"  s_cbranch_vccnz .Lc_sp8\n"
"  s_branch .Lc_bk8\n"
".Lc_sp9:\n"
"  ds_read_b32 v253, v252\n"
"  s_sub_u32 s50, s50, 1\n"
"  s_cbranch_scc1 .Lc_end\n"
"  s_waitcnt lgkmcnt(0)\n"
"  v_cmp_gt_u32 vcc, 10, v253\n"
"  s_cbranch_vccnz .Lc_sp9\n"
"  s_branch .Lc_bk9\n"
".Lc_sp10:\n"
"  ds_read_b32 v253, v252\n"
"  s_sub_u32 s50, s50, 1\n"
"  s_cbranch_scc1 .Lc_end\n"
"  s_waitcnt lgkmcnt(0)\n"
"  v_cmp_gt_u32 vcc, 11, v253\n"
"  s_cbranch_vccnz .Lc_sp10\n"
"  s_branch .Lc_bk10\n"
".Lc_sp11:\n"
"  ds_read_b32 v253, v252\n"
"  s_sub_u32 s50, s50, 1\n"
"  s_cbranch_scc1 .Lc_end\n"
"  s_waitcnt lgkmcnt(0)\n"
"  v_cmp_gt_u32 vcc, 12, v253\n"
"  s_cbranch_vccnz .Lc_sp11\n"
"  s_branch .Lc_bk11\n"
".Lc_sp12:\n"
"  ds_read_b32 v253, v252\n"
"  s_sub_u32 s50, s50, 1\n"
"  s_cbranch_scc1 .Lc_end\n"
"  s_waitcnt lgkmcnt(0)\n"
"  v_cmp_gt_u32 vcc, 13, v253\n"
"  s_cbranch_vccnz .Lc_sp12\n"
"  s_branch .Lc_bk12\n"
".Lc_sp13:\n"
"  ds_read_b32 v253, v252\n"
"  s_sub_u32 s50, s50, 1\n"
"  s_cbranch_scc1 .Lc_end\n"
"  s_waitcnt lgkmcnt(0)\n"
"  v_cmp_gt_u32 vcc, 14, v253\n"
"  s_cbranch_vccnz .Lc_sp13\n"
"  s_branch .Lc_bk13\n"
".Lc_sp14:\n"
"  ds_read_b32 v253, v252\n"
"  s_sub_u32 s50, s50, 1\n"
"  s_cbranch_scc1 .Lc_end\n"
"  s_waitcnt lgkmcnt(0)\n"
"  v_cmp_gt_u32 vcc, 15, v253\n"
"  s_cbranch_vccnz .Lc_sp14\n"
"  s_branch .Lc_bk14\n"
".Lc_sp15:\n"
"  ds_read_b32 v253, v252\n"
"  s_sub_u32 s50, s50, 1\n"
"  s_cbranch_scc1 .Lc_end\n"
"  s_waitcnt lgkmcnt(0)\n"
"  v_cmp_gt_u32 vcc, 16, v253\n"
"  s_cbranch_vccnz .Lc_sp15\n"
"  s_branch .Lc_bk15\n"
".Lc_sp16:\n"
"  ds_read_b32 v253, v252\n"
"  s_sub_u32 s50, s50, 1\n"
"  s_cbranch_scc1 .Lc_end\n"
"  s_waitcnt lgkmcnt(0)\n"
"  v_cmp_gt_u32 vcc, 17, v253\n"
"  s_cbranch_vccnz .Lc_sp16\n"
"  s_branch .Lc_bk16\n"
".Lc_sp17:\n"
"  ds_read_b32 v253, v252\n"
"  s_sub_u32 s50, s50, 1\n"
"  s_cbranch_scc1 .Lc_end\n"
"  s_waitcnt lgkmcnt(0)\n"
"  v_cmp_gt_u32 vcc, 18, v253\n"
"  s_cbranch_vccnz .Lc_sp17\n"
"  s_branch .Lc_bk17\n"
".Lc_sp18:\n"
"  ds_read_b32 v253, v252\n"
"  s_sub_u32 s50, s50, 1\n"
"  s_cbranch_scc1 .Lc_end\n"
"  s_waitcnt lgkmcnt(0)\n"
"  v_cmp_gt_u32 vcc, 19, v253\n"
"  s_cbranch_vccnz .Lc_sp18\n"
"  s_branch .Lc_bk18\n"
".Lc_sp19:\n"
"  ds_read_b32 v253, v252\n"
"  s_sub_u32 s50, s50, 1\n"
"  s_cbranch_scc1 .Lc_end\n"
"  s_waitcnt lgkmcnt(0)\n"
"  v_cmp_gt_u32 vcc, 20, v253\n"
"  s_cbranch_vccnz .Lc_sp19\n"
"  s_branch .Lc_bk19\n"
".Lc_sp20:\n"
"  ds_read_b32 v253, v252\n"
"  s_sub_u32 s50, s50, 1\n"
"  s_cbranch_scc1 .Lc_end\n"
"  s_waitcnt lgkmcnt(0)\n"
"  v_cmp_gt_u32 vcc, 21, v253\n"
"  s_cbranch_vccnz .Lc_sp20\n"
"  s_branch .Lc_bk20\n"
".Lc_sp21:\n"
"  ds_read_b32 v253, v252\n"
"  s_sub_u32 s50, s50, 1\n"
"  s_cbranch_scc1 .Lc_end\n"
"  s_waitcnt lgkmcnt(0)\n"
"  v_cmp_gt_u32 vcc, 22, v253\n"
"  s_cbranch_vccnz .Lc_sp21\n"
"  s_branch .Lc_bk21\n"
".Lc_sp22:\n"
"  ds_read_b32 v253, v252\n"
"  s_sub_u32 s50, s50, 1\n"
"  s_cbranch_scc1 .Lc_end\n"
"  s_waitcnt lgkmcnt(0)\n"
"  v_cmp_gt_u32 vcc, 23, v253\n"
"  s_cbranch_vccnz .Lc_sp22\n"
"  s_branch .Lc_bk22\n"
".Lc_sp23:\n"
"  ds_read_b32 v253, v252\n"
"  s_sub_u32 s50, s50, 1\n"
"  s_cbranch_scc1 .Lc_end\n"
"  s_waitcnt lgkmcnt(0)\n"
"  v_cmp_gt_u32 vcc, 24, v253\n"
"  s_cbranch_vccnz .Lc_sp23\n"
"  s_branch .Lc_bk23\n"
".Lc_sp24:\n"
"  ds_read_b32 v253, v252\n"
"  s_sub_u32 s50, s50, 1\n"
"  s_cbranch_scc1 .Lc_end\n"
"  s_waitcnt lgkmcnt(0)\n"
"  v_cmp_gt_u32 vcc, 25, v253\n"
"  s_cbranch_vccnz .Lc_sp24\n"
"  s_branch .Lc_bk24\n"
".Lc_sp26:\n"
"  ds_read_b32 v253, v252\n"
"  s_sub_u32 s50, s50, 1\n"
"  s_cbranch_scc1 .Lc_end\n"
"  s_waitcnt lgkmcnt(0)\n"
"  v_cmp_gt_u32 vcc, 28, v253\n"
"  s_cbranch_vccnz .Lc_sp26\n"
"  s_branch .Lc_bk26\n"
".Lc_sp27:\n"
"  ds_read_b32 v253, v252\n"
"  s_sub_u32 s50, s50, 1\n"
"  s_cbranch_scc1 .Lc_end\n"
"  s_waitcnt lgkmcnt(0)\n"
"  v_cmp_gt_u32 vcc, s48, v253\n"
"  s_cbranch_vccnz .Lc_sp27\n"
"  s_branch .Lc_bk27\n"
".Lc_sp28:\n"
"  ds_read_b32 v253, v252\n"
"  s_sub_u32 s50, s50, 1\n"
"  s_cbranch_scc1 .Lc_end\n"
"  s_waitcnt lgkmcnt(0)\n"
"  v_cmp_gt_u32 vcc, s48, v253\n"
"  s_cbranch_vccnz .Lc_sp28\n"
"  s_branch .Lc_bk28\n"
".Lc_sp29:\n"
"  ds_read_b32 v253, v252\n"
"  s_sub_u32 s50, s50, 1\n"
"  s_cbranch_scc1 .Lc_end\n"
"  s_waitcnt lgkmcnt(0)\n"
"  v_cmp_gt_u32 vcc, s48, v253\n"
"  s_cbranch_vccnz .Lc_sp29\n"
"  s_branch .Lc_bk29\n"
".Lc_sp30:\n"
"  ds_read_b32 v253, v252\n"
"  s_sub_u32 s50, s50, 1\n"
"  s_cbranch_scc1 .Lc_end\n"
"  s_waitcnt lgkmcnt(0)\n"
"  v_cmp_gt_u32 vcc, s48, v253\n"
"  s_cbranch_vccnz .Lc_sp30\n"
"  s_branch .Lc_bk30\n"
".Lc_sp31:\n"
"  ds_read_b32 v253, v252\n"
"  s_sub_u32 s50, s50, 1\n"
"  s_cbranch_scc1 .Lc_end\n"
"  s_waitcnt lgkmcnt(0)\n"
"  v_cmp_gt_u32 vcc, s48, v253\n"
"  s_cbranch_vccnz .Lc_sp31\n"
"  s_branch .Lc_bk31\n"
".Lc_sp32:\n"
"  ds_read_b32 v253, v252\n"
"  s_sub_u32 s50, s50, 1\n"
"  s_cbranch_scc1 .Lc_end\n"
"  s_waitcnt lgkmcnt(0)\n"
"  v_cmp_gt_u32 vcc, s48, v253\n"
"  s_cbranch_vccnz .Lc_sp32\n"
"  s_branch .Lc_bk32\n"
".Lc_spci:\n"
"  ds_read_b32 v253, v252\n"
"  s_sub_u32 s50, s50, 1\n"
"  s_cbranch_scc1 .Lc_end\n"
"  s_waitcnt lgkmcnt(0)\n"
"  v_cmp_gt_u32 vcc, 27, v253\n"
"  s_cbranch_vccnz .Lc_spci\n"
"  s_branch .Lc_bkci\n"
".Lc_sp0:\n"
"  ds_read_b32 v253, v252\n"
"  s_sub_u32 s50, s50, 1\n"
"  s_cbranch_scc1 .Lc_end\n"
"  s_waitcnt lgkmcnt(0)\n"
"  v_cmp_gt_u32 vcc, 1, v253\n"
"  s_cbranch_vccnz .Lc_sp0\n"
"  s_branch .Lc_bk0\n"
".Lc_end:\n"

        ::"{v[240:255]}"(vi), "{s[36:51]}"(si), "{s[96:99]}"(srd((char*)ws + FLAG_OFF + tb * 1024 + j * 4 - LIN * 16, LIN * 16 + 1024))
        : "memory", "vcc", "scc", "v0", "v1", "v2", "v3", "v4", "v5", "v6", "v7", "v8", "v9", "v10", "v11", "v12", "v13", "v14", "v15", "v16", "v17", "v18", "v19", "v20", "v21", "v22", "v23", "v24", "v25", "v26", "v27", "v28", "v29", "v30", "v31", "v32", "v33", "v34", "v35", "v36", "v37", "v38", "v39", "v40", "v41", "v42", "v43", "v44", "v45", "v46", "v47", "v48", "v49", "v50", "v51", "v52", "v53", "v54", "v55", "v56", "v57", "v58", "v59", "v60", "v61", "v62", "v63", "v64", "v65", "v66", "v67", "v68", "v69", "v70", "v71", "v72", "v73", "v74", "v75", "v76", "v77", "v78", "v79", "v80", "v81", "v82", "v83", "v84", "v85", "v86", "v87", "v88", "v89", "v90", "v91", "v92", "v93", "v94", "v95", "v96", "v97", "v98", "v99", "v100", "v101", "v102", "v103", "v104", "v105", "v106", "v107", "v108", "v109", "v110", "v111", "v112", "v113", "v114", "v115", "v116", "v117", "v118", "v119", "v120", "v121", "v122", "v123", "v124", "v125", "v126", "v127", "v128", "v129", "v130", "v131", "v132", "v133", "v134", "v135", "v136", "v137", "v138", "v139", "v140", "v141", "v142", "v143", "v144", "v145", "v146", "v147", "v148", "v149", "v150", "v151", "v152", "v153", "v154", "v155", "v156", "v157", "v158", "v159", "v160", "v161", "v162", "v163", "v164", "v165", "v166", "v167", "v168", "v169", "v170", "v171", "v172", "v173", "v174", "v175", "v176", "v177", "v178", "v179", "v180", "v181", "v182", "v183", "v184", "v185", "v186", "v187", "v188", "v189", "v190", "v191", "v192", "v193", "v194", "v195", "v196", "v197", "v198", "v199", "v200", "v201", "v202", "v203", "v204", "v205", "v206", "v207", "v208", "v209", "v210", "v211", "v212", "v213", "v214", "v215", "v216", "v217", "v218", "v219", "v220", "v221", "v222", "v223", "v224", "v225", "v226", "v227", "v228", "v229", "v230", "v231", "v232", "v233", "v234", "v235", "v236", "v237", "v238", "v239", "s56", "s57", "s58", "s59", "s60", "s61", "s62", "s63", "s64", "s65", "s66", "s67", "s68", "s69", "s70", "s71", "s72", "s73", "s74", "s75", "s76", "s77", "s78", "s79", "s80", "s81", "s82", "s83", "s84", "s85", "s86", "s87", "s88", "s89", "s90", "s91", "s92", "s93", "s94", "s95");
  } else {
    const unsigned h = wid - 4, hh = h & 1, r = h >> 1;
    const u32x4 rw = srd(j < 9 ? wr : wi, D_ * D_ * 2), rwi = srd(wi, D_ * D_ * 2), rws = srd(ws, WS_BYTES);
    const unsigned swr = ((lane0 >> 5) * 4 + ((lane0 & 3) ^ fsw(hh * 2 + ((lane0 >> 4) & 1)))) * 16;
    const unsigned vw = (eg * 64 + h * 8 + ((lane0 >> 2) & 7)) * 1152 + swr, vw5 = (t5 * 16 + hh * 8 + ((lane0 >> 2) & 7)) * 1152 + r * 128 + swr;
    const unsigned vxn = XN_OFF + tb * 147456 + h * 4096 + lane0 * 16;
    unsigned lane = lane0;
    asm volatile(
"  s_add_u32 m0, s72, 49152\n"
"  s_mov_b32 s85, 0\n"
"  buffer_load_dwordx4 v0, s[36:39], s85 offen lds\n"
"  s_add_u32 m0, s72, 53248\n"
"  s_mov_b32 s85, 36864\n"
"  buffer_load_dwordx4 v0, s[36:39], s85 offen lds\n"
"  s_add_u32 m0, s72, 145408\n"
"  s_mov_b32 s85, 0\n"
"  buffer_load_dwordx4 v1, s[40:43], s85 offen lds\n"
"  s_add_u32 m0, s75, 57344\n"
"  s_mov_b32 s85, 0\n"
"  buffer_load_dwordx4 v2, s[52:55], s85 offen lds\n"
"  buffer_load_dwordx4 v2, s[52:55], s85 offen offset:1024 lds\n"
"  buffer_load_dwordx4 v2, s[52:55], s85 offen offset:2048 lds\n"
"  buffer_load_dwordx4 v2, s[52:55], s85 offen offset:3072 lds\n"
"  s_add_u32 m0, s72, 73728\n"
"  s_mov_b32 s85, 128\n"
"  buffer_load_dwordx4 v0, s[36:39], s85 offen lds\n"
"  s_add_u32 m0, s72, 77824\n"
"  s_mov_b32 s85, 36992\n"
"  buffer_load_dwordx4 v0, s[36:39], s85 offen lds\n"
"  s_add_u32 m0, s75, 81920\n"
"  s_mov_b32 s85, 16384\n"
"  buffer_load_dwordx4 v2, s[52:55], s85 offen lds\n"
"  buffer_load_dwordx4 v2, s[52:55], s85 offen offset:1024 lds\n"
"  buffer_load_dwordx4 v2, s[52:55], s85 offen offset:2048 lds\n"
"  buffer_load_dwordx4 v2, s[52:55], s85 offen offset:3072 lds\n"

        : "+v"(lane)
        : "{v0}"(vw), "{v1}"(vw5), "{v2}"(vxn), "{s[36:39]}"(rw), "{s[40:43]}"(rwi), "{s[52:55]}"(rws), "{s72}"(ldsb + h * 1024), "{s75}"(ldsb + h * 4096)
        : "memory", "scc", "s85");
    const unsigned c = lane & 15, g = lane >> 4, row = (lane >> 2) & 7, seg = lane >> 5, pos = lane & 3;
    const unsigned fb0 = j * 288 + (jodd ? 32 : 0), fbl = j * 288 + (jodd ? 0 : 256);
    const unsigned swt = (pos ^ ((row >> 2) * 2)) * 16;
    const unsigned k1 = 16 * hh + 8 * (row >> 2) + 4 * r + (row & 3);
    const unsigned sh = (idx & 7) * 4 + h, l62 = lane < 62 ? lane : 62, q9 = l62 / 9, m9 = l62 - q9 * 9;
    const unsigned t2 = (j * 288 + 9 * sh + m9) * 1152 + q9 * 128;
    const unsigned l54 = lane < 54 ? lane : 53, q18 = l54 / 18, m18 = l54 - q18 * 18;
    const unsigned t1 = (18 * sh + m18) * 9216 + ((j * 576) & ~127), q4 = (q18 ? 4 : 3) + jodd;
    const unsigned l5 = (sh & 1) * 5 + lane;
    const unsigned ab = AB_OFF + j * 294912;
    const unsigned tok = tb * 128 + (j >> 2) * 32 + 8 * (c >> 2) + 4 * jodd + (c & 3);
    const unsigned one = (c & 1) ? 0x3f800000u : 0x3f80u, cq = (c & 7) >> 1;
    const u32x16 va = {vw,
                       vw5,
                       vxn,
                       k1 * 9216 + fb0 * 2 + seg * 64 + swt,
                       (k1 + 32 * seg) * 9216 + fbl * 2 + swt,
                       (fb0 + h * 8 + row) * 1152 + seg * 64 + swt,
                       (fbl + h * 8 + row) * 1152 + seg * 64 + swt,
                       ab + h * 3072 + lane * 16,
                       ab + 12288 + h * 5120 + lane * 16,
                       (tb * 128 + k1) * 4096 + j * 256 + swr,
                       ldsb + FL + 1024 + pos * 260,
                       ldsb + FL + g * 256 + h * 64 + c * 4,
                       ldsb + Q2 + h * 1024 + lane * 16,
                       t2,
                       ab + sh * 1024 + (lane & 7) * 128 + (lane >> 3) * 32768,
                       PART_OFF + (tb * 144 + j) * 16384 + g * 1024 + (h * 16 + c) * 16};
    const u32x8 vb = {g < 2 ? tok * 1152 + (((j >> 1) & 1) * 32 + g * 16 + 4 * h) * 2 : OOB,
                      t1 + (q18 + jodd) * 128,
                      t1 + (q4 < 5 ? q4 : 0) * 128,
                      FLAG_OFF + tb * 1024 + (j >> 2) * 256 + c * 4,
                      cq == 0 ? one : 0, cq == 1 ? one : 0, cq == 2 ? one : 0, cq == 3 ? one : 0};
    const u32x4 vc = {XN_OFF + tb * 147456 + (lane >> 3) * 16384 + ((idx >> 3) * 32 + h * 8 + (lane & 7)) * 128, FLAG_OFF + j * 1024 + (tb >> 2) * 256 + c * 4,
                      (eg * 64 + 2 * sh + (lane & 1)) * 1152 + (lane >> 1) * 128, (t5 * 16 + (sh >> 1)) * 1152 + (l5 < 9 ? l5 : 8) * 128};
    const u32x16 sa = cat4(rw, rwi, srd(w1, D_ * F_ * 2), srd(w2, D_ * F_ * 2));
    const u32x16 sb = cat4(rws, srd(cr, S_ * S_ * 2), srd(ci, S_ * S_ * 2), srd(x, S_ * D_ * 2));
    const u32x4 hs = {ldsb + h * 1024, ldsb + h * 3072, ldsb + h * 5120, ldsb + h * 4096};
    const u32x8 sc = __builtin_shufflevector(srd(out, S_ * D_ * 2), hs, 0, 1, 2, 3, 4, 5, 6, 7);
    asm volatile(
"  s_mov_b32 s81, 4194304\n"
"  s_barrier\n"
"  s_waitcnt vmcnt(7)\n"
"  s_add_u32 m0, s72, 98304\n"
"  s_mov_b32 s85, 256\n"
"  buffer_load_dwordx4 v0, s[36:39], s85 offen lds\n"
"  s_waitcnt vmcnt(7)\n"
"  v_mov_b32 v28, 1\n"
"  ds_max_u32 v11, v28\n"
"  s_add_u32 m0, s72, 102400\n"
"  s_mov_b32 s85, 37120\n"
"  buffer_load_dwordx4 v0, s[36:39], s85 offen lds\n"
"  s_waitcnt vmcnt(7)\n"
"  s_add_u32 m0, s72, 149504\n"
"  s_mov_b32 s85, 256\n"
"  buffer_load_dwordx4 v1, s[40:43], s85 offen lds\n"
"  s_waitcnt vmcnt(7)\n"
"  s_add_u32 m0, s75, 106496\n"
"  s_mov_b32 s85, 32768\n"
"  buffer_load_dwordx4 v2, s[52:55], s85 offen lds\n"
"  s_waitcnt vmcnt(7)\n"
"  buffer_load_dwordx4 v2, s[52:55], s85 offen offset:1024 lds\n"
"  s_waitcnt vmcnt(7)\n"
"  buffer_load_dwordx4 v2, s[52:55], s85 offen offset:2048 lds\n"
"  s_waitcnt vmcnt(7)\n"
"  buffer_load_dwordx4 v2, s[52:55], s85 offen offset:3072 lds\n"
"  s_waitcnt vmcnt(7)\n"
"  v_mov_b32 v28, 2\n"
"  ds_max_u32 v11, v28\n"
"  s_add_u32 m0, s72, 0\n"
"  s_mov_b32 s85, 0\n"
"  buffer_load_dwordx4 v3, s[44:47], s85 offen lds\n"
"  s_waitcnt vmcnt(7)\n"
"  s_add_u32 m0, s72, 4096\n"
"  s_mov_b32 s85, 294912\n"
"  buffer_load_dwordx4 v3, s[44:47], s85 offen lds\n"
"  s_waitcnt vmcnt(7)\n"
"  s_add_u32 m0, s72, 8192\n"
"  s_mov_b32 s85, 589824\n"
"  buffer_load_dwordx4 v3, s[44:47], s85 offen lds\n"
"  s_waitcnt vmcnt(7)\n"
"  s_add_u32 m0, s72, 12288\n"
"  s_mov_b32 s85, 884736\n"
"  buffer_load_dwordx4 v3, s[44:47], s85 offen lds\n"
"  s_waitcnt vmcnt(7)\n"
"  s_add_u32 m0, s72, 16384\n"
"  s_mov_b32 s85, 1179648\n"
"  buffer_load_dwordx4 v3, s[44:47], s85 offen lds\n"
"  s_waitcnt vmcnt(7)\n"
"  s_add_u32 m0, s72, 20480\n"
"  s_mov_b32 s85, 1474560\n"
"  buffer_load_dwordx4 v3, s[44:47], s85 offen lds\n"
"  s_waitcnt vmcnt(7)\n"
"  s_add_u32 m0, s72, 24576\n"
"  s_mov_b32 s85, 384\n"
"  buffer_load_dwordx4 v0, s[36:39], s85 offen lds\n"
"  s_waitcnt vmcnt(7)\n"
"  v_mov_b32 v28, 3\n"
"  ds_max_u32 v11, v28\n"
"  s_add_u32 m0, s72, 28672\n"
"  s_mov_b32 s85, 37248\n"
"  buffer_load_dwordx4 v0, s[36:39], s85 offen lds\n"
"  s_waitcnt vmcnt(7)\n"
"  s_add_u32 m0, s75, 32768\n"
"  s_mov_b32 s85, 49152\n"
"  buffer_load_dwordx4 v2, s[52:55], s85 offen lds\n"
"  s_waitcnt vmcnt(7)\n"
"  buffer_load_dwordx4 v2, s[52:55], s85 offen offset:1024 lds\n"
"  ds_read_b32 v29, v10\n"
"  s_waitcnt vmcnt(7)\n"
"  buffer_load_dwordx4 v2, s[52:55], s85 offen offset:2048 lds\n"
"  s_waitcnt vmcnt(7)\n"
"  buffer_load_dwordx4 v2, s[52:55], s85 offen offset:3072 lds\n"
"  s_waitcnt lgkmcnt(0)\n"
"  v_cmp_gt_u32 vcc, 1, v29\n"
"  s_cbranch_vccz .Lh_go1\n"
"  s_waitcnt vmcnt(6)\n"
"  v_mov_b32 v28, 4\n"
"  ds_max_u32 v11, v28\n"
"  ds_read_b32 v29, v10\n"
"  s_waitcnt lgkmcnt(0)\n"
"  v_cmp_gt_u32 vcc, 1, v29\n"
"  s_cbranch_vccz .Lh_go1\n"
"  s_waitcnt vmcnt(0)\n"
"  v_mov_b32 v28, 5\n"
"  ds_max_u32 v11, v28\n"
".Lh_sp1:\n"
"  ds_read_b32 v29, v10\n"
"  s_waitcnt lgkmcnt(0)\n"
"  v_cmp_gt_u32 vcc, 1, v29\n"
"  s_cbranch_vccz .Lh_go1\n"
"  s_sleep 1\n"
"  s_sub_u32 s81, s81, 1\n"
"  s_cbranch_scc0 .Lh_sp1\n"
"  s_branch .Lh_end\n"
".Lh_go1:\n"
"  s_waitcnt vmcnt(7)\n"
"  s_add_u32 m0, s72, 49152\n"
"  s_mov_b32 s85, 512\n"
"  buffer_load_dwordx4 v0, s[36:39], s85 offen lds\n"
"  s_waitcnt vmcnt(7)\n"
"  v_mov_b32 v28, 4\n"
"  ds_max_u32 v11, v28\n"
"  s_add_u32 m0, s72, 53248\n"
"  s_mov_b32 s85, 37376\n"
"  buffer_load_dwordx4 v0, s[36:39], s85 offen lds\n"
"  s_waitcnt vmcnt(7)\n"
"  s_add_u32 m0, s72, 153600\n"
"  s_mov_b32 s85, 512\n"
"  buffer_load_dwordx4 v1, s[40:43], s85 offen lds\n"
"  s_waitcnt vmcnt(7)\n"
"  s_add_u32 m0, s75, 57344\n"
"  s_mov_b32 s85, 65536\n"
"  buffer_load_dwordx4 v2, s[52:55], s85 offen lds\n"
"  s_waitcnt vmcnt(7)\n"
"  buffer_load_dwordx4 v2, s[52:55], s85 offen offset:1024 lds\n"
"  ds_read_b32 v29, v10\n"
"  s_waitcnt vmcnt(7)\n"
"  buffer_load_dwordx4 v2, s[52:55], s85 offen offset:2048 lds\n"
"  s_waitcnt vmcnt(7)\n"
"  buffer_load_dwordx4 v2, s[52:55], s85 offen offset:3072 lds\n"
"  s_waitcnt lgkmcnt(0)\n"
"  v_cmp_gt_u32 vcc, 2, v29\n"
"  s_cbranch_vccz .Lh_go2\n"
"  s_waitcnt vmcnt(7)\n"
"  v_mov_b32 v28, 5\n"
"  ds_max_u32 v11, v28\n"
"  ds_read_b32 v29, v10\n"
"  s_waitcnt lgkmcnt(0)\n"
"  v_cmp_gt_u32 vcc, 2, v29\n"
"  s_cbranch_vccz .Lh_go2\n"
"  s_waitcnt vmcnt(0)\n"
"  v_mov_b32 v28, 6\n"
"  ds_max_u32 v11, v28\n"
".Lh_sp2:\n"
"  ds_read_b32 v29, v10\n"
"  s_waitcnt lgkmcnt(0)\n"
"  v_cmp_gt_u32 vcc, 2, v29\n"
"  s_cbranch_vccz .Lh_go2\n"
"  s_sleep 1\n"
"  s_sub_u32 s81, s81, 1\n"
"  s_cbranch_scc0 .Lh_sp2\n"
"  s_branch .Lh_end\n"
".Lh_go2:\n"
"  s_waitcnt vmcnt(7)\n"
"  v_mov_b32 v28, 5\n"
"  ds_max_u32 v11, v28\n"
"  s_add_u32 m0, s72, 73728\n"
"  s_mov_b32 s85, 640\n"
"  buffer_load_dwordx4 v0, s[36:39], s85 offen lds\n"
"  s_waitcnt vmcnt(7)\n"
"  s_add_u32 m0, s72, 77824\n"
"  s_mov_b32 s85, 37504\n"
"  buffer_load_dwordx4 v0, s[36:39], s85 offen lds\n"
"  s_waitcnt vmcnt(7)\n"
"  s_add_u32 m0, s75, 81920\n"
"  s_mov_b32 s85, 81920\n"
"  buffer_load_dwordx4 v2, s[52:55], s85 offen lds\n"
"  s_waitcnt vmcnt(7)\n"
"  buffer_load_dwordx4 v2, s[52:55], s85 offen offset:1024 lds\n"
"  ds_read_b32 v29, v10\n"
"  s_waitcnt vmcnt(7)\n"
"  buffer_load_dwordx4 v2, s[52:55], s85 offen offset:2048 lds\n"
"  s_waitcnt vmcnt(7)\n"
"  buffer_load_dwordx4 v2, s[52:55], s85 offen offset:3072 lds\n"
"  s_waitcnt lgkmcnt(0)\n"
"  v_cmp_gt_u32 vcc, 3, v29\n"
"  s_cbranch_vccz .Lh_go3\n"
"  s_waitcnt vmcnt(6)\n"
"  v_mov_b32 v28, 6\n"
"  ds_max_u32 v11, v28\n"
"  ds_read_b32 v29, v10\n"
"  s_waitcnt lgkmcnt(0)\n"
"  v_cmp_gt_u32 vcc, 3, v29\n"
"  s_cbranch_vccz .Lh_go3\n"
"  s_waitcnt vmcnt(0)\n"
"  v_mov_b32 v28, 7\n"
"  ds_max_u32 v11, v28\n"
".Lh_sp3:\n"
"  ds_read_b32 v29, v10\n"
"  s_waitcnt lgkmcnt(0)\n"
"  v_cmp_gt_u32 vcc, 3, v29\n"
"  s_cbranch_vccz .Lh_go3\n"
"  s_sleep 1\n"
"  s_sub_u32 s81, s81, 1\n"
"  s_cbranch_scc0 .Lh_sp3\n"
"  s_branch .Lh_end\n"
".Lh_go3:\n"
"  s_waitcnt vmcnt(7)\n"
"  s_add_u32 m0, s72, 98304\n"
"  s_mov_b32 s85, 1769472\n"
"  buffer_load_dwordx4 v3, s[44:47], s85 offen lds\n"
"  s_waitcnt vmcnt(7)\n"
"  v_mov_b32 v28, 6\n"
"  ds_max_u32 v11, v28\n"
"  s_add_u32 m0, s72, 102400\n"
"  s_mov_b32 s85, 2064384\n"
"  buffer_load_dwordx4 v3, s[44:47], s85 offen lds\n"
"  s_waitcnt vmcnt(7)\n"
"  s_add_u32 m0, s72, 106496\n"
"  s_mov_b32 s85, 2359296\n"
"  buffer_load_dwordx4 v3, s[44:47], s85 offen lds\n"
"  s_waitcnt vmcnt(7)\n"
"  s_add_u32 m0, s72, 110592\n"
"  s_mov_b32 s85, 2654208\n"
"  buffer_load_dwordx4 v3, s[44:47], s85 offen lds\n"
"  ds_read_b32 v29, v10\n"
"  s_waitcnt vmcnt(7)\n"
"  s_add_u32 m0, s72, 114688\n"
"  s_mov_b32 s85, 2949120\n"
"  buffer_load_dwordx4 v3, s[44:47], s85 offen lds\n"
"  s_waitcnt vmcnt(7)\n"
"  s_add_u32 m0, s72, 118784\n"
"  s_mov_b32 s85, 3244032\n"
"  buffer_load_dwordx4 v3, s[44:47], s85 offen lds\n"
"  s_waitcnt lgkmcnt(0)\n"
"  v_cmp_gt_u32 vcc, 4, v29\n"
"  s_cbranch_vccz .Lh_go4\n"
"  s_waitcnt vmcnt(6)\n"
"  v_mov_b32 v28, 7\n"
"  ds_max_u32 v11, v28\n"
"  ds_read_b32 v29, v10\n"
"  s_waitcnt lgkmcnt(0)\n"
"  v_cmp_gt_u32 vcc, 4, v29\n"
"  s_cbranch_vccz .Lh_go4\n"
"  s_waitcnt vmcnt(0)\n"
"  v_mov_b32 v28, 8\n"
"  ds_max_u32 v11, v28\n"
".Lh_sp4:\n"
"  ds_read_b32 v29, v10\n"
"  s_waitcnt lgkmcnt(0)\n"
"  v_cmp_gt_u32 vcc, 4, v29\n"
"  s_cbranch_vccz .Lh_go4\n"
"  s_sleep 1\n"
"  s_sub_u32 s81, s81, 1\n"
"  s_cbranch_scc0 .Lh_sp4\n"
"  s_branch .Lh_end\n"
".Lh_go4:\n"
"  s_waitcnt vmcnt(7)\n"
"  s_add_u32 m0, s72, 0\n"
"  s_mov_b32 s85, 768\n"
"  buffer_load_dwordx4 v0, s[36:39], s85 offen lds\n"
"  s_waitcnt vmcnt(7)\n"
"  v_mov_b32 v28, 7\n"
"  ds_max_u32 v11, v28\n"
"  s_add_u32 m0, s72, 4096\n"
"  s_mov_b32 s85, 37632\n"
"  buffer_load_dwordx4 v0, s[36:39], s85 offen lds\n"
"  s_waitcnt vmcnt(7)\n"
"  s_add_u32 m0, s72, 157696\n"
"  s_mov_b32 s85, 768\n"
"  buffer_load_dwordx4 v1, s[40:43], s85 offen lds\n"
"  s_waitcnt vmcnt(7)\n"
"  s_add_u32 m0, s75, 8192\n"
"  s_mov_b32 s85, 98304\n"
"  buffer_load_dwordx4 v2, s[52:55], s85 offen lds\n"
"  s_waitcnt vmcnt(7)\n"
"  buffer_load_dwordx4 v2, s[52:55], s85 offen offset:1024 lds\n"
"  ds_read_b32 v29, v10\n"
"  s_waitcnt vmcnt(7)\n"
"  buffer_load_dwordx4 v2, s[52:55], s85 offen offset:2048 lds\n"
"  s_waitcnt vmcnt(7)\n"
"  buffer_load_dwordx4 v2, s[52:55], s85 offen offset:3072 lds\n"
"  s_waitcnt lgkmcnt(0)\n"
"  v_cmp_gt_u32 vcc, 5, v29\n"
"  s_cbranch_vccz .Lh_go5\n"
"  s_waitcnt vmcnt(7)\n"
"  v_mov_b32 v28, 8\n"
"  ds_max_u32 v11, v28\n"
"  ds_read_b32 v29, v10\n"
"  s_waitcnt lgkmcnt(0)\n"
"  v_cmp_gt_u32 vcc, 5, v29\n"
"  s_cbranch_vccz .Lh_go5\n"
"  s_waitcnt vmcnt(0)\n"
"  v_mov_b32 v28, 9\n"
"  ds_max_u32 v11, v28\n"
".Lh_sp5:\n"
"  ds_read_b32 v29, v10\n"
"  s_waitcnt lgkmcnt(0)\n"
"  v_cmp_gt_u32 vcc, 5, v29\n"
"  s_cbranch_vccz .Lh_go5\n"
"  s_sleep 1\n"
"  s_sub_u32 s81, s81, 1\n"
"  s_cbranch_scc0 .Lh_sp5\n"
"  s_branch .Lh_end\n"
".Lh_go5:\n"
"  s_waitcnt vmcnt(7)\n"
"  v_mov_b32 v28, 8\n"
"  ds_max_u32 v11, v28\n"
"  s_add_u32 m0, s72, 24576\n"
"  s_mov_b32 s85, 896\n"
"  buffer_load_dwordx4 v0, s[36:39], s85 offen lds\n"
"  s_waitcnt vmcnt(7)\n"
"  s_add_u32 m0, s72, 28672\n"
"  s_mov_b32 s85, 37760\n"
"  buffer_load_dwordx4 v0, s[36:39], s85 offen lds\n"
"  s_waitcnt vmcnt(7)\n"
"  s_add_u32 m0, s75, 32768\n"
"  s_mov_b32 s85, 114688\n"
"  buffer_load_dwordx4 v2, s[52:55], s85 offen lds\n"
"  s_waitcnt vmcnt(7)\n"
"  buffer_load_dwordx4 v2, s[52:55], s85 offen offset:1024 lds\n"
"  ds_read_b32 v29, v10\n"
"  s_waitcnt vmcnt(7)\n"
"  buffer_load_dwordx4 v2, s[52:55], s85 offen offset:2048 lds\n"
"  s_waitcnt vmcnt(7)\n"
"  buffer_load_dwordx4 v2, s[52:55], s85 offen offset:3072 lds\n"
"  s_waitcnt lgkmcnt(0)\n"
"  v_cmp_gt_u32 vcc, 6, v29\n"
"  s_cbranch_vccz .Lh_go6\n"
"  s_waitcnt vmcnt(6)\n"
"  v_mov_b32 v28, 9\n"
"  ds_max_u32 v11, v28\n"
"  ds_read_b32 v29, v10\n"
"  s_waitcnt lgkmcnt(0)\n"
"  v_cmp_gt_u32 vcc, 6, v29\n"
"  s_cbranch_vccz .Lh_go6\n"
"  s_waitcnt vmcnt(0)\n"
"  v_mov_b32 v28, 10\n"
"  ds_max_u32 v11, v28\n"
".Lh_sp6:\n"
"  ds_read_b32 v29, v10\n"
"  s_waitcnt lgkmcnt(0)\n"
"  v_cmp_gt_u32 vcc, 6, v29\n"
"  s_cbranch_vccz .Lh_go6\n"
"  s_sleep 1\n"
"  s_sub_u32 s81, s81, 1\n"
"  s_cbranch_scc0 .Lh_sp6\n"
"  s_branch .Lh_end\n"
".Lh_go6:\n"
"  s_waitcnt vmcnt(7)\n"
"  s_add_u32 m0, s72, 49152\n"
"  s_mov_b32 s85, 1024\n"
"  buffer_load_dwordx4 v0, s[36:39], s85 offen lds\n"
"  s_waitcnt vmcnt(7)\n"
"  v_mov_b32 v28, 9\n"
"  ds_max_u32 v11, v28\n"
"  s_add_u32 m0, s72, 53248\n"
"  s_mov_b32 s85, 37888\n"
"  buffer_load_dwordx4 v0, s[36:39], s85 offen lds\n"
"  s_waitcnt vmcnt(7)\n"
"  s_add_u32 m0, s72, 159744\n"
"  s_mov_b32 s85, 896\n"
"  buffer_load_dwordx4 v1, s[40:43], s85 offen lds\n"
"  s_waitcnt vmcnt(7)\n"
"  s_add_u32 m0, s75, 57344\n"
"  s_mov_b32 s85, 131072\n"
"  buffer_load_dwordx4 v2, s[52:55], s85 offen lds\n"
"  s_waitcnt vmcnt(7)\n"
"  buffer_load_dwordx4 v2, s[52:55], s85 offen offset:1024 lds\n"
"  ds_read_b32 v29, v10\n"
"  s_waitcnt vmcnt(7)\n"
"  buffer_load_dwordx4 v2, s[52:55], s85 offen offset:2048 lds\n"
"  s_waitcnt vmcnt(7)\n"
"  buffer_load_dwordx4 v2, s[52:55], s85 offen offset:3072 lds\n"
"  s_waitcnt lgkmcnt(0)\n"
"  v_cmp_gt_u32 vcc, 7, v29\n"
"  s_cbranch_vccz .Lh_go7\n"
"  s_waitcnt vmcnt(7)\n"
"  v_mov_b32 v28, 10\n"
"  ds_max_u32 v11, v28\n"
"  ds_read_b32 v29, v10\n"
"  s_waitcnt lgkmcnt(0)\n"
"  v_cmp_gt_u32 vcc, 7, v29\n"
"  s_cbranch_vccz .Lh_go7\n"
"  s_waitcnt vmcnt(0)\n"
"  v_mov_b32 v28, 11\n"
"  ds_max_u32 v11, v28\n"
".Lh_sp7:\n"
"  ds_read_b32 v29, v10\n"
"  s_waitcnt lgkmcnt(0)\n"
"  v_cmp_gt_u32 vcc, 7, v29\n"
"  s_cbranch_vccz .Lh_go7\n"
"  s_sleep 1\n"
"  s_sub_u32 s81, s81, 1\n"
"  s_cbranch_scc0 .Lh_sp7\n"
"  s_branch .Lh_end\n"
".Lh_go7:\n"
"  s_waitcnt vmcnt(7)\n"
"  v_mov_b32 v28, 10\n"
"  ds_max_u32 v11, v28\n"
"  s_add_u32 m0, s72, 73728\n"
"  s_mov_b32 s85, 3538944\n"
"  buffer_load_dwordx4 v3, s[44:47], s85 offen lds\n"
"  s_waitcnt vmcnt(7)\n"
"  s_add_u32 m0, s72, 77824\n"
"  s_mov_b32 s85, 3833856\n"
"  buffer_load_dwordx4 v3, s[44:47], s85 offen lds\n"
"  s_waitcnt vmcnt(7)\n"
"  s_add_u32 m0, s72, 81920\n"
"  s_mov_b32 s85, 4128768\n"
"  buffer_load_dwordx4 v3, s[44:47], s85 offen lds\n"
"  s_waitcnt vmcnt(7)\n"
"  s_add_u32 m0, s72, 86016\n"
"  s_mov_b32 s85, 4423680\n"
"  buffer_load_dwordx4 v3, s[44:47], s85 offen lds\n"
"  ds_read_b32 v29, v10\n"
"  s_waitcnt vmcnt(7)\n"
"  s_add_u32 m0, s72, 90112\n"
"  s_mov_b32 s85, 4718592\n"
"  buffer_load_dwordx4 v3, s[44:47], s85 offen lds\n"
"  s_waitcnt vmcnt(7)\n"
"  s_add_u32 m0, s72, 94208\n"
"  s_mov_b32 s85, 5013504\n"
"  buffer_load_dwordx4 v3, s[44:47], s85 offen lds\n"
"  s_waitcnt lgkmcnt(0)\n"
"  v_cmp_gt_u32 vcc, 8, v29\n"
"  s_cbranch_vccz .Lh_go8\n"
"  s_waitcnt vmcnt(6)\n"
"  v_mov_b32 v28, 11\n"
"  ds_max_u32 v11, v28\n"
"  ds_read_b32 v29, v10\n"
"  s_waitcnt lgkmcnt(0)\n"
"  v_cmp_gt_u32 vcc, 8, v29\n"
"  s_cbranch_vccz .Lh_go8\n"
"  s_waitcnt vmcnt(0)\n"
"  v_mov_b32 v28, 12\n"
"  ds_max_u32 v11, v28\n"
".Lh_sp8:\n"
"  ds_read_b32 v29, v10\n"
"  s_waitcnt lgkmcnt(0)\n"
"  v_cmp_gt_u32 vcc, 8, v29\n"
"  s_cbranch_vccz .Lh_go8\n"
"  s_sleep 1\n"
"  s_sub_u32 s81, s81, 1\n"
"  s_cbranch_scc0 .Lh_sp8\n"
"  s_branch .Lh_end\n"
".Lh_go8:\n"
"  s_waitcnt vmcnt(7)\n"
"  s_add_u32 m0, s72, 98304\n"
"  s_mov_b32 s85, 128\n"
"  buffer_load_dwordx4 v3, s[44:47], s85 offen lds\n"
"  s_waitcnt vmcnt(7)\n"
"  v_mov_b32 v28, 11\n"
"  ds_max_u32 v11, v28\n"
"  s_add_u32 m0, s72, 102400\n"
"  s_mov_b32 s85, 295040\n"
"  buffer_load_dwordx4 v3, s[44:47], s85 offen lds\n"
"  s_waitcnt vmcnt(7)\n"
"  s_add_u32 m0, s72, 106496\n"
"  s_mov_b32 s85, 589952\n"
"  buffer_load_dwordx4 v3, s[44:47], s85 offen lds\n"
"  s_waitcnt vmcnt(7)\n"
"  s_add_u32 m0, s72, 110592\n"
"  s_mov_b32 s85, 884864\n"
"  buffer_load_dwordx4 v3, s[44:47], s85 offen lds\n"
"  ds_read_b32 v29, v10\n"
"  s_waitcnt vmcnt(7)\n"
"  s_add_u32 m0, s72, 114688\n"
"  s_mov_b32 s85, 1179776\n"
"  buffer_load_dwordx4 v3, s[44:47], s85 offen lds\n"
"  s_waitcnt vmcnt(7)\n"
"  s_add_u32 m0, s72, 118784\n"
"  s_mov_b32 s85, 1474688\n"
"  buffer_load_dwordx4 v3, s[44:47], s85 offen lds\n"
"  s_waitcnt lgkmcnt(0)\n"
"  v_cmp_gt_u32 vcc, 9, v29\n"
"  s_cbranch_vccz .Lh_go9\n"
"  s_waitcnt vmcnt(6)\n"
"  v_mov_b32 v28, 12\n"
"  ds_max_u32 v11, v28\n"
"  ds_read_b32 v29, v10\n"
"  s_waitcnt lgkmcnt(0)\n"
"  v_cmp_gt_u32 vcc, 9, v29\n"
"  s_cbranch_vccz .Lh_go9\n"
"  s_waitcnt vmcnt(0)\n"
"  v_mov_b32 v28, 13\n"
"  ds_max_u32 v11, v28\n"
".Lh_sp9:\n"
"  ds_read_b32 v29, v10\n"
"  s_waitcnt lgkmcnt(0)\n"
"  v_cmp_gt_u32 vcc, 9, v29\n"
"  s_cbranch_vccz .Lh_go9\n"
"  s_sleep 1\n"
"  s_sub_u32 s81, s81, 1\n"
"  s_cbranch_scc0 .Lh_sp9\n"
"  s_branch .Lh_end\n"
".Lh_go9:\n"
"  s_waitcnt vmcnt(7)\n"
"  s_add_u32 m0, s72, 0\n"
"  s_mov_b32 s85, 1769600\n"
"  buffer_load_dwordx4 v3, s[44:47], s85 offen lds\n"
"  s_waitcnt vmcnt(7)\n"
"  v_mov_b32 v28, 12\n"
"  ds_max_u32 v11, v28\n"
"  s_add_u32 m0, s72, 4096\n"
"  s_mov_b32 s85, 2064512\n"
"  buffer_load_dwordx4 v3, s[44:47], s85 offen lds\n"
"  s_waitcnt vmcnt(7)\n"
"  s_add_u32 m0, s72, 8192\n"
"  s_mov_b32 s85, 2359424\n"
"  buffer_load_dwordx4 v3, s[44:47], s85 offen lds\n"
"  s_waitcnt vmcnt(7)\n"
"  s_add_u32 m0, s72, 12288\n"
"  s_mov_b32 s85, 2654336\n"
"  buffer_load_dwordx4 v3, s[44:47], s85 offen lds\n"
"  ds_read_b32 v29, v10\n"
"  s_waitcnt vmcnt(7)\n"
"  s_add_u32 m0, s72, 16384\n"
"  s_mov_b32 s85, 2949248\n"
"  buffer_load_dwordx4 v3, s[44:47], s85 offen lds\n"
"  s_waitcnt vmcnt(7)\n"
"  s_add_u32 m0, s72, 20480\n"
"  s_mov_b32 s85, 3244160\n"
"  buffer_load_dwordx4 v3, s[44:47], s85 offen lds\n"
"  s_waitcnt lgkmcnt(0)\n"
"  v_cmp_gt_u32 vcc, 10, v29\n"
"  s_cbranch_vccz .Lh_go10\n"
"  s_waitcnt vmcnt(6)\n"
"  v_mov_b32 v28, 13\n"
"  ds_max_u32 v11, v28\n"
"  ds_read_b32 v29, v10\n"
"  s_waitcnt lgkmcnt(0)\n"
"  v_cmp_gt_u32 vcc, 10, v29\n"
"  s_cbranch_vccz .Lh_go10\n"
"  s_waitcnt vmcnt(0)\n"
"  v_mov_b32 v28, 14\n"
"  ds_max_u32 v11, v28\n"
".Lh_sp10:\n"
"  ds_read_b32 v29, v10\n"
"  s_waitcnt lgkmcnt(0)\n"
"  v_cmp_gt_u32 vcc, 10, v29\n"
"  s_cbranch_vccz .Lh_go10\n"
"  s_sleep 1\n"
"  s_sub_u32 s81, s81, 1\n"
"  s_cbranch_scc0 .Lh_sp10\n"
"  s_branch .Lh_end\n"
".Lh_go10:\n"
"  s_waitcnt vmcnt(7)\n"
"  s_add_u32 m0, s72, 24576\n"
"  s_mov_b32 s85, 3539072\n"
"  buffer_load_dwordx4 v3, s[44:47], s85 offen lds\n"
"  s_waitcnt vmcnt(7)\n"
"  v_mov_b32 v28, 13\n"
"  ds_max_u32 v11, v28\n"
"  s_add_u32 m0, s72, 28672\n"
"  s_mov_b32 s85, 3833984\n"
"  buffer_load_dwordx4 v3, s[44:47], s85 offen lds\n"
"  s_waitcnt vmcnt(7)\n"
"  s_add_u32 m0, s72, 32768\n"
"  s_mov_b32 s85, 4128896\n"
"  buffer_load_dwordx4 v3, s[44:47], s85 offen lds\n"
"  s_waitcnt vmcnt(7)\n"
"  s_add_u32 m0, s72, 36864\n"
"  s_mov_b32 s85, 4423808\n"
"  buffer_load_dwordx4 v3, s[44:47], s85 offen lds\n"
"  ds_read_b32 v29, v10\n"
"  s_waitcnt vmcnt(7)\n"
"  s_add_u32 m0, s72, 40960\n"
"  s_mov_b32 s85, 4718720\n"
"  buffer_load_dwordx4 v3, s[44:47], s85 offen lds\n"
"  s_waitcnt vmcnt(7)\n"
"  s_add_u32 m0, s72, 45056\n"
"  s_mov_b32 s85, 5013632\n"
"  buffer_load_dwordx4 v3, s[44:47], s85 offen lds\n"
"  s_waitcnt lgkmcnt(0)\n"
"  v_cmp_gt_u32 vcc, 11, v29\n"
"  s_cbranch_vccz .Lh_go11\n"
"  s_waitcnt vmcnt(6)\n"
"  v_mov_b32 v28, 14\n"
"  ds_max_u32 v11, v28\n"
"  ds_read_b32 v29, v10\n"
"  s_waitcnt lgkmcnt(0)\n"
"  v_cmp_gt_u32 vcc, 11, v29\n"
"  s_cbranch_vccz .Lh_go11\n"
"  s_waitcnt vmcnt(0)\n"
"  v_mov_b32 v28, 15\n"
"  ds_max_u32 v11, v28\n"
".Lh_sp11:\n"
"  ds_read_b32 v29, v10\n"
"  s_waitcnt lgkmcnt(0)\n"
"  v_cmp_gt_u32 vcc, 11, v29\n"
"  s_cbranch_vccz .Lh_go11\n"
"  s_sleep 1\n"
"  s_sub_u32 s81, s81, 1\n"
"  s_cbranch_scc0 .Lh_sp11\n"
"  s_branch .Lh_end\n"
".Lh_go11:\n"
"  s_waitcnt vmcnt(7)\n"
"  s_add_u32 m0, s72, 49152\n"
"  s_mov_b32 s85, 256\n"
"  buffer_load_dwordx4 v3, s[44:47], s85 offen lds\n"
"  s_waitcnt vmcnt(7)\n"
"  v_mov_b32 v28, 14\n"
"  ds_max_u32 v11, v28\n"
"  s_add_u32 m0, s72, 53248\n"
"  s_mov_b32 s85, 295168\n"
"  buffer_load_dwordx4 v3, s[44:47], s85 offen lds\n"
"  s_waitcnt vmcnt(7)\n"
"  s_add_u32 m0, s72, 57344\n"
"  s_mov_b32 s85, 590080\n"
"  buffer_load_dwordx4 v3, s[44:47], s85 offen lds\n"
"  s_waitcnt vmcnt(7)\n"
"  s_add_u32 m0, s72, 61440\n"
"  s_mov_b32 s85, 884992\n"
"  buffer_load_dwordx4 v3, s[44:47], s85 offen lds\n"
"  ds_read_b32 v29, v10\n"
"  s_waitcnt vmcnt(7)\n"
"  s_add_u32 m0, s72, 65536\n"
"  s_mov_b32 s85, 1179904\n"
"  buffer_load_dwordx4 v3, s[44:47], s85 offen lds\n"
"  s_waitcnt vmcnt(7)\n"
"  s_add_u32 m0, s72, 69632\n"
"  s_mov_b32 s85, 1474816\n"
"  buffer_load_dwordx4 v3, s[44:47], s85 offen lds\n"
"  s_waitcnt lgkmcnt(0)\n"
"  v_cmp_gt_u32 vcc, 12, v29\n"
"  s_cbranch_vccz .Lh_go12\n"
"  s_waitcnt vmcnt(6)\n"
"  v_mov_b32 v28, 15\n"
"  ds_max_u32 v11, v28\n"
"  ds_read_b32 v29, v10\n"
"  s_waitcnt lgkmcnt(0)\n"
"  v_cmp_gt_u32 vcc, 12, v29\n"
"  s_cbranch_vccz .Lh_go12\n"
"  s_waitcnt vmcnt(0)\n"
"  v_mov_b32 v28, 16\n"
"  ds_max_u32 v11, v28\n"
".Lh_sp12:\n"
"  ds_read_b32 v29, v10\n"
"  s_waitcnt lgkmcnt(0)\n"
"  v_cmp_gt_u32 vcc, 12, v29\n"
"  s_cbranch_vccz .Lh_go12\n"
"  s_sleep 1\n"
"  s_sub_u32 s81, s81, 1\n"
"  s_cbranch_scc0 .Lh_sp12\n"
"  s_branch .Lh_end\n"
".Lh_go12:\n"
"  s_waitcnt vmcnt(7)\n"
"  s_add_u32 m0, s72, 73728\n"
"  s_mov_b32 s85, 1769728\n"
"  buffer_load_dwordx4 v3, s[44:47], s85 offen lds\n"
"  s_waitcnt vmcnt(7)\n"
"  v_mov_b32 v28, 15\n"
"  ds_max_u32 v11, v28\n"
"  s_add_u32 m0, s72, 77824\n"
"  s_mov_b32 s85, 2064640\n"
"  buffer_load_dwordx4 v3, s[44:47], s85 offen lds\n"
"  s_waitcnt vmcnt(7)\n"
"  s_add_u32 m0, s72, 81920\n"
"  s_mov_b32 s85, 2359552\n"
"  buffer_load_dwordx4 v3, s[44:47], s85 offen lds\n"
"  s_waitcnt vmcnt(7)\n"
"  s_add_u32 m0, s72, 86016\n"
"  s_mov_b32 s85, 2654464\n"
"  buffer_load_dwordx4 v3, s[44:47], s85 offen lds\n"
"  ds_read_b32 v29, v10\n"
"  s_waitcnt vmcnt(7)\n"
"  s_add_u32 m0, s72, 90112\n"
"  s_mov_b32 s85, 2949376\n"
"  buffer_load_dwordx4 v3, s[44:47], s85 offen lds\n"
"  s_waitcnt vmcnt(7)\n"
"  s_add_u32 m0, s72, 94208\n"
"  s_mov_b32 s85, 3244288\n"
"  buffer_load_dwordx4 v3, s[44:47], s85 offen lds\n"
"  s_waitcnt vmcnt(7)\n"
"  s_add_u32 m0, s72, 122880\n"
"  s_mov_b32 s85, 0\n"
"  buffer_load_dwordx4 v9, s[56:59], s85 offen nt lds\n"
"  s_waitcnt vmcnt(7)\n"
"  v_mov_b32 v28, 16\n"
"  ds_max_u32 v11, v28\n"
"  s_add_u32 m0, s72, 126976\n"
"  s_mov_b32 s85, 131072\n"
"  buffer_load_dwordx4 v9, s[56:59], s85 offen nt lds\n"
"  s_waitcnt vmcnt(7)\n"
"  s_add_u32 m0, s72, 131072\n"
"  s_mov_b32 s85, 262144\n"
"  buffer_load_dwordx4 v9, s[56:59], s85 offen nt lds\n"
"  s_waitcnt vmcnt(7)\n"
"  s_add_u32 m0, s72, 135168\n"
"  s_mov_b32 s85, 393216\n"
"  buffer_load_dwordx4 v9, s[56:59], s85 offen nt lds\n"
"  s_waitcnt vmcnt(7)\n"
"  s_add_u32 m0, s72, 145408\n"
"  s_mov_b32 s85, 128\n"
"  buffer_load_dwordx4 v9, s[56:59], s85 offen nt lds\n"
"  s_waitcnt vmcnt(7)\n"
"  s_add_u32 m0, s72, 149504\n"
"  s_mov_b32 s85, 131200\n"
"  buffer_load_dwordx4 v9, s[56:59], s85 offen nt lds\n"
"  s_waitcnt vmcnt(7)\n"
"  s_add_u32 m0, s72, 153600\n"
"  s_mov_b32 s85, 262272\n"
"  buffer_load_dwordx4 v9, s[56:59], s85 offen nt lds\n"
"  s_waitcnt vmcnt(7)\n"
"  v_mov_b32 v28, 17\n"
"  ds_max_u32 v11, v28\n"
"  s_add_u32 m0, s72, 157696\n"
"  s_mov_b32 s85, 393344\n"
"  buffer_load_dwordx4 v9, s[56:59], s85 offen nt lds\n"
".Lh_sp13:\n"
"  ds_read_b32 v29, v10\n"
"  s_waitcnt lgkmcnt(0)\n"
"  v_cmp_gt_u32 vcc, 13, v29\n"
"  s_cbranch_vccz .Lh_go13\n"
"  s_sleep 1\n"
"  s_sub_u32 s81, s81, 1\n"
"  s_cbranch_scc0 .Lh_sp13\n"
"  s_branch .Lh_end\n"
".Lh_go13:\n"
"  s_waitcnt vmcnt(7)\n"
"  s_add_u32 m0, s72, 98304\n"
"  s_mov_b32 s85, 3539200\n"
"  buffer_load_dwordx4 v3, s[44:47], s85 offen lds\n"
"  s_waitcnt vmcnt(7)\n"
"  s_add_u32 m0, s72, 102400\n"
"  s_mov_b32 s85, 3834112\n"
"  buffer_load_dwordx4 v3, s[44:47], s85 offen lds\n"
"  s_waitcnt vmcnt(7)\n"
"  s_add_u32 m0, s72, 106496\n"
"  s_mov_b32 s85, 4129024\n"
"  buffer_load_dwordx4 v3, s[44:47], s85 offen lds\n"
"  s_waitcnt vmcnt(7)\n"
"  s_add_u32 m0, s72, 110592\n"
"  s_mov_b32 s85, 4423936\n"
"  buffer_load_dwordx4 v3, s[44:47], s85 offen lds\n"
"  ds_read_b32 v29, v10\n"
"  s_waitcnt vmcnt(7)\n"
"  s_add_u32 m0, s72, 114688\n"
"  s_mov_b32 s85, 4718848\n"
"  buffer_load_dwordx4 v3, s[44:47], s85 offen lds\n"
"  s_waitcnt vmcnt(7)\n"
"  s_add_u32 m0, s72, 118784\n"
"  s_mov_b32 s85, 5013760\n"
"  buffer_load_dwordx4 v3, s[44:47], s85 offen lds\n"
"  s_waitcnt lgkmcnt(0)\n"
"  v_cmp_gt_u32 vcc, 14, v29\n"
"  s_cbranch_vccz .Lh_go14\n"
"  s_waitcnt vmcnt(0)\n"
"  v_mov_b32 v28, 18\n"
"  ds_max_u32 v11, v28\n"
".Lh_sp14:\n"
"  ds_read_b32 v29, v10\n"
"  s_waitcnt lgkmcnt(0)\n"
"  v_cmp_gt_u32 vcc, 14, v29\n"
"  s_cbranch_vccz .Lh_go14\n"
"  s_sleep 1\n"
"  s_sub_u32 s81, s81, 1\n"
"  s_cbranch_scc0 .Lh_sp14\n"
"  s_branch .Lh_end\n"
".Lh_go14:\n"
"  s_waitcnt vmcnt(7)\n"
"  s_add_u32 m0, s72, 0\n"
"  s_mov_b32 s85, 384\n"
"  buffer_load_dwordx4 v3, s[44:47], s85 offen lds\n"
"  s_waitcnt vmcnt(7)\n"
"  s_add_u32 m0, s72, 4096\n"
"  s_mov_b32 s85, 295296\n"
"  buffer_load_dwordx4 v3, s[44:47], s85 offen lds\n"
"  s_waitcnt vmcnt(7)\n"
"  s_add_u32 m0, s72, 8192\n"
"  s_mov_b32 s85, 590208\n"
"  buffer_load_dwordx4 v3, s[44:47], s85 offen lds\n"
"  s_waitcnt vmcnt(7)\n"
"  s_add_u32 m0, s72, 12288\n"
"  s_mov_b32 s85, 885120\n"
"  buffer_load_dwordx4 v3, s[44:47], s85 offen lds\n"
"  ds_read_b32 v29, v10\n"
"  s_waitcnt vmcnt(7)\n"
"  s_add_u32 m0, s72, 16384\n"
"  s_mov_b32 s85, 1180032\n"
"  buffer_load_dwordx4 v3, s[44:47], s85 offen lds\n"
"  s_waitcnt vmcnt(7)\n"
"  s_add_u32 m0, s72, 20480\n"
"  s_mov_b32 s85, 1474944\n"
"  buffer_load_dwordx4 v3, s[44:47], s85 offen lds\n"
"  s_waitcnt lgkmcnt(0)\n"
"  v_cmp_gt_u32 vcc, 15, v29\n"
"  s_cbranch_vccz .Lh_go15\n"
"  s_waitcnt vmcnt(6)\n"
"  v_mov_b32 v28, 18\n"
"  ds_max_u32 v11, v28\n"
"  ds_read_b32 v29, v10\n"
"  s_waitcnt lgkmcnt(0)\n"
"  v_cmp_gt_u32 vcc, 15, v29\n"
"  s_cbranch_vccz .Lh_go15\n"
"  s_waitcnt vmcnt(0)\n"
"  v_mov_b32 v28, 19\n"
"  ds_max_u32 v11, v28\n"
".Lh_sp15:\n"
"  ds_read_b32 v29, v10\n"
"  s_waitcnt lgkmcnt(0)\n"
"  v_cmp_gt_u32 vcc, 15, v29\n"
"  s_cbranch_vccz .Lh_go15\n"
"  s_sleep 1\n"
"  s_sub_u32 s81, s81, 1\n"
"  s_cbranch_scc0 .Lh_sp15\n"
"  s_branch .Lh_end\n"
".Lh_go15:\n"
"  s_waitcnt vmcnt(7)\n"
"  s_add_u32 m0, s72, 24576\n"
"  s_mov_b32 s85, 1769856\n"
"  buffer_load_dwordx4 v3, s[44:47], s85 offen lds\n"
"  s_waitcnt vmcnt(7)\n"
"  v_mov_b32 v28, 18\n"
"  ds_max_u32 v11, v28\n"
"  s_add_u32 m0, s72, 28672\n"
"  s_mov_b32 s85, 2064768\n"
"  buffer_load_dwordx4 v3, s[44:47], s85 offen lds\n"
"  s_waitcnt vmcnt(7)\n"
"  s_add_u32 m0, s72, 32768\n"
"  s_mov_b32 s85, 2359680\n"
"  buffer_load_dwordx4 v3, s[44:47], s85 offen lds\n"
"  s_waitcnt vmcnt(7)\n"
"  s_add_u32 m0, s72, 36864\n"
"  s_mov_b32 s85, 2654592\n"
"  buffer_load_dwordx4 v3, s[44:47], s85 offen lds\n"
"  ds_read_b32 v29, v10\n"
"  s_waitcnt vmcnt(7)\n"
"  s_add_u32 m0, s72, 40960\n"
"  s_mov_b32 s85, 2949504\n"
"  buffer_load_dwordx4 v3, s[44:47], s85 offen lds\n"
"  s_waitcnt vmcnt(7)\n"
"  s_add_u32 m0, s72, 45056\n"
"  s_mov_b32 s85, 3244416\n"
"  buffer_load_dwordx4 v3, s[44:47], s85 offen lds\n"
"  s_waitcnt vmcnt(7)\n"
"  s_mov_b32 s80, 0\n"
"  s_mov_b32 exec_lo, 0xffffffff\n"
"  s_mov_b32 exec_hi, 0x1fff\n"
"  buffer_load_dword v32, v13, s[48:51], s80 offen\n"
"  s_mov_b64 exec, -1\n"
"  s_waitcnt vmcnt(7)\n"
"  v_mov_b32 v28, 19\n"
"  ds_max_u32 v11, v28\n"
"  s_mov_b32 s80, 640\n"
"  s_mov_b32 exec_lo, 0xffffffff\n"
"  s_mov_b32 exec_hi, 0xf\n"
"  buffer_load_dword v32, v13, s[48:51], s80 offen\n"
"  s_mov_b64 exec, -1\n"
"  s_waitcnt lgkmcnt(0)\n"
"  v_cmp_gt_u32 vcc, 16, v29\n"
"  s_cbranch_vccz .Lh_go16\n"
"  s_waitcnt vmcnt(2)\n"
"  v_mov_b32 v28, 20\n"
"  ds_max_u32 v11, v28\n"
".Lh_sp16:\n"
"  ds_read_b32 v29, v10\n"
"  s_waitcnt lgkmcnt(0)\n"
"  v_cmp_gt_u32 vcc, 16, v29\n"
"  s_cbranch_vccz .Lh_go16\n"
"  s_sleep 1\n"
"  s_sub_u32 s81, s81, 1\n"
"  s_cbranch_scc0 .Lh_sp16\n"
"  s_branch .Lh_end\n"
".Lh_go16:\n"
"  s_waitcnt vmcnt(7)\n"
"  s_add_u32 m0, s72, 49152\n"
"  s_mov_b32 s85, 3539328\n"
"  buffer_load_dwordx4 v3, s[44:47], s85 offen lds\n"
"  s_waitcnt vmcnt(7)\n"
"  s_add_u32 m0, s72, 53248\n"
"  s_mov_b32 s85, 3834240\n"
"  buffer_load_dwordx4 v3, s[44:47], s85 offen lds\n"
"  s_waitcnt vmcnt(7)\n"
"  s_add_u32 m0, s72, 57344\n"
"  s_mov_b32 s85, 4129152\n"
"  buffer_load_dwordx4 v3, s[44:47], s85 offen lds\n"
"  s_waitcnt vmcnt(7)\n"
"  s_add_u32 m0, s72, 61440\n"
"  s_mov_b32 s85, 4424064\n"
"  buffer_load_dwordx4 v3, s[44:47], s85 offen lds\n"
"  ds_read_b32 v29, v10\n"
"  s_waitcnt vmcnt(7)\n"
"  s_add_u32 m0, s72, 65536\n"
"  s_mov_b32 s85, 4718976\n"
"  buffer_load_dwordx4 v3, s[44:47], s85 offen lds\n"
"  s_waitcnt vmcnt(7)\n"
"  v_mov_b32 v28, 20\n"
"  ds_max_u32 v11, v28\n"
"  s_add_u32 m0, s72, 69632\n"
"  s_mov_b32 s85, 5013888\n"
"  buffer_load_dwordx4 v3, s[44:47], s85 offen lds\n"
"  s_waitcnt lgkmcnt(0)\n"
"  v_cmp_gt_u32 vcc, 17, v29\n"
"  s_cbranch_vccz .Lh_go17\n"
"  s_waitcnt vmcnt(0)\n"
"  v_mov_b32 v28, 21\n"
"  ds_max_u32 v11, v28\n"
".Lh_sp17:\n"
"  ds_read_b32 v29, v10\n"
"  s_waitcnt lgkmcnt(0)\n"
"  v_cmp_gt_u32 vcc, 17, v29\n"
"  s_cbranch_vccz .Lh_go17\n"
"  s_sleep 1\n"
"  s_sub_u32 s81, s81, 1\n"
"  s_cbranch_scc0 .Lh_sp17\n"
"  s_branch .Lh_end\n"
".Lh_go17:\n"
"  s_waitcnt vmcnt(7)\n"
"  s_add_u32 m0, s72, 73728\n"
"  s_mov_b32 s85, 0\n"
"  buffer_load_dwordx4 v4, s[44:47], s85 offen lds\n"
"  s_waitcnt vmcnt(7)\n"
"  s_add_u32 m0, s72, 77824\n"
"  s_mov_b32 s85, 589824\n"
"  buffer_load_dwordx4 v4, s[44:47], s85 offen lds\n"
"  s_waitcnt vmcnt(7)\n"
"  s_add_u32 m0, s72, 81920\n"
"  s_mov_b32 s85, 1179648\n"
"  buffer_load_dwordx4 v4, s[44:47], s85 offen lds\n"
"  ds_read_b32 v29, v10\n"
"  s_waitcnt vmcnt(7)\n"
"  s_add_u32 m0, s72, 86016\n"
"  s_mov_b32 s85, 1769472\n"
"  buffer_load_dwordx4 v4, s[44:47], s85 offen lds\n"
"  s_waitcnt vmcnt(7)\n"
"  s_add_u32 m0, s72, 90112\n"
"  s_mov_b32 s85, 2359296\n"
"  buffer_load_dwordx4 v4, s[44:47], s85 offen lds\n"
"  s_waitcnt vmcnt(7)\n"
"  buffer_load_dword v30, v25, s[52:55], 0 offen sc1\n"
"  s_waitcnt lgkmcnt(0)\n"
"  v_cmp_gt_u32 vcc, 18, v29\n"
"  s_cbranch_vccz .Lh_go18\n"
"  s_waitcnt vmcnt(6)\n"
"  v_mov_b32 v28, 21\n"
"  ds_max_u32 v11, v28\n"
"  ds_read_b32 v29, v10\n"
"  s_waitcnt lgkmcnt(0)\n"
"  v_cmp_gt_u32 vcc, 18, v29\n"
"  s_cbranch_vccz .Lh_go18\n"
"  s_waitcnt vmcnt(1)\n"
"  v_mov_b32 v28, 22\n"
"  ds_max_u32 v11, v28\n"
".Lh_sp18:\n"
"  ds_read_b32 v29, v10\n"
"  s_waitcnt lgkmcnt(0)\n"
"  v_cmp_gt_u32 vcc, 18, v29\n"
"  s_cbranch_vccz .Lh_go18\n"
"  s_sleep 1\n"
"  s_sub_u32 s81, s81, 1\n"
"  s_cbranch_scc0 .Lh_sp18\n"
"  s_branch .Lh_end\n"
".Lh_go18:\n"
"  s_waitcnt vmcnt(7)\n"
"  s_add_u32 m0, s72, 98304\n"
"  s_mov_b32 s85, 2949120\n"
"  buffer_load_dwordx4 v4, s[44:47], s85 offen lds\n"
"  s_waitcnt vmcnt(7)\n"
"  v_mov_b32 v28, 21\n"
"  ds_max_u32 v11, v28\n"
"  s_add_u32 m0, s72, 102400\n"
"  s_mov_b32 s85, 3538944\n"
"  buffer_load_dwordx4 v4, s[44:47], s85 offen lds\n"
"  ds_read_b32 v29, v10\n"
"  s_waitcnt vmcnt(7)\n"
"  s_add_u32 m0, s72, 106496\n"
"  s_mov_b32 s85, 4128768\n"
"  buffer_load_dwordx4 v4, s[44:47], s85 offen lds\n"
"  s_waitcnt vmcnt(7)\n"
"  s_add_u32 m0, s72, 110592\n"
"  s_mov_b32 s85, 4718592\n"
"  buffer_load_dwordx4 v4, s[44:47], s85 offen lds\n"
"  s_waitcnt vmcnt(4)\n"
"  v_mov_b32 v28, 22\n"
"  ds_max_u32 v11, v28\n"
"  v_cmp_gt_u32 vcc, 1, v30\n"
"  s_cbranch_vccz .Lh_go19\n"
"  s_waitcnt vmcnt(0)\n"
"  v_mov_b32 v28, 23\n"
"  ds_max_u32 v11, v28\n"
"  s_mov_b32 s80, 0\n"
".Lh_sp19:\n"
"  buffer_load_dword v30, v25, s[52:55], s80 offen sc1\n"
"  s_waitcnt vmcnt(0)\n"
"  v_cmp_gt_u32 vcc, 1, v30\n"
"  s_cbranch_vccz .Lh_go19\n"
"  s_sleep 2\n"
"  s_sub_u32 s81, s81, 1\n"
"  s_cbranch_scc0 .Lh_sp19\n"
"  s_branch .Lh_end\n"
".Lh_go19:\n"
"  s_mov_b32 s80, 0\n"
"  s_mov_b32 exec_lo, 0xffffffff\n"
"  s_mov_b32 exec_hi, 0xff\n"
"  buffer_load_dword v33, v14, s[52:55], s80 offen sc1\n"
"  s_mov_b64 exec, -1\n"
"  s_mov_b32 s80, 163840\n"
"  s_mov_b32 exec_lo, 0xffffffff\n"
"  s_mov_b32 exec_hi, 0x0\n"
"  buffer_load_dword v33, v14, s[52:55], s80 offen sc1\n"
"  s_mov_b64 exec, -1\n"
"  s_waitcnt lgkmcnt(0)\n"
"  v_cmp_gt_u32 vcc, 19, v29\n"
"  s_cbranch_vccz .Lh_go20\n"
"  s_waitcnt vmcnt(2)\n"
"  v_mov_b32 v28, 23\n"
"  ds_max_u32 v11, v28\n"
".Lh_sp20:\n"
"  ds_read_b32 v29, v10\n"
"  s_waitcnt lgkmcnt(0)\n"
"  v_cmp_gt_u32 vcc, 19, v29\n"
"  s_cbranch_vccz .Lh_go20\n"
"  s_sleep 1\n"
"  s_sub_u32 s81, s81, 1\n"
"  s_cbranch_scc0 .Lh_sp20\n"
"  s_branch .Lh_end\n"
".Lh_go20:\n"
"  s_add_u32 m0, s72, 0\n"
"  s_mov_b32 s85, 0\n"
"  buffer_load_dwordx4 v5, s[48:51], s85 offen lds\n"
"  s_add_u32 m0, s72, 4096\n"
"  s_mov_b32 s85, 36864\n"
"  buffer_load_dwordx4 v5, s[48:51], s85 offen lds\n"
"  s_waitcnt vmcnt(7)\n"
"  s_add_u32 m0, s72, 8192\n"
"  s_mov_b32 s85, 73728\n"
"  buffer_load_dwordx4 v5, s[48:51], s85 offen lds\n"
"  s_waitcnt vmcnt(7)\n"
"  s_add_u32 m0, s72, 12288\n"
"  s_mov_b32 s85, 110592\n"
"  buffer_load_dwordx4 v5, s[48:51], s85 offen lds\n"
"  ds_read_b32 v29, v10\n"
"  s_waitcnt vmcnt(7)\n"
"  s_add_u32 m0, s72, 16384\n"
"  s_mov_b32 s85, 147456\n"
"  buffer_load_dwordx4 v5, s[48:51], s85 offen lds\n"
"  s_waitcnt vmcnt(7)\n"
"  v_mov_b32 v28, 23\n"
"  ds_max_u32 v11, v28\n"
"  s_add_u32 m0, s72, 20480\n"
"  s_mov_b32 s85, 184320\n"
"  buffer_load_dwordx4 v5, s[48:51], s85 offen lds\n"
"  s_waitcnt lgkmcnt(0)\n"
"  v_cmp_gt_u32 vcc, 20, v29\n"
"  s_cbranch_vccz .Lh_go21\n"
"  s_waitcnt vmcnt(0)\n"
"  v_mov_b32 v28, 24\n"
"  ds_max_u32 v11, v28\n"
".Lh_sp21:\n"
"  ds_read_b32 v29, v10\n"
"  s_waitcnt lgkmcnt(0)\n"
"  v_cmp_gt_u32 vcc, 20, v29\n"
"  s_cbranch_vccz .Lh_go21\n"
"  s_sleep 1\n"
"  s_sub_u32 s81, s81, 1\n"
"  s_cbranch_scc0 .Lh_sp21\n"
"  s_branch .Lh_end\n"
".Lh_go21:\n"
"  s_waitcnt vmcnt(7)\n"
"  s_add_u32 m0, s72, 24576\n"
"  s_mov_b32 s85, 221184\n"
"  buffer_load_dwordx4 v5, s[48:51], s85 offen lds\n"
"  s_waitcnt vmcnt(7)\n"
"  s_add_u32 m0, s72, 28672\n"
"  s_mov_b32 s85, 258048\n"
"  buffer_load_dwordx4 v5, s[48:51], s85 offen lds\n"
"  s_waitcnt vmcnt(7)\n"
"  s_add_u32 m0, s72, 32768\n"
"  s_mov_b32 s85, 0\n"
"  buffer_load_dwordx4 v6, s[48:51], s85 offen lds\n"
"  s_waitcnt vmcnt(7)\n"
"  s_add_u32 m0, s73, 36864\n"
"  s_mov_b32 s85, 0\n"
"  buffer_load_dwordx4 v7, s[52:55], s85 offen sc1 lds\n"
"  ds_read_b32 v29, v10\n"
"  s_waitcnt vmcnt(7)\n"
"  buffer_load_dwordx4 v7, s[52:55], s85 offen offset:1024 sc1 lds\n"
"  s_waitcnt vmcnt(7)\n"
"  buffer_load_dwordx4 v7, s[52:55], s85 offen offset:2048 sc1 lds\n"
"  s_waitcnt vmcnt(7)\n"
"  s_mov_b32 s80, 0\n"
"  buffer_load_dwordx4 v[72:75], v9, s[60:63], s80 offen nt\n"
"  s_waitcnt vmcnt(7)\n"
"  v_mov_b32 v28, 24\n"
"  ds_max_u32 v11, v28\n"
"  s_mov_b32 s80, 128\n"
"  buffer_load_dwordx4 v[76:79], v9, s[60:63], s80 offen nt\n"
"  s_waitcnt vmcnt(7)\n"
"  s_mov_b32 s80, 131072\n"
"  buffer_load_dwordx4 v[80:83], v9, s[60:63], s80 offen nt\n"
"  s_waitcnt vmcnt(7)\n"
"  s_mov_b32 s80, 131200\n"
"  buffer_load_dwordx4 v[84:87], v9, s[60:63], s80 offen nt\n"
"  s_waitcnt vmcnt(7)\n"
"  s_mov_b32 s80, 262144\n"
"  buffer_load_dwordx4 v[88:91], v9, s[60:63], s80 offen nt\n"
"  s_waitcnt vmcnt(7)\n"
"  s_mov_b32 s80, 262272\n"
"  buffer_load_dwordx4 v[92:95], v9, s[60:63], s80 offen nt\n"
"  s_waitcnt vmcnt(7)\n"
"  s_mov_b32 s80, 393216\n"
"  buffer_load_dwordx4 v[96:99], v9, s[60:63], s80 offen nt\n"
"  s_waitcnt vmcnt(7)\n"
"  v_mov_b32 v28, 25\n"
"  ds_max_u32 v11, v28\n"
"  s_mov_b32 s80, 393344\n"
"  buffer_load_dwordx4 v[100:103], v9, s[60:63], s80 offen nt\n"
".Lh_sp22:\n"
"  ds_read_b32 v29, v10\n"
"  s_waitcnt lgkmcnt(0)\n"
"  v_cmp_gt_u32 vcc, 21, v29\n"
"  s_cbranch_vccz .Lh_go22\n"
"  s_sleep 1\n"
"  s_sub_u32 s81, s81, 1\n"
"  s_cbranch_scc0 .Lh_sp22\n"
"  s_branch .Lh_end\n"
".Lh_go22:\n"
"  s_waitcnt vmcnt(7)\n"
"  s_add_u32 m0, s74, 49152\n"
"  s_mov_b32 s85, 0\n"
"  buffer_load_dwordx4 v8, s[52:55], s85 offen sc1 lds\n"
"  s_waitcnt vmcnt(7)\n"
"  buffer_load_dwordx4 v8, s[52:55], s85 offen offset:1024 sc1 lds\n"
"  s_waitcnt vmcnt(7)\n"
"  s_add_u32 m0, s74, 51200\n"
"  s_mov_b32 s85, 2048\n"
"  buffer_load_dwordx4 v8, s[52:55], s85 offen sc1 lds\n"
"  ds_read_b32 v29, v10\n"
"  s_waitcnt vmcnt(7)\n"
"  buffer_load_dwordx4 v8, s[52:55], s85 offen offset:1024 sc1 lds\n"
"  s_waitcnt vmcnt(7)\n"
"  buffer_load_dwordx4 v8, s[52:55], s85 offen offset:2048 sc1 lds\n"
"  s_waitcnt lgkmcnt(0)\n"
"  v_cmp_gt_u32 vcc, 22, v29\n"
"  s_cbranch_vccz .Lh_go23\n"
"  s_waitcnt vmcnt(0)\n"
"  v_mov_b32 v28, 26\n"
"  ds_max_u32 v11, v28\n"
".Lh_sp23:\n"
"  ds_read_b32 v29, v10\n"
"  s_waitcnt lgkmcnt(0)\n"
"  v_cmp_gt_u32 vcc, 22, v29\n"
"  s_cbranch_vccz .Lh_go23\n"
"  s_sleep 1\n"
"  s_sub_u32 s81, s81, 1\n"
"  s_cbranch_scc0 .Lh_sp23\n"
"  s_branch .Lh_end\n"
".Lh_go23:\n"
"  s_waitcnt vmcnt(7)\n"
"  s_add_u32 m0, s72, 69632\n"
"  s_mov_b32 s85, 128\n"
"  buffer_load_dwordx4 v5, s[48:51], s85 offen lds\n"
"  s_waitcnt vmcnt(7)\n"
"  s_add_u32 m0, s72, 73728\n"
"  s_mov_b32 s85, 36992\n"
"  buffer_load_dwordx4 v5, s[48:51], s85 offen lds\n"
"  s_waitcnt vmcnt(7)\n"
"  s_add_u32 m0, s72, 77824\n"
"  s_mov_b32 s85, 73856\n"
"  buffer_load_dwordx4 v5, s[48:51], s85 offen lds\n"
"  s_waitcnt vmcnt(7)\n"
"  s_add_u32 m0, s72, 81920\n"
"  s_mov_b32 s85, 110720\n"
"  buffer_load_dwordx4 v5, s[48:51], s85 offen lds\n"
"  ds_read_b32 v29, v10\n"
"  s_waitcnt vmcnt(7)\n"
"  s_add_u32 m0, s72, 86016\n"
"  s_mov_b32 s85, 147584\n"
"  buffer_load_dwordx4 v5, s[48:51], s85 offen lds\n"
"  s_waitcnt vmcnt(7)\n"
"  s_add_u32 m0, s72, 90112\n"
"  s_mov_b32 s85, 184448\n"
"  buffer_load_dwordx4 v5, s[48:51], s85 offen lds\n"
"  s_waitcnt lgkmcnt(0)\n"
"  v_cmp_gt_u32 vcc, 23, v29\n"
"  s_cbranch_vccz .Lh_go24\n"
"  s_waitcnt vmcnt(6)\n"
"  v_mov_b32 v28, 26\n"
"  ds_max_u32 v11, v28\n"
".Lh_sp24:\n"
"  ds_read_b32 v29, v10\n"
"  s_waitcnt lgkmcnt(0)\n"
"  v_cmp_gt_u32 vcc, 23, v29\n"
"  s_cbranch_vccz .Lh_go24\n"
"  s_sleep 1\n"
"  s_sub_u32 s81, s81, 1\n"
"  s_cbranch_scc0 .Lh_sp24\n"
"  s_branch .Lh_end\n"
".Lh_go24:\n"
"  s_waitcnt vmcnt(6)\n"
"  v_mov_b32 v28, 26\n"
"  ds_max_u32 v11, v28\n"
"  ds_write_b128 v12, v[72:75] offset:0\n"
"  ds_write_b128 v12, v[76:79] offset:22528\n"
"  ds_write_b128 v12, v[80:83] offset:4096\n"
"  ds_write_b128 v12, v[84:87] offset:26624\n"
"  ds_write_b128 v12, v[88:91] offset:8192\n"
"  ds_write_b128 v12, v[92:95] offset:30720\n"
"  ds_write_b128 v12, v[96:99] offset:12288\n"
"  ds_write_b128 v12, v[100:103] offset:34816\n"
"  v_mov_b32 v28, 27\n"
"  ds_max_u32 v11, v28\n"
"  s_add_u32 m0, s72, 94208\n"
"  s_mov_b32 s85, 221312\n"
"  buffer_load_dwordx4 v5, s[48:51], s85 offen lds\n"
"  s_add_u32 m0, s72, 98304\n"
"  s_mov_b32 s85, 258176\n"
"  buffer_load_dwordx4 v5, s[48:51], s85 offen lds\n"
"  s_waitcnt vmcnt(7)\n"
"  s_add_u32 m0, s72, 102400\n"
"  s_mov_b32 s85, 128\n"
"  buffer_load_dwordx4 v6, s[48:51], s85 offen lds\n"
"  s_waitcnt vmcnt(7)\n"
"  s_add_u32 m0, s73, 106496\n"
"  s_mov_b32 s85, 32768\n"
"  buffer_load_dwordx4 v7, s[52:55], s85 offen sc1 lds\n"
"  ds_read_b32 v29, v10\n"
"  s_waitcnt vmcnt(7)\n"
"  buffer_load_dwordx4 v7, s[52:55], s85 offen offset:1024 sc1 lds\n"
"  s_waitcnt vmcnt(7)\n"
"  buffer_load_dwordx4 v7, s[52:55], s85 offen offset:2048 sc1 lds\n"
"  s_waitcnt lgkmcnt(0)\n"
"  v_cmp_gt_u32 vcc, 25, v29\n"
"  s_cbranch_vccz .Lh_go25\n"
"  s_waitcnt vmcnt(6)\n"
"  v_mov_b32 v28, 28\n"
"  ds_max_u32 v11, v28\n"
"  ds_read_b32 v29, v10\n"
"  s_waitcnt lgkmcnt(0)\n"
"  v_cmp_gt_u32 vcc, 25, v29\n"
"  s_cbranch_vccz .Lh_go25\n"
"  s_waitcnt vmcnt(0)\n"
"  v_mov_b32 v28, 29\n"
"  ds_max_u32 v11, v28\n"
".Lh_sp25:\n"
"  ds_read_b32 v29, v10\n"
"  s_waitcnt lgkmcnt(0)\n"
"  v_cmp_gt_u32 vcc, 25, v29\n"
"  s_cbranch_vccz .Lh_go25\n"
"  s_sleep 1\n"
"  s_sub_u32 s81, s81, 1\n"
"  s_cbranch_scc0 .Lh_sp25\n"
"  s_branch .Lh_end\n"
".Lh_go25:\n"
"  s_waitcnt vmcnt(7)\n"
"  s_add_u32 m0, s74, 118784\n"
"  s_mov_b32 s85, 32768\n"
"  buffer_load_dwordx4 v8, s[52:55], s85 offen sc1 lds\n"
"  s_waitcnt vmcnt(7)\n"
"  v_mov_b32 v28, 28\n"
"  ds_max_u32 v11, v28\n"
"  buffer_load_dwordx4 v8, s[52:55], s85 offen offset:1024 sc1 lds\n"
"  s_waitcnt vmcnt(7)\n"
"  s_add_u32 m0, s74, 120832\n"
"  s_mov_b32 s85, 34816\n"
"  buffer_load_dwordx4 v8, s[52:55], s85 offen sc1 lds\n"
"  ds_read_b32 v29, v10\n"
"  s_waitcnt vmcnt(7)\n"
"  buffer_load_dwordx4 v8, s[52:55], s85 offen offset:1024 sc1 lds\n"
"  s_waitcnt vmcnt(7)\n"
"  buffer_load_dwordx4 v8, s[52:55], s85 offen offset:2048 sc1 lds\n"
"  s_waitcnt lgkmcnt(0)\n"
"  v_cmp_gt_u32 vcc, 24, v29\n"
"  s_cbranch_vccz .Lh_go26\n"
"  s_waitcnt vmcnt(5)\n"
"  v_mov_b32 v28, 29\n"
"  ds_max_u32 v11, v28\n"
"  ds_read_b32 v29, v10\n"
"  s_waitcnt lgkmcnt(0)\n"
"  v_cmp_gt_u32 vcc, 24, v29\n"
"  s_cbranch_vccz .Lh_go26\n"
"  s_waitcnt vmcnt(0)\n"
"  v_mov_b32 v28, 30\n"
"  ds_max_u32 v11, v28\n"
".Lh_sp26:\n"
"  ds_read_b32 v29, v10\n"
"  s_waitcnt lgkmcnt(0)\n"
"  v_cmp_gt_u32 vcc, 24, v29\n"
"  s_cbranch_vccz .Lh_go26\n"
"  s_sleep 1\n"
"  s_sub_u32 s81, s81, 1\n"
"  s_cbranch_scc0 .Lh_sp26\n"
"  s_branch .Lh_end\n"
".Lh_go26:\n"
"  s_waitcnt vmcnt(7)\n"
"  s_add_u32 m0, s72, 0\n"
"  s_mov_b32 s85, 256\n"
"  buffer_load_dwordx4 v5, s[48:51], s85 offen lds\n"
"  s_waitcnt vmcnt(7)\n"
"  s_add_u32 m0, s72, 4096\n"
"  s_mov_b32 s85, 37120\n"
"  buffer_load_dwordx4 v5, s[48:51], s85 offen lds\n"
"  s_waitcnt vmcnt(7)\n"
"  v_mov_b32 v28, 29\n"
"  ds_max_u32 v11, v28\n"
"  s_add_u32 m0, s72, 8192\n"
"  s_mov_b32 s85, 73984\n"
"  buffer_load_dwordx4 v5, s[48:51], s85 offen lds\n"
"  s_waitcnt vmcnt(7)\n"
"  s_add_u32 m0, s72, 12288\n"
"  s_mov_b32 s85, 110848\n"
"  buffer_load_dwordx4 v5, s[48:51], s85 offen lds\n"
"  ds_read_b32 v29, v10\n"
"  s_waitcnt vmcnt(7)\n"
"  s_add_u32 m0, s72, 16384\n"
"  s_mov_b32 s85, 147712\n"
"  buffer_load_dwordx4 v5, s[48:51], s85 offen lds\n"
"  s_waitcnt vmcnt(7)\n"
"  s_add_u32 m0, s72, 20480\n"
"  s_mov_b32 s85, 184576\n"
"  buffer_load_dwordx4 v5, s[48:51], s85 offen lds\n"
"  s_waitcnt lgkmcnt(0)\n"
"  v_cmp_gt_u32 vcc, 25, v29\n"
"  s_cbranch_vccz .Lh_go27\n"
"  s_waitcnt vmcnt(6)\n"
"  v_mov_b32 v28, 30\n"
"  ds_max_u32 v11, v28\n"
"  ds_read_b32 v29, v10\n"
"  s_waitcnt lgkmcnt(0)\n"
"  v_cmp_gt_u32 vcc, 25, v29\n"
"  s_cbranch_vccz .Lh_go27\n"
"  s_waitcnt vmcnt(0)\n"
"  v_mov_b32 v28, 31\n"
"  ds_max_u32 v11, v28\n"
".Lh_sp27:\n"
"  ds_read_b32 v29, v10\n"
"  s_waitcnt lgkmcnt(0)\n"
"  v_cmp_gt_u32 vcc, 25, v29\n"
"  s_cbranch_vccz .Lh_go27\n"
"  s_sleep 1\n"
"  s_sub_u32 s81, s81, 1\n"
"  s_cbranch_scc0 .Lh_sp27\n"
"  s_branch .Lh_end\n"
".Lh_go27:\n"
"  s_waitcnt vmcnt(7)\n"
"  s_add_u32 m0, s72, 24576\n"
"  s_mov_b32 s85, 221440\n"
"  buffer_load_dwordx4 v5, s[48:51], s85 offen lds\n"
"  s_waitcnt vmcnt(7)\n"
"  v_mov_b32 v28, 30\n"
"  ds_max_u32 v11, v28\n"
"  s_add_u32 m0, s72, 28672\n"
"  s_mov_b32 s85, 258304\n"
"  buffer_load_dwordx4 v5, s[48:51], s85 offen lds\n"
"  s_waitcnt vmcnt(7)\n"
"  s_add_u32 m0, s72, 32768\n"
"  s_mov_b32 s85, 256\n"
"  buffer_load_dwordx4 v6, s[48:51], s85 offen lds\n"
"  s_waitcnt vmcnt(7)\n"
"  s_add_u32 m0, s73, 36864\n"
"  s_mov_b32 s85, 65536\n"
"  buffer_load_dwordx4 v7, s[52:55], s85 offen sc1 lds\n"
"  ds_read_b32 v29, v10\n"
"  s_waitcnt vmcnt(7)\n"
"  buffer_load_dwordx4 v7, s[52:55], s85 offen offset:1024 sc1 lds\n"
"  s_waitcnt vmcnt(7)\n"
"  buffer_load_dwordx4 v7, s[52:55], s85 offen offset:2048 sc1 lds\n"
"  s_waitcnt lgkmcnt(0)\n"
"  v_cmp_gt_u32 vcc, 26, v29\n"
"  s_cbranch_vccz .Lh_go28\n"
"  s_waitcnt vmcnt(6)\n"
"  v_mov_b32 v28, 31\n"
"  ds_max_u32 v11, v28\n"
"  ds_read_b32 v29, v10\n"
"  s_waitcnt lgkmcnt(0)\n"
"  v_cmp_gt_u32 vcc, 26, v29\n"
"  s_cbranch_vccz .Lh_go28\n"
"  s_waitcnt vmcnt(0)\n"
"  v_mov_b32 v28, 32\n"
"  ds_max_u32 v11, v28\n"
".Lh_sp28:\n"
"  ds_read_b32 v29, v10\n"
"  s_waitcnt lgkmcnt(0)\n"
"  v_cmp_gt_u32 vcc, 26, v29\n"
"  s_cbranch_vccz .Lh_go28\n"
"  s_sleep 1\n"
"  s_sub_u32 s81, s81, 1\n"
"  s_cbranch_scc0 .Lh_sp28\n"
"  s_branch .Lh_end\n"
".Lh_go28:\n"
"  s_waitcnt vmcnt(7)\n"
"  s_add_u32 m0, s74, 49152\n"
"  s_mov_b32 s85, 65536\n"
"  buffer_load_dwordx4 v8, s[52:55], s85 offen sc1 lds\n"
"  s_waitcnt vmcnt(7)\n"
"  v_mov_b32 v28, 31\n"
"  ds_max_u32 v11, v28\n"
"  buffer_load_dwordx4 v8, s[52:55], s85 offen offset:1024 sc1 lds\n"
"  s_waitcnt vmcnt(7)\n"
"  s_add_u32 m0, s74, 51200\n"
"  s_mov_b32 s85, 67584\n"
"  buffer_load_dwordx4 v8, s[52:55], s85 offen sc1 lds\n"
"  ds_read_b32 v29, v10\n"
"  s_waitcnt vmcnt(7)\n"
"  buffer_load_dwordx4 v8, s[52:55], s85 offen offset:1024 sc1 lds\n"
"  s_waitcnt vmcnt(7)\n"
"  buffer_load_dwordx4 v8, s[52:55], s85 offen offset:2048 sc1 lds\n"
"  v_mov_b32 v64, 0x7f817f81\n"
"  v_mov_b32 v65, 0x7f817f81\n"
"  v_mov_b32 v66, 0x7f817f81\n"
"  v_mov_b32 v67, 0x7f817f81\n"
"  s_waitcnt vmcnt(7)\n"
"  s_mov_b32 s80, 2097152\n"
"  buffer_store_dwordx4 v[64:67], v15, s[52:55], s80 offen sc1\n"
"  s_waitcnt vmcnt(7)\n"
"  s_mov_b32 s80, 2101248\n"
"  buffer_store_dwordx4 v[64:67], v15, s[52:55], s80 offen sc1\n"
"  s_waitcnt vmcnt(7)\n"
"  v_mov_b32 v28, 32\n"
"  ds_max_u32 v11, v28\n"
"  s_mov_b32 s80, 2105344\n"
"  buffer_store_dwordx4 v[64:67], v15, s[52:55], s80 offen sc1\n"
"  s_waitcnt vmcnt(7)\n"
"  s_mov_b32 s80, 2109440\n"
"  buffer_store_dwordx4 v[64:67], v15, s[52:55], s80 offen sc1\n"
"  s_waitcnt lgkmcnt(0)\n"
"  v_cmp_gt_u32 vcc, 27, v29\n"
"  s_cbranch_vccz .Lh_go29\n"
"  s_waitcnt vmcnt(4)\n"
"  v_mov_b32 v28, 33\n"
"  ds_max_u32 v11, v28\n"
".Lh_sp29:\n"
"  ds_read_b32 v29, v10\n"
"  s_waitcnt lgkmcnt(0)\n"
"  v_cmp_gt_u32 vcc, 27, v29\n"
"  s_cbranch_vccz .Lh_go29\n"
"  s_sleep 1\n"
"  s_sub_u32 s81, s81, 1\n"
"  s_cbranch_scc0 .Lh_sp29\n"
"  s_branch .Lh_end\n"
".Lh_go29:\n"
"  s_waitcnt vmcnt(7)\n"
"  s_add_u32 m0, s72, 69632\n"
"  s_mov_b32 s85, 384\n"
"  buffer_load_dwordx4 v5, s[48:51], s85 offen lds\n"
"  s_waitcnt vmcnt(7)\n"
"  s_add_u32 m0, s72, 73728\n"
"  s_mov_b32 s85, 37248\n"
"  buffer_load_dwordx4 v5, s[48:51], s85 offen lds\n"
"  s_waitcnt vmcnt(7)\n"
"  s_add_u32 m0, s72, 77824\n"
"  s_mov_b32 s85, 74112\n"
"  buffer_load_dwordx4 v5, s[48:51], s85 offen lds\n"
"  s_waitcnt vmcnt(7)\n"
"  v_mov_b32 v28, 33\n"
"  ds_max_u32 v11, v28\n"
"  s_add_u32 m0, s72, 81920\n"
"  s_mov_b32 s85, 110976\n"
"  buffer_load_dwordx4 v5, s[48:51], s85 offen lds\n"
"  ds_read_b32 v29, v10\n"
"  s_waitcnt vmcnt(7)\n"
"  s_add_u32 m0, s72, 86016\n"
"  s_mov_b32 s85, 147840\n"
"  buffer_load_dwordx4 v5, s[48:51], s85 offen lds\n"
"  s_waitcnt vmcnt(7)\n"
"  s_add_u32 m0, s72, 90112\n"
"  s_mov_b32 s85, 184704\n"
"  buffer_load_dwordx4 v5, s[48:51], s85 offen lds\n"
"  s_waitcnt lgkmcnt(0)\n"
"  v_cmp_gt_u32 vcc, 28, v29\n"
"  s_cbranch_vccz .Lh_go30\n"
"  s_waitcnt vmcnt(0)\n"
"  v_mov_b32 v28, 34\n"
"  ds_max_u32 v11, v28\n"
".Lh_sp30:\n"
"  ds_read_b32 v29, v10\n"
"  s_waitcnt lgkmcnt(0)\n"
"  v_cmp_gt_u32 vcc, 28, v29\n"
"  s_cbranch_vccz .Lh_go30\n"
"  s_sleep 1\n"
"  s_sub_u32 s81, s81, 1\n"
"  s_cbranch_scc0 .Lh_sp30\n"
"  s_branch .Lh_end\n"
".Lh_go30:\n"
"  s_waitcnt vmcnt(7)\n"
"  s_add_u32 m0, s72, 94208\n"
"  s_mov_b32 s85, 221568\n"
"  buffer_load_dwordx4 v5, s[48:51], s85 offen lds\n"
"  s_waitcnt vmcnt(7)\n"
"  s_add_u32 m0, s72, 98304\n"
"  s_mov_b32 s85, 258432\n"
"  buffer_load_dwordx4 v5, s[48:51], s85 offen lds\n"
"  s_waitcnt vmcnt(7)\n"
"  s_add_u32 m0, s72, 102400\n"
"  s_mov_b32 s85, 384\n"
"  buffer_load_dwordx4 v6, s[48:51], s85 offen lds\n"
"  s_waitcnt vmcnt(7)\n"
"  s_add_u32 m0, s73, 106496\n"
"  s_mov_b32 s85, 98304\n"
"  buffer_load_dwordx4 v7, s[52:55], s85 offen sc1 lds\n"
"  ds_read_b32 v29, v10\n"
"  s_waitcnt vmcnt(7)\n"
"  buffer_load_dwordx4 v7, s[52:55], s85 offen offset:1024 sc1 lds\n"
"  s_waitcnt vmcnt(7)\n"
"  buffer_load_dwordx4 v7, s[52:55], s85 offen offset:2048 sc1 lds\n"
"  s_waitcnt lgkmcnt(0)\n"
"  v_cmp_gt_u32 vcc, 29, v29\n"
"  s_cbranch_vccz .Lh_go31\n"
"  s_waitcnt vmcnt(6)\n"
"  v_mov_b32 v28, 34\n"
"  ds_max_u32 v11, v28\n"
"  ds_read_b32 v29, v10\n"
"  s_waitcnt lgkmcnt(0)\n"
"  v_cmp_gt_u32 vcc, 29, v29\n"
"  s_cbranch_vccz .Lh_go31\n"
"  s_waitcnt vmcnt(0)\n"
"  v_mov_b32 v28, 35\n"
"  ds_max_u32 v11, v28\n"
".Lh_sp31:\n"
"  ds_read_b32 v29, v10\n"
"  s_waitcnt lgkmcnt(0)\n"
"  v_cmp_gt_u32 vcc, 29, v29\n"
"  s_cbranch_vccz .Lh_go31\n"
"  s_sleep 1\n"
"  s_sub_u32 s81, s81, 1\n"
"  s_cbranch_scc0 .Lh_sp31\n"
"  s_branch .Lh_end\n"
".Lh_go31:\n"
"  s_waitcnt vmcnt(7)\n"
"  s_add_u32 m0, s74, 118784\n"
"  s_mov_b32 s85, 98304\n"
"  buffer_load_dwordx4 v8, s[52:55], s85 offen sc1 lds\n"
"  s_waitcnt vmcnt(7)\n"
"  v_mov_b32 v28, 34\n"
"  ds_max_u32 v11, v28\n"
"  buffer_load_dwordx4 v8, s[52:55], s85 offen offset:1024 sc1 lds\n"
"  s_waitcnt vmcnt(7)\n"
"  s_add_u32 m0, s74, 120832\n"
"  s_mov_b32 s85, 100352\n"
"  buffer_load_dwordx4 v8, s[52:55], s85 offen sc1 lds\n"
"  ds_read_b32 v29, v10\n"
"  s_waitcnt vmcnt(7)\n"
"  buffer_load_dwordx4 v8, s[52:55], s85 offen offset:1024 sc1 lds\n"
"  s_waitcnt vmcnt(7)\n"
"  buffer_load_dwordx4 v8, s[52:55], s85 offen offset:2048 sc1 lds\n"
"  s_waitcnt lgkmcnt(0)\n"
"  v_cmp_gt_u32 vcc, 30, v29\n"
"  s_cbranch_vccz .Lh_go32\n"
"  s_waitcnt vmcnt(5)\n"
"  v_mov_b32 v28, 35\n"
"  ds_max_u32 v11, v28\n"
"  ds_read_b32 v29, v10\n"
"  s_waitcnt lgkmcnt(0)\n"
"  v_cmp_gt_u32 vcc, 30, v29\n"
"  s_cbranch_vccz .Lh_go32\n"
"  s_waitcnt vmcnt(0)\n"
"  v_mov_b32 v28, 36\n"
"  ds_max_u32 v11, v28\n"
".Lh_sp32:\n"
"  ds_read_b32 v29, v10\n"
"  s_waitcnt lgkmcnt(0)\n"
"  v_cmp_gt_u32 vcc, 30, v29\n"
"  s_cbranch_vccz .Lh_go32\n"
"  s_sleep 1\n"
"  s_sub_u32 s81, s81, 1\n"
"  s_cbranch_scc0 .Lh_sp32\n"
"  s_branch .Lh_end\n"
".Lh_go32:\n"
"  s_waitcnt vmcnt(7)\n"
"  s_add_u32 m0, s72, 0\n"
"  s_mov_b32 s85, 512\n"
"  buffer_load_dwordx4 v5, s[48:51], s85 offen lds\n"
"  s_waitcnt vmcnt(7)\n"
"  s_add_u32 m0, s72, 4096\n"
"  s_mov_b32 s85, 37376\n"
"  buffer_load_dwordx4 v5, s[48:51], s85 offen lds\n"
"  s_waitcnt vmcnt(7)\n"
"  v_mov_b32 v28, 35\n"
"  ds_max_u32 v11, v28\n"
"  s_add_u32 m0, s72, 8192\n"
"  s_mov_b32 s85, 74240\n"
"  buffer_load_dwordx4 v5, s[48:51], s85 offen lds\n"
"  s_waitcnt vmcnt(7)\n"
"  s_add_u32 m0, s72, 12288\n"
"  s_mov_b32 s85, 111104\n"
"  buffer_load_dwordx4 v5, s[48:51], s85 offen lds\n"
"  ds_read_b32 v29, v10\n"
"  s_waitcnt vmcnt(7)\n"
"  s_add_u32 m0, s72, 16384\n"
"  s_mov_b32 s85, 147968\n"
"  buffer_load_dwordx4 v5, s[48:51], s85 offen lds\n"
"  s_waitcnt vmcnt(7)\n"
"  s_add_u32 m0, s72, 20480\n"
"  s_mov_b32 s85, 184832\n"
"  buffer_load_dwordx4 v5, s[48:51], s85 offen lds\n"
"  s_waitcnt lgkmcnt(0)\n"
"  v_cmp_gt_u32 vcc, 31, v29\n"
"  s_cbranch_vccz .Lh_go33\n"
"  s_waitcnt vmcnt(6)\n"
"  v_mov_b32 v28, 36\n"
"  ds_max_u32 v11, v28\n"
"  ds_read_b32 v29, v10\n"
"  s_waitcnt lgkmcnt(0)\n"
"  v_cmp_gt_u32 vcc, 31, v29\n"
"  s_cbranch_vccz .Lh_go33\n"
"  s_waitcnt vmcnt(0)\n"
"  v_mov_b32 v28, 37\n"
"  ds_max_u32 v11, v28\n"
".Lh_sp33:\n"
"  ds_read_b32 v29, v10\n"
"  s_waitcnt lgkmcnt(0)\n"
"  v_cmp_gt_u32 vcc, 31, v29\n"
"  s_cbranch_vccz .Lh_go33\n"
"  s_sleep 1\n"
"  s_sub_u32 s81, s81, 1\n"
"  s_cbranch_scc0 .Lh_sp33\n"
"  s_branch .Lh_end\n"
".Lh_go33:\n"
"  s_waitcnt vmcnt(7)\n"
"  s_add_u32 m0, s72, 24576\n"
"  s_mov_b32 s85, 221696\n"
"  buffer_load_dwordx4 v5, s[48:51], s85 offen lds\n"
"  s_waitcnt vmcnt(7)\n"
"  v_mov_b32 v28, 36\n"
"  ds_max_u32 v11, v28\n"
"  s_add_u32 m0, s72, 28672\n"
"  s_mov_b32 s85, 258560\n"
"  buffer_load_dwordx4 v5, s[48:51], s85 offen lds\n"
"  s_waitcnt vmcnt(7)\n"
"  s_add_u32 m0, s72, 32768\n"
"  s_mov_b32 s85, 512\n"
"  buffer_load_dwordx4 v6, s[48:51], s85 offen lds\n"
"  s_waitcnt vmcnt(7)\n"
"  s_add_u32 m0, s73, 36864\n"
"  s_mov_b32 s85, 131072\n"
"  buffer_load_dwordx4 v7, s[52:55], s85 offen sc1 lds\n"
"  ds_read_b32 v29, v10\n"
"  s_waitcnt vmcnt(7)\n"
"  buffer_load_dwordx4 v7, s[52:55], s85 offen offset:1024 sc1 lds\n"
"  s_waitcnt vmcnt(7)\n"
"  buffer_load_dwordx4 v7, s[52:55], s85 offen offset:2048 sc1 lds\n"
"  s_waitcnt lgkmcnt(0)\n"
"  v_cmp_gt_u32 vcc, 32, v29\n"
"  s_cbranch_vccz .Lh_go34\n"
"  s_waitcnt vmcnt(6)\n"
"  v_mov_b32 v28, 37\n"
"  ds_max_u32 v11, v28\n"
"  ds_read_b32 v29, v10\n"
"  s_waitcnt lgkmcnt(0)\n"
"  v_cmp_gt_u32 vcc, 32, v29\n"
"  s_cbranch_vccz .Lh_go34\n"
"  s_waitcnt vmcnt(0)\n"
"  v_mov_b32 v28, 38\n"
"  ds_max_u32 v11, v28\n"
".Lh_sp34:\n"
"  ds_read_b32 v29, v10\n"
"  s_waitcnt lgkmcnt(0)\n"
"  v_cmp_gt_u32 vcc, 32, v29\n"
"  s_cbranch_vccz .Lh_go34\n"
"  s_sleep 1\n"
"  s_sub_u32 s81, s81, 1\n"
"  s_cbranch_scc0 .Lh_sp34\n"
"  s_branch .Lh_end\n"
".Lh_go34:\n"
"  s_waitcnt vmcnt(7)\n"
"  s_mov_b32 s80, 16384\n"
"  buffer_load_dword v30, v19, s[52:55], s80 offen sc1\n"
"  s_waitcnt vmcnt(7)\n"
"  v_mov_b32 v28, 37\n"
"  ds_max_u32 v11, v28\n"
"  s_add_u32 m0, s74, 49152\n"
"  s_mov_b32 s85, 131072\n"
"  buffer_load_dwordx4 v8, s[52:55], s85 offen sc1 lds\n"
"  s_waitcnt vmcnt(7)\n"
"  buffer_load_dwordx4 v8, s[52:55], s85 offen offset:1024 sc1 lds\n"
"  s_waitcnt vmcnt(7)\n"
"  s_add_u32 m0, s74, 51200\n"
"  s_mov_b32 s85, 133120\n"
"  buffer_load_dwordx4 v8, s[52:55], s85 offen sc1 lds\n"
"  ds_read_b32 v29, v10\n"
"  s_waitcnt vmcnt(7)\n"
"  buffer_load_dwordx4 v8, s[52:55], s85 offen offset:1024 sc1 lds\n"
"  s_waitcnt vmcnt(7)\n"
"  buffer_load_dwordx4 v8, s[52:55], s85 offen offset:2048 sc1 lds\n"
"  s_waitcnt lgkmcnt(0)\n"
"  v_cmp_gt_u32 vcc, 33, v29\n"
"  s_cbranch_vccz .Lh_go35\n"
"  s_waitcnt vmcnt(6)\n"
"  v_mov_b32 v28, 38\n"
"  ds_max_u32 v11, v28\n"
"  ds_read_b32 v29, v10\n"
"  s_waitcnt lgkmcnt(0)\n"
"  v_cmp_gt_u32 vcc, 33, v29\n"
"  s_cbranch_vccz .Lh_go35\n"
"  s_waitcnt vmcnt(0)\n"
"  v_mov_b32 v28, 39\n"
"  ds_max_u32 v11, v28\n"
".Lh_sp35:\n"
"  ds_read_b32 v29, v10\n"
"  s_waitcnt lgkmcnt(0)\n"
"  v_cmp_gt_u32 vcc, 33, v29\n"
"  s_cbranch_vccz .Lh_go35\n"
"  s_sleep 1\n"
"  s_sub_u32 s81, s81, 1\n"
"  s_cbranch_scc0 .Lh_sp35\n"
"  s_branch .Lh_end\n"
".Lh_go35:\n"
"  s_waitcnt vmcnt(7)\n"
"  s_add_u32 m0, s72, 69632\n"
"  s_mov_b32 s85, 640\n"
"  buffer_load_dwordx4 v5, s[48:51], s85 offen lds\n"
"  s_waitcnt vmcnt(7)\n"
"  v_mov_b32 v28, 38\n"
"  ds_max_u32 v11, v28\n"
"  s_add_u32 m0, s72, 73728\n"
"  s_mov_b32 s85, 37504\n"
"  buffer_load_dwordx4 v5, s[48:51], s85 offen lds\n"
"  s_waitcnt vmcnt(7)\n"
"  s_add_u32 m0, s72, 77824\n"
"  s_mov_b32 s85, 74368\n"
"  buffer_load_dwordx4 v5, s[48:51], s85 offen lds\n"
"  s_waitcnt vmcnt(7)\n"
"  s_add_u32 m0, s72, 81920\n"
"  s_mov_b32 s85, 111232\n"
"  buffer_load_dwordx4 v5, s[48:51], s85 offen lds\n"
"  ds_read_b32 v29, v10\n"
"  s_waitcnt vmcnt(7)\n"
"  s_add_u32 m0, s72, 86016\n"
"  s_mov_b32 s85, 148096\n"
"  buffer_load_dwordx4 v5, s[48:51], s85 offen lds\n"
"  s_waitcnt vmcnt(7)\n"
"  s_add_u32 m0, s72, 90112\n"
"  s_mov_b32 s85, 184960\n"
"  buffer_load_dwordx4 v5, s[48:51], s85 offen lds\n"
"  s_waitcnt lgkmcnt(0)\n"
"  v_cmp_gt_u32 vcc, 34, v29\n"
"  s_cbranch_vccz .Lh_go36\n"
"  s_waitcnt vmcnt(6)\n"
"  v_mov_b32 v28, 39\n"
"  ds_max_u32 v11, v28\n"
"  ds_read_b32 v29, v10\n"
"  s_waitcnt lgkmcnt(0)\n"
"  v_cmp_gt_u32 vcc, 34, v29\n"
"  s_cbranch_vccz .Lh_go36\n"
"  s_waitcnt vmcnt(0)\n"
"  v_mov_b32 v28, 40\n"
"  ds_max_u32 v11, v28\n"
".Lh_sp36:\n"
"  ds_read_b32 v29, v10\n"
"  s_waitcnt lgkmcnt(0)\n"
"  v_cmp_gt_u32 vcc, 34, v29\n"
"  s_cbranch_vccz .Lh_go36\n"
"  s_sleep 1\n"
"  s_sub_u32 s81, s81, 1\n"
"  s_cbranch_scc0 .Lh_sp36\n"
"  s_branch .Lh_end\n"
".Lh_go36:\n"
"  s_waitcnt vmcnt(7)\n"
"  s_add_u32 m0, s72, 94208\n"
"  s_mov_b32 s85, 221824\n"
"  buffer_load_dwordx4 v5, s[48:51], s85 offen lds\n"
"  s_waitcnt vmcnt(7)\n"
"  v_mov_b32 v28, 39\n"
"  ds_max_u32 v11, v28\n"
"  s_add_u32 m0, s72, 98304\n"
"  s_mov_b32 s85, 258688\n"
"  buffer_load_dwordx4 v5, s[48:51], s85 offen lds\n"
"  s_waitcnt vmcnt(7)\n"
"  s_add_u32 m0, s72, 102400\n"
"  s_mov_b32 s85, 640\n"
"  buffer_load_dwordx4 v6, s[48:51], s85 offen lds\n"
"  s_waitcnt vmcnt(7)\n"
"  s_add_u32 m0, s73, 106496\n"
"  s_mov_b32 s85, 163840\n"
"  buffer_load_dwordx4 v7, s[52:55], s85 offen sc1 lds\n"
"  ds_read_b32 v29, v10\n"
"  s_waitcnt vmcnt(7)\n"
"  buffer_load_dwordx4 v7, s[52:55], s85 offen offset:1024 sc1 lds\n"
"  s_waitcnt vmcnt(7)\n"
"  buffer_load_dwordx4 v7, s[52:55], s85 offen offset:2048 sc1 lds\n"
"  s_waitcnt lgkmcnt(0)\n"
"  v_cmp_gt_u32 vcc, 35, v29\n"
"  s_cbranch_vccz .Lh_go37\n"
"  s_waitcnt vmcnt(6)\n"
"  v_mov_b32 v28, 40\n"
"  ds_max_u32 v11, v28\n"
"  ds_read_b32 v29, v10\n"
"  s_waitcnt lgkmcnt(0)\n"
"  v_cmp_gt_u32 vcc, 35, v29\n"
"  s_cbranch_vccz .Lh_go37\n"
"  s_waitcnt vmcnt(0)\n"
"  v_mov_b32 v28, 41\n"
"  ds_max_u32 v11, v28\n"
".Lh_sp37:\n"
"  ds_read_b32 v29, v10\n"
"  s_waitcnt lgkmcnt(0)\n"
"  v_cmp_gt_u32 vcc, 35, v29\n"
"  s_cbranch_vccz .Lh_go37\n"
"  s_sleep 1\n"
"  s_sub_u32 s81, s81, 1\n"
"  s_cbranch_scc0 .Lh_sp37\n"
"  s_branch .Lh_end\n"
".Lh_go37:\n"
"  v_cmp_gt_u32 vcc, 1, v30\n"
"  s_cbranch_vccz .Lh_go38\n"
"  s_waitcnt vmcnt(0)\n"
"  v_mov_b32 v28, 41\n"
"  ds_max_u32 v11, v28\n"
"  s_mov_b32 s80, 16384\n"
".Lh_sp38:\n"
"  buffer_load_dword v30, v19, s[52:55], s80 offen sc1\n"
"  s_waitcnt vmcnt(0)\n"
"  v_cmp_gt_u32 vcc, 1, v30\n"
"  s_cbranch_vccz .Lh_go38\n"
"  s_sleep 2\n"
"  s_sub_u32 s81, s81, 1\n"
"  s_cbranch_scc0 .Lh_sp38\n"
"  s_branch .Lh_end\n"
".Lh_go38:\n"
"  s_waitcnt vmcnt(7)\n"
"  s_mov_b32 s80, 0\n"
"  buffer_load_dwordx4 v[104:107], v15, s[52:55], s80 offen sc1\n"
"  s_waitcnt vmcnt(7)\n"
"  v_mov_b32 v28, 40\n"
"  ds_max_u32 v11, v28\n"
"  s_mov_b32 s80, 4096\n"
"  buffer_load_dwordx4 v[108:111], v15, s[52:55], s80 offen sc1\n"
"  s_waitcnt vmcnt(7)\n"
"  s_mov_b32 s80, 8192\n"
"  buffer_load_dwordx4 v[112:115], v15, s[52:55], s80 offen sc1\n"
"  s_waitcnt vmcnt(7)\n"
"  s_mov_b32 s80, 12288\n"
"  buffer_load_dwordx4 v[116:119], v15, s[52:55], s80 offen sc1\n"
"  s_waitcnt vmcnt(7)\n"
"  buffer_load_dwordx2 v[40:41], v16, s[64:67], 0 offen offset:0\n"
"  s_waitcnt vmcnt(7)\n"
"  buffer_load_dwordx2 v[42:43], v16, s[64:67], 0 offen offset:128\n"
"  s_waitcnt vmcnt(7)\n"
"  s_mov_b32 s80, 32768\n"
"  buffer_load_dword v30, v19, s[52:55], s80 offen sc1\n"
"  s_waitcnt vmcnt(7)\n"
"  v_mov_b32 v28, 41\n"
"  ds_max_u32 v11, v28\n"
"  s_add_u32 m0, s74, 118784\n"
"  s_mov_b32 s85, 163840\n"
"  buffer_load_dwordx4 v8, s[52:55], s85 offen sc1 lds\n"
"  s_waitcnt vmcnt(7)\n"
"  buffer_load_dwordx4 v8, s[52:55], s85 offen offset:1024 sc1 lds\n"
"  s_waitcnt vmcnt(7)\n"
"  s_add_u32 m0, s74, 120832\n"
"  s_mov_b32 s85, 165888\n"
"  buffer_load_dwordx4 v8, s[52:55], s85 offen sc1 lds\n"
"  ds_read_b32 v29, v10\n"
"  s_waitcnt vmcnt(7)\n"
"  buffer_load_dwordx4 v8, s[52:55], s85 offen offset:1024 sc1 lds\n"
"  s_waitcnt vmcnt(7)\n"
"  buffer_load_dwordx4 v8, s[52:55], s85 offen offset:2048 sc1 lds\n"
"  s_waitcnt lgkmcnt(0)\n"
"  v_cmp_gt_u32 vcc, 36, v29\n"
"  s_cbranch_vccz .Lh_go39\n"
"  s_waitcnt vmcnt(0)\n"
"  v_mov_b32 v28, 42\n"
"  ds_max_u32 v11, v28\n"
".Lh_sp39:\n"
"  ds_read_b32 v29, v10\n"
"  s_waitcnt lgkmcnt(0)\n"
"  v_cmp_gt_u32 vcc, 36, v29\n"
"  s_cbranch_vccz .Lh_go39\n"
"  s_sleep 1\n"
"  s_sub_u32 s81, s81, 1\n"
"  s_cbranch_scc0 .Lh_sp39\n"
"  s_branch .Lh_end\n"
".Lh_go39:\n"
"  s_waitcnt vmcnt(7)\n"
"  s_add_u32 m0, s72, 0\n"
"  s_mov_b32 s85, 768\n"
"  buffer_load_dwordx4 v5, s[48:51], s85 offen lds\n"
"  s_waitcnt vmcnt(7)\n"
"  s_add_u32 m0, s72, 4096\n"
"  s_mov_b32 s85, 37632\n"
"  buffer_load_dwordx4 v5, s[48:51], s85 offen lds\n"
"  s_waitcnt vmcnt(7)\n"
"  s_add_u32 m0, s72, 8192\n"
"  s_mov_b32 s85, 74496\n"
"  buffer_load_dwordx4 v5, s[48:51], s85 offen lds\n"
"  s_waitcnt vmcnt(7)\n"
"  s_add_u32 m0, s72, 12288\n"
"  s_mov_b32 s85, 111360\n"
"  buffer_load_dwordx4 v5, s[48:51], s85 offen lds\n"
"  ds_read_b32 v29, v10\n"
"  s_waitcnt vmcnt(7)\n"
"  s_add_u32 m0, s72, 16384\n"
"  s_mov_b32 s85, 148224\n"
"  buffer_load_dwordx4 v5, s[48:51], s85 offen lds\n"
"  s_waitcnt vmcnt(7)\n"
"  s_add_u32 m0, s72, 20480\n"
"  s_mov_b32 s85, 185088\n"
"  buffer_load_dwordx4 v5, s[48:51], s85 offen lds\n"
"  s_waitcnt lgkmcnt(0)\n"
"  v_cmp_gt_u32 vcc, 37, v29\n"
"  s_cbranch_vccz .Lh_go40\n"
"  s_waitcnt vmcnt(6)\n"
"  v_mov_b32 v28, 42\n"
"  ds_max_u32 v11, v28\n"
"  ds_read_b32 v29, v10\n"
"  s_waitcnt lgkmcnt(0)\n"
"  v_cmp_gt_u32 vcc, 37, v29\n"
"  s_cbranch_vccz .Lh_go40\n"
"  s_waitcnt vmcnt(0)\n"
"  v_mov_b32 v28, 43\n"
"  ds_max_u32 v11, v28\n"
".Lh_sp40:\n"
"  ds_read_b32 v29, v10\n"
"  s_waitcnt lgkmcnt(0)\n"
"  v_cmp_gt_u32 vcc, 37, v29\n"
"  s_cbranch_vccz .Lh_go40\n"
"  s_sleep 1\n"
"  s_sub_u32 s81, s81, 1\n"
"  s_cbranch_scc0 .Lh_sp40\n"
"  s_branch .Lh_end\n"
".Lh_go40:\n"
"  s_waitcnt vmcnt(7)\n"
"  s_add_u32 m0, s72, 24576\n"
"  s_mov_b32 s85, 221952\n"
"  buffer_load_dwordx4 v5, s[48:51], s85 offen lds\n"
"  s_waitcnt vmcnt(7)\n"
"  v_mov_b32 v28, 42\n"
"  ds_max_u32 v11, v28\n"
"  s_add_u32 m0, s72, 28672\n"
"  s_mov_b32 s85, 258816\n"
"  buffer_load_dwordx4 v5, s[48:51], s85 offen lds\n"
"  s_waitcnt vmcnt(7)\n"
"  s_add_u32 m0, s72, 32768\n"
"  s_mov_b32 s85, 768\n"
"  buffer_load_dwordx4 v6, s[48:51], s85 offen lds\n"
"  s_waitcnt vmcnt(7)\n"
"  s_add_u32 m0, s73, 36864\n"
"  s_mov_b32 s85, 196608\n"
"  buffer_load_dwordx4 v7, s[52:55], s85 offen sc1 lds\n"
"  ds_read_b32 v29, v10\n"
"  s_waitcnt vmcnt(7)\n"
"  buffer_load_dwordx4 v7, s[52:55], s85 offen offset:1024 sc1 lds\n"
"  s_waitcnt vmcnt(7)\n"
"  buffer_load_dwordx4 v7, s[52:55], s85 offen offset:2048 sc1 lds\n"
"  s_waitcnt lgkmcnt(0)\n"
"  v_cmp_gt_u32 vcc, 38, v29\n"
"  s_cbranch_vccz .Lh_go41\n"
"  s_waitcnt vmcnt(6)\n"
"  v_mov_b32 v28, 43\n"
"  ds_max_u32 v11, v28\n"
"  ds_read_b32 v29, v10\n"
"  s_waitcnt lgkmcnt(0)\n"
"  v_cmp_gt_u32 vcc, 38, v29\n"
"  s_cbranch_vccz .Lh_go41\n"
"  s_waitcnt vmcnt(0)\n"
"  v_mov_b32 v28, 44\n"
"  ds_max_u32 v11, v28\n"
".Lh_sp41:\n"
"  ds_read_b32 v29, v10\n"
"  s_waitcnt lgkmcnt(0)\n"
"  v_cmp_gt_u32 vcc, 38, v29\n"
"  s_cbranch_vccz .Lh_go41\n"
"  s_sleep 1\n"
"  s_sub_u32 s81, s81, 1\n"
"  s_cbranch_scc0 .Lh_sp41\n"
"  s_branch .Lh_end\n"
".Lh_go41:\n"
"  v_cmp_gt_u32 vcc, 1, v30\n"
"  s_cbranch_vccz .Lh_go42\n"
"  s_waitcnt vmcnt(0)\n"
"  v_mov_b32 v28, 44\n"
"  ds_max_u32 v11, v28\n"
"  s_mov_b32 s80, 32768\n"
".Lh_sp42:\n"
"  buffer_load_dword v30, v19, s[52:55], s80 offen sc1\n"
"  s_waitcnt vmcnt(0)\n"
"  v_cmp_gt_u32 vcc, 1, v30\n"
"  s_cbranch_vccz .Lh_go42\n"
"  s_sleep 2\n"
"  s_sub_u32 s81, s81, 1\n"
"  s_cbranch_scc0 .Lh_sp42\n"
"  s_branch .Lh_end\n"
".Lh_go42:\n"
"  s_waitcnt vmcnt(7)\n"
"  s_mov_b32 s80, 262144\n"
"  buffer_load_dwordx4 v[120:123], v15, s[52:55], s80 offen sc1\n"
"  s_waitcnt vmcnt(7)\n"
"  v_mov_b32 v28, 43\n"
"  ds_max_u32 v11, v28\n"
"  s_mov_b32 s80, 266240\n"
"  buffer_load_dwordx4 v[124:127], v15, s[52:55], s80 offen sc1\n"
"  s_waitcnt vmcnt(7)\n"
"  s_mov_b32 s80, 270336\n"
"  buffer_load_dwordx4 v[128:131], v15, s[52:55], s80 offen sc1\n"
"  s_waitcnt vmcnt(7)\n"
"  s_mov_b32 s80, 274432\n"
"  buffer_load_dwordx4 v[132:135], v15, s[52:55], s80 offen sc1\n"
"  s_waitcnt vmcnt(7)\n"
"  buffer_load_dwordx2 v[44:45], v16, s[64:67], 0 offen offset:256\n"
"  s_waitcnt vmcnt(7)\n"
"  buffer_load_dwordx2 v[46:47], v16, s[64:67], 0 offen offset:384\n"
"  s_waitcnt vmcnt(7)\n"
"  s_mov_b32 s80, 49152\n"
"  buffer_load_dword v30, v19, s[52:55], s80 offen sc1\n"
"  v_lshlrev_b32 v60, 16, v40\n"
"  v_and_b32 v61, 0xffff0000, v40\n"
"  v_lshlrev_b32 v62, 16, v41\n"
"  v_and_b32 v63, 0xffff0000, v41\n"
"  s_nop 1\n"
"  v_mfma_f32_16x16x32_bf16 v[60:63], v[20:23], v[104:107], v[60:63]\n"
"  v_mfma_f32_16x16x32_bf16 v[60:63], v[20:23], v[108:111], v[60:63]\n"
"  v_mfma_f32_16x16x32_bf16 v[60:63], v[20:23], v[112:115], v[60:63]\n"
"  v_mfma_f32_16x16x32_bf16 v[60:63], v[20:23], v[116:119], v[60:63]\n"
"  s_nop 7\n"
"  v_cvt_pk_bf16_f32 v40, v60, v61\n"
"  v_cvt_pk_bf16_f32 v41, v62, v63\n"
"  buffer_store_dwordx2 v[40:41], v16, s[68:71], 0 offen offset:0\n"
"  s_waitcnt vmcnt(7)\n"
"  v_mov_b32 v28, 44\n"
"  ds_max_u32 v11, v28\n"
"  s_add_u32 m0, s74, 49152\n"
"  s_mov_b32 s85, 196608\n"
"  buffer_load_dwordx4 v8, s[52:55], s85 offen sc1 lds\n"
"  s_waitcnt vmcnt(7)\n"
"  buffer_load_dwordx4 v8, s[52:55], s85 offen offset:1024 sc1 lds\n"
"  s_waitcnt vmcnt(7)\n"
"  s_add_u32 m0, s74, 51200\n"
"  s_mov_b32 s85, 198656\n"
"  buffer_load_dwordx4 v8, s[52:55], s85 offen sc1 lds\n"
"  ds_read_b32 v29, v10\n"
"  s_waitcnt vmcnt(7)\n"
"  buffer_load_dwordx4 v8, s[52:55], s85 offen offset:1024 sc1 lds\n"
"  s_waitcnt vmcnt(7)\n"
"  buffer_load_dwordx4 v8, s[52:55], s85 offen offset:2048 sc1 lds\n"
"  s_waitcnt lgkmcnt(0)\n"
"  v_cmp_gt_u32 vcc, 39, v29\n"
"  s_cbranch_vccz .Lh_go43\n"
"  s_waitcnt vmcnt(0)\n"
"  v_mov_b32 v28, 45\n"
"  ds_max_u32 v11, v28\n"
".Lh_sp43:\n"
"  ds_read_b32 v29, v10\n"
"  s_waitcnt lgkmcnt(0)\n"
"  v_cmp_gt_u32 vcc, 39, v29\n"
"  s_cbranch_vccz .Lh_go43\n"
"  s_sleep 1\n"
"  s_sub_u32 s81, s81, 1\n"
"  s_cbranch_scc0 .Lh_sp43\n"
"  s_branch .Lh_end\n"
".Lh_go43:\n"
"  s_waitcnt vmcnt(7)\n"
"  s_add_u32 m0, s72, 69632\n"
"  s_mov_b32 s85, 896\n"
"  buffer_load_dwordx4 v5, s[48:51], s85 offen lds\n"
"  s_waitcnt vmcnt(7)\n"
"  s_add_u32 m0, s72, 73728\n"
"  s_mov_b32 s85, 37760\n"
"  buffer_load_dwordx4 v5, s[48:51], s85 offen lds\n"
"  s_waitcnt vmcnt(7)\n"
"  s_add_u32 m0, s72, 77824\n"
"  s_mov_b32 s85, 74624\n"
"  buffer_load_dwordx4 v5, s[48:51], s85 offen lds\n"
"  s_waitcnt vmcnt(7)\n"
"  s_add_u32 m0, s72, 81920\n"
"  s_mov_b32 s85, 111488\n"
"  buffer_load_dwordx4 v5, s[48:51], s85 offen lds\n"
"  ds_read_b32 v29, v10\n"
"  s_waitcnt vmcnt(7)\n"
"  s_add_u32 m0, s72, 86016\n"
"  s_mov_b32 s85, 148352\n"
"  buffer_load_dwordx4 v5, s[48:51], s85 offen lds\n"
"  s_waitcnt vmcnt(7)\n"
"  s_add_u32 m0, s72, 90112\n"
"  s_mov_b32 s85, 185216\n"
"  buffer_load_dwordx4 v5, s[48:51], s85 offen lds\n"
"  s_waitcnt lgkmcnt(0)\n"
"  v_cmp_gt_u32 vcc, 40, v29\n"
"  s_cbranch_vccz .Lh_go44\n"
"  s_waitcnt vmcnt(6)\n"
"  v_mov_b32 v28, 45\n"
"  ds_max_u32 v11, v28\n"
"  ds_read_b32 v29, v10\n"
"  s_waitcnt lgkmcnt(0)\n"
"  v_cmp_gt_u32 vcc, 40, v29\n"
"  s_cbranch_vccz .Lh_go44\n"
"  s_waitcnt vmcnt(0)\n"
"  v_mov_b32 v28, 46\n"
"  ds_max_u32 v11, v28\n"
".Lh_sp44:\n"
"  ds_read_b32 v29, v10\n"
"  s_waitcnt lgkmcnt(0)\n"
"  v_cmp_gt_u32 vcc, 40, v29\n"
"  s_cbranch_vccz .Lh_go44\n"
"  s_sleep 1\n"
"  s_sub_u32 s81, s81, 1\n"
"  s_cbranch_scc0 .Lh_sp44\n"
"  s_branch .Lh_end\n"
".Lh_go44:\n"
"  s_waitcnt vmcnt(7)\n"
"  s_add_u32 m0, s72, 94208\n"
"  s_mov_b32 s85, 222080\n"
"  buffer_load_dwordx4 v5, s[48:51], s85 offen lds\n"
"  s_waitcnt vmcnt(7)\n"
"  v_mov_b32 v28, 45\n"
"  ds_max_u32 v11, v28\n"
"  s_add_u32 m0, s72, 98304\n"
"  s_mov_b32 s85, 258944\n"
"  buffer_load_dwordx4 v5, s[48:51], s85 offen lds\n"
"  s_waitcnt vmcnt(7)\n"
"  s_add_u32 m0, s72, 102400\n"
"  s_mov_b32 s85, 896\n"
"  buffer_load_dwordx4 v6, s[48:51], s85 offen lds\n"
"  s_waitcnt vmcnt(7)\n"
"  s_add_u32 m0, s73, 106496\n"
"  s_mov_b32 s85, 229376\n"
"  buffer_load_dwordx4 v7, s[52:55], s85 offen sc1 lds\n"
"  ds_read_b32 v29, v10\n"
"  s_waitcnt vmcnt(7)\n"
"  buffer_load_dwordx4 v7, s[52:55], s85 offen offset:1024 sc1 lds\n"
"  s_waitcnt vmcnt(7)\n"
"  buffer_load_dwordx4 v7, s[52:55], s85 offen offset:2048 sc1 lds\n"
"  s_waitcnt lgkmcnt(0)\n"
"  v_cmp_gt_u32 vcc, 41, v29\n"
"  s_cbranch_vccz .Lh_go45\n"
"  s_waitcnt vmcnt(6)\n"
"  v_mov_b32 v28, 46\n"
"  ds_max_u32 v11, v28\n"
"  ds_read_b32 v29, v10\n"
"  s_waitcnt lgkmcnt(0)\n"
"  v_cmp_gt_u32 vcc, 41, v29\n"
"  s_cbranch_vccz .Lh_go45\n"
"  s_waitcnt vmcnt(0)\n"
"  v_mov_b32 v28, 47\n"
"  ds_max_u32 v11, v28\n"
".Lh_sp45:\n"
"  ds_read_b32 v29, v10\n"
"  s_waitcnt lgkmcnt(0)\n"
"  v_cmp_gt_u32 vcc, 41, v29\n"
"  s_cbranch_vccz .Lh_go45\n"
"  s_sleep 1\n"
"  s_sub_u32 s81, s81, 1\n"
"  s_cbranch_scc0 .Lh_sp45\n"
"  s_branch .Lh_end\n"
".Lh_go45:\n"
"  v_cmp_gt_u32 vcc, 1, v30\n"
"  s_cbranch_vccz .Lh_go46\n"
"  s_waitcnt vmcnt(0)\n"
"  v_mov_b32 v28, 47\n"
"  ds_max_u32 v11, v28\n"
"  s_mov_b32 s80, 49152\n"
".Lh_sp46:\n"
"  buffer_load_dword v30, v19, s[52:55], s80 offen sc1\n"
"  s_waitcnt vmcnt(0)\n"
"  v_cmp_gt_u32 vcc, 1, v30\n"
"  s_cbranch_vccz .Lh_go46\n"
"  s_sleep 2\n"
"  s_sub_u32 s81, s81, 1\n"
"  s_cbranch_scc0 .Lh_sp46\n"
"  s_branch .Lh_end\n"
".Lh_go46:\n"
"  s_waitcnt vmcnt(7)\n"
"  s_mov_b32 s80, 524288\n"
"  buffer_load_dwordx4 v[136:139], v15, s[52:55], s80 offen sc1\n"
"  s_waitcnt vmcnt(7)\n"
"  v_mov_b32 v28, 46\n"
"  ds_max_u32 v11, v28\n"
"  s_mov_b32 s80, 528384\n"
"  buffer_load_dwordx4 v[140:143], v15, s[52:55], s80 offen sc1\n"
"  s_waitcnt vmcnt(7)\n"
"  s_mov_b32 s80, 532480\n"
"  buffer_load_dwordx4 v[144:147], v15, s[52:55], s80 offen sc1\n"
"  s_waitcnt vmcnt(7)\n"
"  s_mov_b32 s80, 536576\n"
"  buffer_load_dwordx4 v[148:151], v15, s[52:55], s80 offen sc1\n"
"  s_waitcnt vmcnt(7)\n"
"  buffer_load_dwordx2 v[48:49], v16, s[64:67], 0 offen offset:512\n"
"  s_waitcnt vmcnt(7)\n"
"  buffer_load_dwordx2 v[50:51], v16, s[64:67], 0 offen offset:640\n"
"  s_waitcnt vmcnt(7)\n"
"  s_mov_b32 s80, 65536\n"
"  buffer_load_dword v30, v19, s[52:55], s80 offen sc1\n"
"  v_lshlrev_b32 v60, 16, v42\n"
"  v_and_b32 v61, 0xffff0000, v42\n"
"  v_lshlrev_b32 v62, 16, v43\n"
"  v_and_b32 v63, 0xffff0000, v43\n"
"  s_nop 1\n"
"  v_mfma_f32_16x16x32_bf16 v[60:63], v[20:23], v[120:123], v[60:63]\n"
"  v_mfma_f32_16x16x32_bf16 v[60:63], v[20:23], v[124:127], v[60:63]\n"
"  v_mfma_f32_16x16x32_bf16 v[60:63], v[20:23], v[128:131], v[60:63]\n"
"  v_mfma_f32_16x16x32_bf16 v[60:63], v[20:23], v[132:135], v[60:63]\n"
"  s_nop 7\n"
"  v_cvt_pk_bf16_f32 v42, v60, v61\n"
"  v_cvt_pk_bf16_f32 v43, v62, v63\n"
"  buffer_store_dwordx2 v[42:43], v16, s[68:71], 0 offen offset:128\n"
"  s_waitcnt vmcnt(7)\n"
"  v_mov_b32 v28, 47\n"
"  ds_max_u32 v11, v28\n"
"  s_add_u32 m0, s74, 118784\n"
"  s_mov_b32 s85, 229376\n"
"  buffer_load_dwordx4 v8, s[52:55], s85 offen sc1 lds\n"
"  s_waitcnt vmcnt(7)\n"
"  buffer_load_dwordx4 v8, s[52:55], s85 offen offset:1024 sc1 lds\n"
"  s_waitcnt vmcnt(7)\n"
"  s_add_u32 m0, s74, 120832\n"
"  s_mov_b32 s85, 231424\n"
"  buffer_load_dwordx4 v8, s[52:55], s85 offen sc1 lds\n"
"  ds_read_b32 v29, v10\n"
"  s_waitcnt vmcnt(7)\n"
"  buffer_load_dwordx4 v8, s[52:55], s85 offen offset:1024 sc1 lds\n"
"  s_waitcnt vmcnt(7)\n"
"  buffer_load_dwordx4 v8, s[52:55], s85 offen offset:2048 sc1 lds\n"
"  s_waitcnt lgkmcnt(0)\n"
"  v_cmp_gt_u32 vcc, 42, v29\n"
"  s_cbranch_vccz .Lh_go47\n"
"  s_waitcnt vmcnt(0)\n"
"  v_mov_b32 v28, 48\n"
"  ds_max_u32 v11, v28\n"
".Lh_sp47:\n"
"  ds_read_b32 v29, v10\n"
"  s_waitcnt lgkmcnt(0)\n"
"  v_cmp_gt_u32 vcc, 42, v29\n"
"  s_cbranch_vccz .Lh_go47\n"
"  s_sleep 1\n"
"  s_sub_u32 s81, s81, 1\n"
"  s_cbranch_scc0 .Lh_sp47\n"
"  s_branch .Lh_end\n"
".Lh_go47:\n"
"  s_waitcnt vmcnt(7)\n"
"  s_add_u32 m0, s72, 0\n"
"  s_mov_b32 s85, 1024\n"
"  buffer_load_dwordx4 v5, s[48:51], s85 offen lds\n"
"  s_waitcnt vmcnt(7)\n"
"  s_add_u32 m0, s72, 4096\n"
"  s_mov_b32 s85, 37888\n"
"  buffer_load_dwordx4 v5, s[48:51], s85 offen lds\n"
"  s_waitcnt vmcnt(7)\n"
"  s_add_u32 m0, s72, 8192\n"
"  s_mov_b32 s85, 74752\n"
"  buffer_load_dwordx4 v5, s[48:51], s85 offen lds\n"
"  s_waitcnt vmcnt(7)\n"
"  s_add_u32 m0, s72, 12288\n"
"  s_mov_b32 s85, 111616\n"
"  buffer_load_dwordx4 v5, s[48:51], s85 offen lds\n"
"  ds_read_b32 v29, v10\n"
"  s_waitcnt vmcnt(7)\n"
"  s_add_u32 m0, s72, 16384\n"
"  s_mov_b32 s85, 148480\n"
"  buffer_load_dwordx4 v5, s[48:51], s85 offen lds\n"
"  s_waitcnt vmcnt(7)\n"
"  s_add_u32 m0, s72, 20480\n"
"  s_mov_b32 s85, 185344\n"
"  buffer_load_dwordx4 v5, s[48:51], s85 offen lds\n"
"  s_waitcnt lgkmcnt(0)\n"
"  v_cmp_gt_u32 vcc, 43, v29\n"
"  s_cbranch_vccz .Lh_go48\n"
"  s_waitcnt vmcnt(6)\n"
"  v_mov_b32 v28, 48\n"
"  ds_max_u32 v11, v28\n"
"  ds_read_b32 v29, v10\n"
"  s_waitcnt lgkmcnt(0)\n"
"  v_cmp_gt_u32 vcc, 43, v29\n"
"  s_cbranch_vccz .Lh_go48\n"
"  s_waitcnt vmcnt(0)\n"
"  v_mov_b32 v28, 49\n"
"  ds_max_u32 v11, v28\n"
".Lh_sp48:\n"
"  ds_read_b32 v29, v10\n"
"  s_waitcnt lgkmcnt(0)\n"
"  v_cmp_gt_u32 vcc, 43, v29\n"
"  s_cbranch_vccz .Lh_go48\n"
"  s_sleep 1\n"
"  s_sub_u32 s81, s81, 1\n"
"  s_cbranch_scc0 .Lh_sp48\n"
"  s_branch .Lh_end\n"
".Lh_go48:\n"
"  s_waitcnt vmcnt(7)\n"
"  s_add_u32 m0, s72, 24576\n"
"  s_mov_b32 s85, 222208\n"
"  buffer_load_dwordx4 v5, s[48:51], s85 offen lds\n"
"  s_waitcnt vmcnt(7)\n"
"  v_mov_b32 v28, 48\n"
"  ds_max_u32 v11, v28\n"
"  s_add_u32 m0, s72, 28672\n"
"  s_mov_b32 s85, 259072\n"
"  buffer_load_dwordx4 v5, s[48:51], s85 offen lds\n"
"  s_waitcnt vmcnt(7)\n"
"  s_add_u32 m0, s72, 32768\n"
"  s_mov_b32 s85, 1024\n"
"  buffer_load_dwordx4 v6, s[48:51], s85 offen lds\n"
"  s_waitcnt vmcnt(7)\n"
"  s_add_u32 m0, s73, 36864\n"
"  s_mov_b32 s85, 262144\n"
"  buffer_load_dwordx4 v7, s[52:55], s85 offen sc1 lds\n"
"  ds_read_b32 v29, v10\n"
"  s_waitcnt vmcnt(7)\n"
"  buffer_load_dwordx4 v7, s[52:55], s85 offen offset:1024 sc1 lds\n"
"  s_waitcnt vmcnt(7)\n"
"  buffer_load_dwordx4 v7, s[52:55], s85 offen offset:2048 sc1 lds\n"
"  s_waitcnt lgkmcnt(0)\n"
"  v_cmp_gt_u32 vcc, 44, v29\n"
"  s_cbranch_vccz .Lh_go49\n"
"  s_waitcnt vmcnt(6)\n"
"  v_mov_b32 v28, 49\n"
"  ds_max_u32 v11, v28\n"
"  ds_read_b32 v29, v10\n"
"  s_waitcnt lgkmcnt(0)\n"
"  v_cmp_gt_u32 vcc, 44, v29\n"
"  s_cbranch_vccz .Lh_go49\n"
"  s_waitcnt vmcnt(0)\n"
"  v_mov_b32 v28, 50\n"
"  ds_max_u32 v11, v28\n"
".Lh_sp49:\n"
"  ds_read_b32 v29, v10\n"
"  s_waitcnt lgkmcnt(0)\n"
"  v_cmp_gt_u32 vcc, 44, v29\n"
"  s_cbranch_vccz .Lh_go49\n"
"  s_sleep 1\n"
"  s_sub_u32 s81, s81, 1\n"
"  s_cbranch_scc0 .Lh_sp49\n"
"  s_branch .Lh_end\n"
".Lh_go49:\n"
"  v_cmp_gt_u32 vcc, 1, v30\n"
"  s_cbranch_vccz .Lh_go50\n"
"  s_waitcnt vmcnt(0)\n"
"  v_mov_b32 v28, 50\n"
"  ds_max_u32 v11, v28\n"
"  s_mov_b32 s80, 65536\n"
".Lh_sp50:\n"
"  buffer_load_dword v30, v19, s[52:55], s80 offen sc1\n"
"  s_waitcnt vmcnt(0)\n"
"  v_cmp_gt_u32 vcc, 1, v30\n"
"  s_cbranch_vccz .Lh_go50\n"
"  s_sleep 2\n"
"  s_sub_u32 s81, s81, 1\n"
"  s_cbranch_scc0 .Lh_sp50\n"
"  s_branch .Lh_end\n"
".Lh_go50:\n"
"  s_waitcnt vmcnt(7)\n"
"  s_mov_b32 s80, 786432\n"
"  buffer_load_dwordx4 v[152:155], v15, s[52:55], s80 offen sc1\n"
"  s_waitcnt vmcnt(7)\n"
"  v_mov_b32 v28, 49\n"
"  ds_max_u32 v11, v28\n"
"  s_mov_b32 s80, 790528\n"
"  buffer_load_dwordx4 v[156:159], v15, s[52:55], s80 offen sc1\n"
"  s_waitcnt vmcnt(7)\n"
"  s_mov_b32 s80, 794624\n"
"  buffer_load_dwordx4 v[160:163], v15, s[52:55], s80 offen sc1\n"
"  s_waitcnt vmcnt(7)\n"
"  s_mov_b32 s80, 798720\n"
"  buffer_load_dwordx4 v[164:167], v15, s[52:55], s80 offen sc1\n"
"  s_waitcnt vmcnt(7)\n"
"  buffer_load_dwordx2 v[52:53], v16, s[64:67], 0 offen offset:768\n"
"  s_waitcnt vmcnt(7)\n"
"  s_mov_b32 s80, 81920\n"
"  buffer_load_dword v30, v19, s[52:55], s80 offen sc1\n"
"  v_lshlrev_b32 v60, 16, v44\n"
"  v_and_b32 v61, 0xffff0000, v44\n"
"  v_lshlrev_b32 v62, 16, v45\n"
"  v_and_b32 v63, 0xffff0000, v45\n"
"  s_nop 1\n"
"  v_mfma_f32_16x16x32_bf16 v[60:63], v[20:23], v[136:139], v[60:63]\n"
"  v_mfma_f32_16x16x32_bf16 v[60:63], v[20:23], v[140:143], v[60:63]\n"
"  v_mfma_f32_16x16x32_bf16 v[60:63], v[20:23], v[144:147], v[60:63]\n"
"  v_mfma_f32_16x16x32_bf16 v[60:63], v[20:23], v[148:151], v[60:63]\n"
"  s_nop 7\n"
"  v_cvt_pk_bf16_f32 v44, v60, v61\n"
"  v_cvt_pk_bf16_f32 v45, v62, v63\n"
"  buffer_store_dwordx2 v[44:45], v16, s[68:71], 0 offen offset:256\n"
"  s_waitcnt vmcnt(7)\n"
"  v_mov_b32 v28, 50\n"
"  ds_max_u32 v11, v28\n"
"  s_add_u32 m0, s74, 49152\n"
"  s_mov_b32 s85, 262144\n"
"  buffer_load_dwordx4 v8, s[52:55], s85 offen sc1 lds\n"
"  s_waitcnt vmcnt(7)\n"
"  buffer_load_dwordx4 v8, s[52:55], s85 offen offset:1024 sc1 lds\n"
"  s_waitcnt vmcnt(7)\n"
"  s_add_u32 m0, s74, 51200\n"
"  s_mov_b32 s85, 264192\n"
"  buffer_load_dwordx4 v8, s[52:55], s85 offen sc1 lds\n"
"  ds_read_b32 v29, v10\n"
"  s_waitcnt vmcnt(7)\n"
"  buffer_load_dwordx4 v8, s[52:55], s85 offen offset:1024 sc1 lds\n"
"  s_waitcnt vmcnt(7)\n"
"  buffer_load_dwordx4 v8, s[52:55], s85 offen offset:2048 sc1 lds\n"
"  s_waitcnt lgkmcnt(0)\n"
"  v_cmp_gt_u32 vcc, 47, v29\n"
"  s_cbranch_vccz .Lh_go51\n"
"  s_waitcnt vmcnt(0)\n"
"  v_mov_b32 v28, 1000\n"
"  ds_max_u32 v11, v28\n"
".Lh_sp51:\n"
"  ds_read_b32 v29, v10\n"
"  s_waitcnt lgkmcnt(0)\n"
"  v_cmp_gt_u32 vcc, 47, v29\n"
"  s_cbranch_vccz .Lh_go51\n"
"  s_sleep 1\n"
"  s_sub_u32 s81, s81, 1\n"
"  s_cbranch_scc0 .Lh_sp51\n"
"  s_branch .Lh_end\n"
".Lh_go51:\n"
"  s_waitcnt vmcnt(6)\n"
"  v_cmp_gt_u32 vcc, 1, v30\n"
"  s_cbranch_vccz .Lh_go52\n"
"  s_waitcnt vmcnt(0)\n"
"  v_mov_b32 v28, 1000\n"
"  ds_max_u32 v11, v28\n"
"  s_mov_b32 s80, 81920\n"
".Lh_sp52:\n"
"  buffer_load_dword v30, v19, s[52:55], s80 offen sc1\n"
"  s_waitcnt vmcnt(0)\n"
"  v_cmp_gt_u32 vcc, 1, v30\n"
"  s_cbranch_vccz .Lh_go52\n"
"  s_sleep 2\n"
"  s_sub_u32 s81, s81, 1\n"
"  s_cbranch_scc0 .Lh_sp52\n"
"  s_branch .Lh_end\n"
".Lh_go52:\n"
"  s_mov_b32 s80, 1048576\n"
"  buffer_load_dwordx4 v[168:171], v15, s[52:55], s80 offen sc1\n"
"  s_mov_b32 s80, 1052672\n"
"  buffer_load_dwordx4 v[172:175], v15, s[52:55], s80 offen sc1\n"
"  s_waitcnt vmcnt(7)\n"
"  s_mov_b32 s80, 1056768\n"
"  buffer_load_dwordx4 v[176:179], v15, s[52:55], s80 offen sc1\n"
"  s_waitcnt vmcnt(7)\n"
"  s_mov_b32 s80, 1060864\n"
"  buffer_load_dwordx4 v[180:183], v15, s[52:55], s80 offen sc1\n"
"  s_waitcnt vmcnt(7)\n"
"  buffer_load_dwordx2 v[54:55], v16, s[64:67], 0 offen offset:896\n"
"  s_waitcnt vmcnt(7)\n"
"  buffer_load_dwordx2 v[56:57], v16, s[64:67], 0 offen offset:1024\n"
"  s_waitcnt vmcnt(7)\n"
"  s_mov_b32 s80, 98304\n"
"  buffer_load_dword v30, v19, s[52:55], s80 offen sc1\n"
"  v_lshlrev_b32 v60, 16, v46\n"
"  v_and_b32 v61, 0xffff0000, v46\n"
"  v_lshlrev_b32 v62, 16, v47\n"
"  v_and_b32 v63, 0xffff0000, v47\n"
"  s_nop 1\n"
"  v_mfma_f32_16x16x32_bf16 v[60:63], v[20:23], v[152:155], v[60:63]\n"
"  v_mfma_f32_16x16x32_bf16 v[60:63], v[20:23], v[156:159], v[60:63]\n"
"  v_mfma_f32_16x16x32_bf16 v[60:63], v[20:23], v[160:163], v[60:63]\n"
"  v_mfma_f32_16x16x32_bf16 v[60:63], v[20:23], v[164:167], v[60:63]\n"
"  s_nop 7\n"
"  v_cvt_pk_bf16_f32 v46, v60, v61\n"
"  v_cvt_pk_bf16_f32 v47, v62, v63\n"
"  buffer_store_dwordx2 v[46:47], v16, s[68:71], 0 offen offset:384\n"
"  ds_read_b32 v29, v10\n"
"  s_waitcnt lgkmcnt(0)\n"
"  v_cmp_gt_u32 vcc, 49, v29\n"
"  s_cbranch_vccz .Lh_go53\n"
"  s_waitcnt vmcnt(8)\n"
"  v_mov_b32 v28, 1000\n"
"  ds_max_u32 v11, v28\n"
".Lh_sp53:\n"
"  ds_read_b32 v29, v10\n"
"  s_waitcnt lgkmcnt(0)\n"
"  v_cmp_gt_u32 vcc, 49, v29\n"
"  s_cbranch_vccz .Lh_go53\n"
"  s_sleep 1\n"
"  s_sub_u32 s81, s81, 1\n"
"  s_cbranch_scc0 .Lh_sp53\n"
"  s_branch .Lh_end\n"
".Lh_go53:\n"
"  s_waitcnt vmcnt(1)\n"
"  v_mov_b32 v28, 1000\n"
"  ds_max_u32 v11, v28\n"
"  v_cmp_gt_u32 vcc, 1, v30\n"
"  s_cbranch_vccz .Lh_go54\n"
"  s_mov_b32 s80, 98304\n"
".Lh_sp54:\n"
"  buffer_load_dword v30, v19, s[52:55], s80 offen sc1\n"
"  s_waitcnt vmcnt(0)\n"
"  v_cmp_gt_u32 vcc, 1, v30\n"
"  s_cbranch_vccz .Lh_go54\n"
"  s_sleep 2\n"
"  s_sub_u32 s81, s81, 1\n"
"  s_cbranch_scc0 .Lh_sp54\n"
"  s_branch .Lh_end\n"
".Lh_go54:\n"
"  s_mov_b32 s80, 1310720\n"
"  buffer_load_dwordx4 v[184:187], v15, s[52:55], s80 offen sc1\n"
"  s_mov_b32 s80, 1314816\n"
"  buffer_load_dwordx4 v[188:191], v15, s[52:55], s80 offen sc1\n"
"  s_mov_b32 s80, 1318912\n"
"  buffer_load_dwordx4 v[192:195], v15, s[52:55], s80 offen sc1\n"
"  s_mov_b32 s80, 1323008\n"
"  buffer_load_dwordx4 v[196:199], v15, s[52:55], s80 offen sc1\n"
"  s_mov_b32 s80, 114688\n"
"  buffer_load_dword v30, v19, s[52:55], s80 offen sc1\n"
"  v_lshlrev_b32 v60, 16, v48\n"
"  v_and_b32 v61, 0xffff0000, v48\n"
"  v_lshlrev_b32 v62, 16, v49\n"
"  v_and_b32 v63, 0xffff0000, v49\n"
"  s_nop 1\n"
"  v_mfma_f32_16x16x32_bf16 v[60:63], v[20:23], v[168:171], v[60:63]\n"
"  v_mfma_f32_16x16x32_bf16 v[60:63], v[20:23], v[172:175], v[60:63]\n"
"  v_mfma_f32_16x16x32_bf16 v[60:63], v[20:23], v[176:179], v[60:63]\n"
"  v_mfma_f32_16x16x32_bf16 v[60:63], v[20:23], v[180:183], v[60:63]\n"
"  s_nop 7\n"
"  v_cvt_pk_bf16_f32 v48, v60, v61\n"
"  v_cvt_pk_bf16_f32 v49, v62, v63\n"
"  buffer_store_dwordx2 v[48:49], v16, s[68:71], 0 offen offset:512\n"
"  s_waitcnt vmcnt(1)\n"
"  v_cmp_gt_u32 vcc, 1, v30\n"
"  s_cbranch_vccz .Lh_go55\n"
"  s_mov_b32 s80, 114688\n"
".Lh_sp55:\n"
"  buffer_load_dword v30, v19, s[52:55], s80 offen sc1\n"
"  s_waitcnt vmcnt(0)\n"
"  v_cmp_gt_u32 vcc, 1, v30\n"
"  s_cbranch_vccz .Lh_go55\n"
"  s_sleep 2\n"
"  s_sub_u32 s81, s81, 1\n"
"  s_cbranch_scc0 .Lh_sp55\n"
"  s_branch .Lh_end\n"
".Lh_go55:\n"
"  s_mov_b32 s80, 1572864\n"
"  buffer_load_dwordx4 v[200:203], v15, s[52:55], s80 offen sc1\n"
"  s_mov_b32 s80, 1576960\n"
"  buffer_load_dwordx4 v[204:207], v15, s[52:55], s80 offen sc1\n"
"  s_mov_b32 s80, 1581056\n"
"  buffer_load_dwordx4 v[208:211], v15, s[52:55], s80 offen sc1\n"
"  s_mov_b32 s80, 1585152\n"
"  buffer_load_dwordx4 v[212:215], v15, s[52:55], s80 offen sc1\n"
"  s_mov_b32 s80, 131072\n"
"  buffer_load_dword v30, v19, s[52:55], s80 offen sc1\n"
"  v_lshlrev_b32 v60, 16, v50\n"
"  v_and_b32 v61, 0xffff0000, v50\n"
"  v_lshlrev_b32 v62, 16, v51\n"
"  v_and_b32 v63, 0xffff0000, v51\n"
"  s_nop 1\n"
"  v_mfma_f32_16x16x32_bf16 v[60:63], v[20:23], v[184:187], v[60:63]\n"
"  v_mfma_f32_16x16x32_bf16 v[60:63], v[20:23], v[188:191], v[60:63]\n"
"  v_mfma_f32_16x16x32_bf16 v[60:63], v[20:23], v[192:195], v[60:63]\n"
"  v_mfma_f32_16x16x32_bf16 v[60:63], v[20:23], v[196:199], v[60:63]\n"
"  s_nop 7\n"
"  v_cvt_pk_bf16_f32 v50, v60, v61\n"
"  v_cvt_pk_bf16_f32 v51, v62, v63\n"
"  buffer_store_dwordx2 v[50:51], v16, s[68:71], 0 offen offset:640\n"
"  s_waitcnt vmcnt(1)\n"
"  v_cmp_gt_u32 vcc, 1, v30\n"
"  s_cbranch_vccz .Lh_go56\n"
"  s_mov_b32 s80, 131072\n"
".Lh_sp56:\n"
"  buffer_load_dword v30, v19, s[52:55], s80 offen sc1\n"
"  s_waitcnt vmcnt(0)\n"
"  v_cmp_gt_u32 vcc, 1, v30\n"
"  s_cbranch_vccz .Lh_go56\n"
"  s_sleep 2\n"
"  s_sub_u32 s81, s81, 1\n"
"  s_cbranch_scc0 .Lh_sp56\n"
"  s_branch .Lh_end\n"
".Lh_go56:\n"
"  s_mov_b32 s80, 1835008\n"
"  buffer_load_dwordx4 v[216:219], v15, s[52:55], s80 offen sc1\n"
"  s_mov_b32 s80, 1839104\n"
"  buffer_load_dwordx4 v[220:223], v15, s[52:55], s80 offen sc1\n"
"  s_mov_b32 s80, 1843200\n"
"  buffer_load_dwordx4 v[224:227], v15, s[52:55], s80 offen sc1\n"
"  s_mov_b32 s80, 1847296\n"
"  buffer_load_dwordx4 v[228:231], v15, s[52:55], s80 offen sc1\n"
"  v_lshlrev_b32 v60, 16, v52\n"
"  v_and_b32 v61, 0xffff0000, v52\n"
"  v_lshlrev_b32 v62, 16, v53\n"
"  v_and_b32 v63, 0xffff0000, v53\n"
"  s_nop 1\n"
"  v_mfma_f32_16x16x32_bf16 v[60:63], v[20:23], v[200:203], v[60:63]\n"
"  v_mfma_f32_16x16x32_bf16 v[60:63], v[20:23], v[204:207], v[60:63]\n"
"  v_mfma_f32_16x16x32_bf16 v[60:63], v[20:23], v[208:211], v[60:63]\n"
"  v_mfma_f32_16x16x32_bf16 v[60:63], v[20:23], v[212:215], v[60:63]\n"
"  s_nop 7\n"
"  v_cvt_pk_bf16_f32 v52, v60, v61\n"
"  v_cvt_pk_bf16_f32 v53, v62, v63\n"
"  buffer_store_dwordx2 v[52:53], v16, s[68:71], 0 offen offset:768\n"
".Lh_sp57:\n"
"  ds_read_b32 v29, v10\n"
"  s_waitcnt lgkmcnt(0)\n"
"  v_cmp_gt_u32 vcc, 50, v29\n"
"  s_cbranch_vccz .Lh_go57\n"
"  s_sleep 1\n"
"  s_sub_u32 s81, s81, 1\n"
"  s_cbranch_scc0 .Lh_sp57\n"
"  s_branch .Lh_end\n"
".Lh_go57:\n"
"  s_mov_b32 s84, 0x7f817f81\n"
".Lh_e8:\n"
"  s_mov_b32 s80, 2097152\n"
"  buffer_load_dwordx4 v[232:235], v15, s[52:55], s80 offen sc1\n"
"  s_mov_b32 s80, 2101248\n"
"  buffer_load_dwordx4 v[236:239], v15, s[52:55], s80 offen sc1\n"
"  s_waitcnt vmcnt(7)\n"
"  s_mov_b32 s80, 2105344\n"
"  buffer_load_dwordx4 v[240:243], v15, s[52:55], s80 offen sc1\n"
"  s_waitcnt vmcnt(7)\n"
"  s_mov_b32 s80, 2109440\n"
"  buffer_load_dwordx4 v[244:247], v15, s[52:55], s80 offen sc1\n"
"  s_waitcnt vmcnt(0)\n"
"  v_cmp_eq_u32 s[88:89], s84, v232\n"
"  v_cmp_eq_u32 s[86:87], s84, v233\n"
"  s_or_b64 s[88:89], s[88:89], s[86:87]\n"
"  v_cmp_eq_u32 vcc, s84, v234\n"
"  s_or_b64 s[88:89], s[88:89], vcc\n"
"  v_cmp_eq_u32 s[86:87], s84, v235\n"
"  s_or_b64 s[88:89], s[88:89], s[86:87]\n"
"  v_cmp_eq_u32 vcc, s84, v236\n"
"  s_or_b64 s[88:89], s[88:89], vcc\n"
"  v_cmp_eq_u32 s[86:87], s84, v237\n"
"  s_or_b64 s[88:89], s[88:89], s[86:87]\n"
"  v_cmp_eq_u32 vcc, s84, v238\n"
"  s_or_b64 s[88:89], s[88:89], vcc\n"
"  v_cmp_eq_u32 s[86:87], s84, v239\n"
"  s_or_b64 s[88:89], s[88:89], s[86:87]\n"
"  v_cmp_eq_u32 vcc, s84, v240\n"
"  s_or_b64 s[88:89], s[88:89], vcc\n"
"  v_cmp_eq_u32 s[86:87], s84, v241\n"
"  s_or_b64 s[88:89], s[88:89], s[86:87]\n"
"  v_cmp_eq_u32 vcc, s84, v242\n"
"  s_or_b64 s[88:89], s[88:89], vcc\n"
"  v_cmp_eq_u32 s[86:87], s84, v243\n"
"  s_or_b64 s[88:89], s[88:89], s[86:87]\n"
"  v_cmp_eq_u32 vcc, s84, v244\n"
"  s_or_b64 s[88:89], s[88:89], vcc\n"
"  v_cmp_eq_u32 s[86:87], s84, v245\n"
"  s_or_b64 s[88:89], s[88:89], s[86:87]\n"
"  v_cmp_eq_u32 vcc, s84, v246\n"
"  s_or_b64 s[88:89], s[88:89], vcc\n"
"  v_cmp_eq_u32 s[86:87], s84, v247\n"
"  s_or_b64 s[88:89], s[88:89], s[86:87]\n"
"  s_cbranch_scc0 .Lh_e8d\n"
"  s_sub_u32 s81, s81, 1\n"
"  s_cbranch_scc0 .Lh_e8\n"
"  s_branch .Lh_end\n"
".Lh_e8d:\n"
"  v_lshlrev_b32 v60, 16, v54\n"
"  v_and_b32 v61, 0xffff0000, v54\n"
"  v_lshlrev_b32 v62, 16, v55\n"
"  v_and_b32 v63, 0xffff0000, v55\n"
"  s_nop 1\n"
"  v_mfma_f32_16x16x32_bf16 v[60:63], v[20:23], v[216:219], v[60:63]\n"
"  v_mfma_f32_16x16x32_bf16 v[60:63], v[20:23], v[220:223], v[60:63]\n"
"  v_mfma_f32_16x16x32_bf16 v[60:63], v[20:23], v[224:227], v[60:63]\n"
"  v_mfma_f32_16x16x32_bf16 v[60:63], v[20:23], v[228:231], v[60:63]\n"
"  s_nop 7\n"
"  v_cvt_pk_bf16_f32 v54, v60, v61\n"
"  v_cvt_pk_bf16_f32 v55, v62, v63\n"
"  buffer_store_dwordx2 v[54:55], v16, s[68:71], 0 offen offset:896\n"
"  v_lshlrev_b32 v60, 16, v56\n"
"  v_and_b32 v61, 0xffff0000, v56\n"
"  v_lshlrev_b32 v62, 16, v57\n"
"  v_and_b32 v63, 0xffff0000, v57\n"
"  s_nop 1\n"
"  v_mfma_f32_16x16x32_bf16 v[60:63], v[20:23], v[232:235], v[60:63]\n"
"  v_mfma_f32_16x16x32_bf16 v[60:63], v[20:23], v[236:239], v[60:63]\n"
"  v_mfma_f32_16x16x32_bf16 v[60:63], v[20:23], v[240:243], v[60:63]\n"
"  v_mfma_f32_16x16x32_bf16 v[60:63], v[20:23], v[244:247], v[60:63]\n"
"  s_nop 7\n"
"  v_cvt_pk_bf16_f32 v56, v60, v61\n"
"  v_cvt_pk_bf16_f32 v57, v62, v63\n"
"  buffer_store_dwordx2 v[56:57], v16, s[68:71], 0 offen offset:1024\n"
".Lh_end:\n"
"  s_waitcnt vmcnt(0)\n"

        ::"{v[0:15]}"(va), "{v[16:23]}"(vb), "{v[24:27]}"(vc), "{s[36:51]}"(sa), "{s[52:67]}"(sb), "{s[68:75]}"(sc)
        : "memory", "vcc", "scc", "v28", "v29", "v30", "v31", "v32", "v33", "v34", "v35", "v36", "v37", "v38", "v39", "v40", "v41", "v42", "v43", "v44", "v45", "v46", "v47", "v48", "v49", "v50", "v51", "v52", "v53", "v54", "v55", "v56", "v57", "v58", "v59", "v60", "v61", "v62", "v63", "v64", "v65", "v66", "v67", "v68", "v69", "v70", "v71", "v72", "v73", "v74", "v75", "v76", "v77", "v78", "v79", "v80", "v81", "v82", "v83", "v84", "v85", "v86", "v87", "v88", "v89", "v90", "v91", "v92", "v93", "v94", "v95", "v96", "v97", "v98", "v99", "v100", "v101", "v102", "v103", "v104", "v105", "v106", "v107", "v108", "v109", "v110", "v111", "v112", "v113", "v114", "v115", "v116", "v117", "v118", "v119", "v120", "v121", "v122", "v123", "v124", "v125", "v126", "v127", "v128", "v129", "v130", "v131", "v132", "v133", "v134", "v135", "v136", "v137", "v138", "v139", "v140", "v141", "v142", "v143", "v144", "v145", "v146", "v147", "v148", "v149", "v150", "v151", "v152", "v153", "v154", "v155", "v156", "v157", "v158", "v159", "v160", "v161", "v162", "v163", "v164", "v165", "v166", "v167", "v168", "v169", "v170", "v171", "v172", "v173", "v174", "v175", "v176", "v177", "v178", "v179", "v180", "v181", "v182", "v183", "v184", "v185", "v186", "v187", "v188", "v189", "v190", "v191", "v192", "v193", "v194", "v195", "v196", "v197", "v198", "v199", "v200", "v201", "v202", "v203", "v204", "v205", "v206", "v207", "v208", "v209", "v210", "v211", "v212", "v213", "v214", "v215", "v216", "v217", "v218", "v219", "v220", "v221", "v222", "v223", "v224", "v225", "v226", "v227", "v228", "v229", "v230", "v231", "v232", "v233", "v234", "v235", "v236", "v237", "v238", "v239", "v240", "v241", "v242", "v243", "v244", "v245", "v246", "v247", "s80", "s81", "s82", "s83", "s84", "s85", "s86", "s87", "s88", "s89", "s90", "s91", "s92", "s93");
  }
}

DI float sum8(float u) {
  auto a = __builtin_amdgcn_permlane32_swap(__float_as_uint(u), __float_as_uint(u), false, false);
  u = __uint_as_float(a[0]) + __uint_as_float(a[1]);
  auto d = __builtin_amdgcn_permlane16_swap(__float_as_uint(u), __float_as_uint(u), false, false);
  u = __uint_as_float(d[0]) + __uint_as_float(d[1]);
  return u + __uint_as_float(__builtin_amdgcn_update_dpp(0u, __float_as_uint(u), 0x128, 0xf, 0xf, false));
}

__global__ __launch_bounds__(576) void pre_kernel(const void* __restrict__ x, const void* __restrict__ wr, const void* __restrict__ wi,
                                                  const void* __restrict__ w1, void* __restrict__ ws) {
  __shared__ __attribute__((aligned(16))) f32x2 st[9 * 8];
  const int wid = __builtin_amdgcn_readfirstlane(threadIdx.x >> 6);
  const unsigned lane = threadIdx.x & 63;
  const unsigned tg = blockIdx.x >> 2, T = (blockIdx.x >> 1) & 1, tk = lane & 7, ch = lane >> 3, cc = (blockIdx.x & 1) * 8 + tk;
  const u32x4 f = __builtin_amdgcn_raw_buffer_load_b128(mk(x, S_ * D_ * 2), (tg * 32 + 8 * (cc >> 2) + (cc & 3) + 4 * T) * 1152 + ch * 16, wid * 128, 0);
  rsrc_t rn = mk(ws, WS_BYTES);
  unsigned pl, ph;
  asm volatile("s_getpc_b64 vcc\n\ts_add_u32 %0, vcc_lo, _ZN2fk11main_kernelEPKvS1_S1_S1_S1_S1_S1_PvS2_@rel32@lo+4\n\ts_addc_u32 %1, vcc_hi, _ZN2fk11main_kernelEPKvS1_S1_S1_S1_S1_S1_PvS2_@rel32@hi+12" : "=s"(pl), "=s"(ph)::"scc", "vcc");
  const unsigned wv = blockIdx.x * 9 + wid, wl = wv * 5 + lane - 18;
  const char* tp = (const char*)w1 + (wv * 18 + (lane < 18 ? lane : 0)) * 128;
  if (lane >= 18 && lane < 23 && wl < 10368) tp = wl < 5184 ? (const char*)wr + wl * 128 : (const char*)wi + (wl - 5184) * 128;
  if (lane == 23 && wv * 128 < CODE_BYTES) tp = (const char*)(((unsigned long long)ph << 32) | pl) + wv * 128;
  float s = 0.f, m2 = 0.f;
#pragma unroll
  for (int k = 0; k < 4; ++k) s = dot2(f[k], 0x3f803f80u, s);
  s = sum8(s);
  const float mw = s * (1.0f / 64);
#pragma unroll
  for (int k = 0; k < 4; ++k) {
    const float lo = blo(f[k]) - mw, hi = bhi(f[k]) - mw;
    m2 = fmaf(lo, lo, fmaf(hi, hi, m2));
  }
  asm volatile("" : "+v"(tp) : "v"(m2) : "memory");
  const unsigned tv = *(gcu32)tp;
  m2 = sum8(m2);
  if (lane < 8) st[wid * 8 + tk] = (f32x2){mw, m2};
  asm volatile("s_waitcnt lgkmcnt(0)\n\ts_barrier" ::: "memory");
  f32x2 p[9];
#pragma unroll
  for (int i = 0; i < 9; ++i) p[i] = st[i * 8 + tk];
  f32x2 t = p[0];
#pragma unroll
  for (int i = 1; i < 9; ++i) t += p[i];
  const float mean = t[0] * (1.0f / 9);
  float dm = 0.f;
#pragma unroll
  for (int i = 0; i < 9; ++i) dm = fmaf(p[i][0] - mean, p[i][0] - mean, dm);
  const float var = fmaf(dm, 64.f, t[1]) * (1.0f / D_);
  const float mn = rbf(mean);
  const float rs = __builtin_amdgcn_rcpf(rbf(__builtin_amdgcn_sqrtf(rbf(rbf(var) + 1e-5f))));
  u32x4 o;
#pragma unroll
  for (int k = 0; k < 4; ++k) {
    const unsigned d = pk(blo(f[k]) - mn, bhi(f[k]) - mn);
    o[k] = pk(blo(d) * rs, bhi(d) * rs);
  }
  const unsigned so = XN_OFF + (((tg >> 2) * 9 + wid) * 4 + (tg & 3)) * 4096 + T * 1024;
  __builtin_amdgcn_raw_buffer_store_b128(o, rn, (ch >> 2) * 2048 + ((ch & 3) * 16 + cc) * 16, so, 0);
  if (wid == 8 && blockIdx.x >= 112) {
    __builtin_amdgcn_raw_buffer_store_b128((u32x4){0, 0, 0, 0}, rn, lane * 16, FLAG_OFF + (blockIdx.x - 112) * 1024, 0);
  }
  asm volatile("" ::"v"(tv));
}
}

extern "C" void kernel_launch(void* const* d_in, const int* in_sizes, int n_in, void* d_out, int out_size, void* d_ws, size_t ws_size,
                              hipStream_t stream) {
  (void)in_sizes; (void)n_in; (void)out_size; (void)ws_size;
  fk::pre_kernel<<<256, 576, 0, stream>>>(d_in[0], d_in[3], d_in[4], d_in[6], d_ws);
  fk::main_kernel<<<256, 512, 0, stream>>>(d_in[0], d_in[1], d_in[2], d_in[3], d_in[4], d_in[6], d_in[7], d_ws, d_out);
}
```
